```python
import torch

def _softmax(x, axis=-1):
    return torch.softmax(x, dim=axis)

def _gelu(x):
    return torch.nn.functional.gelu(x, approximate='tanh')

def _silu(x):
    return torch.nn.functional.silu(x)

def _sigmoid(x):
    return torch.sigmoid(x)

def _layernorm(x, axis=-1, eps=1e-05):
    m = x.mean(dim=axis, keepdim=True)
    v = x.var(dim=axis, keepdim=True, unbiased=False)
    return (x - m) / torch.sqrt(v + eps)

def _scatter_add(tgt, idx, src, axis=0):
    return tgt.clone().index_add_(axis, idx, src)

def _segment_sum(src, seg, n):
    out = torch.zeros((n,) + src.shape[1:], dtype=src.dtype)
    return out.index_add_(0, seg, src)

def _pad(x, pw):
    flat = tuple((v for pair in reversed(pw) for v in pair))
    return torch.nn.functional.pad(x, flat)
kernel_name = 'parallel_sum__linear_kernel_hedgehog__mixer_gmlp__prompt'

def setup_inputs():

    _w = lambda a: (lambda t: t.to(torch.bfloat16) if t.is_floating_point() else t)(torch.as_tensor(a))

    def _A_setup():
        B, S, D, H = (1, 2048, 512, 32)
        Dh = D // H
        return {'x': _w((lambda *s: torch.randn(*s, dtype=torch.bfloat16) * 0.05)(B, S, D)), 'wqkv': _w((lambda *s: torch.randn(*s, dtype=torch.bfloat16) * 0.8)(D, 3 * D)), 'w_phi': _w((lambda *s: torch.randn(*s, dtype=torch.bfloat16) * 0.8)(H, Dh, Dh) * Dh ** (-0.5)), 'wo': _w((lambda *s: torch.randn(*s, dtype=torch.bfloat16) * 0.1)(D, D))}
    _A_in = _A_setup()

    def _B_setup():
        B, S, D, F = (1, 2048, 512, 1376)
        Fh = F // 2
        return {'x': _w((lambda *s: torch.randn(*s, dtype=torch.bfloat16) * 0.05)(B, S, D)), 'w_in': _w((lambda *s: torch.randn(*s, dtype=torch.bfloat16) * 0.4)(D, 2 * Fh)), 'w_sp': _w(0.01 * (lambda *s: torch.randn(*s, dtype=torch.bfloat16) * 0.4)(S, S)), 'b_sp': _w(1.0 + 0.01 * (lambda *s: torch.randn(*s, dtype=torch.bfloat16) * 0.05)(S)), 'w_out': _w((lambda *s: torch.randn(*s, dtype=torch.bfloat16) * 0.05)(Fh, D))}
    _B_in = _B_setup()
    return {'A_x': _A_in['x'], 'A_wqkv': _A_in['wqkv'], 'A_w_phi': _A_in['w_phi'], 'A_wo': _A_in['wo'], 'B_x': _B_in['x'], 'B_w_in': _B_in['w_in'], 'B_w_sp': _B_in['w_sp'], 'B_b_sp': _B_in['b_sp'], 'B_w_out': _B_in['w_out']}

def reference(A_x, A_wqkv, A_w_phi, A_wo, B_x, B_w_in, B_w_sp, B_b_sp, B_w_out):

    def _A_ref(x, wqkv, w_phi, wo):
        B, S, D = x.shape
        H = 32
        Dh = D // H
        qkv = (lambda x, *d: x.permute(*d))((x @ wqkv).reshape(B, S, 3, H, Dh), 2, 0, 3, 1, 4)
        q, k, v = (qkv[0], qkv[1], qkv[2])
        pq = torch.einsum('bhsd,hdf->bhsf', q, w_phi)
        pk = torch.einsum('bhsd,hdf->bhsf', k, w_phi)
        phi_q = torch.concatenate([_softmax(pq, axis=-1), _softmax(-pq, axis=-1)], axis=-1)
        phi_k = torch.concatenate([_softmax(pk, axis=-1), _softmax(-pk, axis=-1)], axis=-1)
        kv = torch.einsum('bhsf,bhsd->bhfd', phi_k, v)
        z = torch.einsum('bhsf->bhf', phi_k)
        num = torch.einsum('bhqf,bhfd->bhqd', phi_q, kv)
        den = torch.einsum('bhqf,bhf->bhq', phi_q, z)[..., None] + 1e-06
        o = num / den
        return (lambda x, *d: x.permute(*d))(o, 0, 2, 1, 3).reshape(B, S, D) @ wo

    def _B_ref(x, w_in, w_sp, b_sp, w_out):
        B, S, D = x.shape
        F = w_in.shape[-1]
        Fh = F // 2
        z = _gelu(torch.einsum('bsd,df->bsf', x, w_in))
        z1 = z[..., :Fh].clone()
        z2 = _layernorm(z[..., Fh:], axis=-1)
        g = torch.einsum('ts,bsf->btf', w_sp, z2) + b_sp[None, :, None]
        return torch.einsum('bsf,fd->bsd', z1 * g, w_out)
    x = A_x
    xn = _layernorm(x, axis=-1)
    return x + _A_ref(xn, A_wqkv, A_w_phi, A_wo) + _B_ref(xn, B_w_in, B_w_sp, B_b_sp, B_w_out)
```

```cpp
#include <hip/hip_runtime.h>
#include <hip/hip_bf16.h>
#include <stdint.h>

typedef __bf16 bf16x8 __attribute__((ext_vector_type(8)));
typedef __bf16 bf16x2 __attribute__((ext_vector_type(2)));
typedef short s16x4 __attribute__((ext_vector_type(4)));
typedef short s16x8 __attribute__((ext_vector_type(8)));
typedef float f32x4 __attribute__((ext_vector_type(4)));
typedef float f32x2 __attribute__((ext_vector_type(2)));
typedef unsigned u32x4 __attribute__((ext_vector_type(4)));
typedef unsigned u32x2 __attribute__((ext_vector_type(2)));
#define LAS __attribute__((address_space(3)))
#define DI __device__ __forceinline__

constexpr int S_ = 2048;
constexpr size_t WS_EP   = 0;
constexpr size_t WS_ZST  = 4096;
constexpr size_t WS_KVZ  = WS_ZST + 16 * 4 * 128 * 16;
constexpr size_t WS_KVP  = WS_KVZ + 32 * 16 * 128;
constexpr size_t WS_Z2F  = WS_KVP + 32 * 16 * 2048;
constexpr size_t WS_Z1F  = WS_Z2F + 16 * 44 * 4096;
constexpr size_t WS_A3F  = WS_Z1F + 128 * 43 * 512;
constexpr size_t WS_PQF  = WS_A3F + 128 * 38 * 1024;
constexpr size_t WS_END  = WS_PQF + 32 * 128 * 1024;

DI float asf(unsigned u) { return __builtin_bit_cast(float, u); }
DI unsigned asu(float f) { return __builtin_bit_cast(unsigned, f); }
DI u32x4 mkdesc(const void* p, unsigned bytes) {
  unsigned long long a = (unsigned long long)p;
  u32x4 d = {(unsigned)a, (unsigned)(a >> 32) & 0xffffu, bytes, 0x00020000u};
  return d;
}
DI void dma16(u32x4 desc, int voff, int soff, int lds) {
  asm volatile("s_mov_b32 m0, %3\n\ts_nop 0\n\tbuffer_load_dwordx4 %0, %1, %2 offen lds"
               :: "v"(voff), "s"(desc), "s"(soff), "s"(lds) : "memory");
}
DI u32x4 poll128(u32x4 desc, int voff) {
  u32x4 r;
  asm volatile("buffer_load_dwordx4 %0, %1, %2, 0 offen sc1\n\ts_waitcnt vmcnt(0)" : "=&v"(r) : "v"(voff), "s"(desc) : "memory");
  return r;
}
DI s16x4 trread(const char* p) { return __builtin_amdgcn_ds_read_tr16_b64_v4i16((LAS s16x4*)p); }
DI bf16x8 cat8(s16x4 a, s16x4 b) {
  s16x8 r = __builtin_shufflevector(a, b, 0, 1, 2, 3, 4, 5, 6, 7);
  return __builtin_bit_cast(bf16x8, r);
}
DI f32x4 mfma32(bf16x8 a, bf16x8 b, f32x4 c) { return __builtin_amdgcn_mfma_f32_16x16x32_bf16(a, b, c, 0, 0, 0); }
DI f32x4 mfma16(s16x4 a, s16x4 b, f32x4 c) { return __builtin_amdgcn_mfma_f32_16x16x16bf16_1k(a, b, c, 0, 0, 0); }
DI unsigned pk2(float a, float b) {
  f32x2 f = {a, b};
  return __builtin_bit_cast(unsigned, __builtin_convertvector(f, bf16x2));
}
DI float bflo(unsigned u) { return __builtin_bit_cast(float, u << 16); }
DI float bfhi(unsigned u) { return __builtin_bit_cast(float, u & 0xffff0000u); }
DI float dot2(unsigned a, unsigned b, float c) {
  return __builtin_amdgcn_fdot2_f32_bf16(__builtin_bit_cast(bf16x2, a), __builtin_bit_cast(bf16x2, b), c, false);
}
DI float redg_add(float v) {
  unsigned u = __builtin_bit_cast(unsigned, v);
  auto r = __builtin_amdgcn_permlane16_swap(u, u, false, false);
  v = asf(r[0]) + asf(r[1]);
  u = __builtin_bit_cast(unsigned, v);
  auto r2 = __builtin_amdgcn_permlane32_swap(u, u, false, false);
  return asf(r2[0]) + asf(r2[1]);
}
DI float redg_max(float v) {
  unsigned u = __builtin_bit_cast(unsigned, v);
  auto r = __builtin_amdgcn_permlane16_swap(u, u, false, false);
  v = fmaxf(asf(r[0]), asf(r[1]));
  u = __builtin_bit_cast(unsigned, v);
  auto r2 = __builtin_amdgcn_permlane32_swap(u, u, false, false);
  return fmaxf(asf(r2[0]), asf(r2[1]));
}
DI float redg_min(float v) {
  unsigned u = __builtin_bit_cast(unsigned, v);
  auto r = __builtin_amdgcn_permlane16_swap(u, u, false, false);
  v = fminf(asf(r[0]), asf(r[1]));
  u = __builtin_bit_cast(unsigned, v);
  auto r2 = __builtin_amdgcn_permlane32_swap(u, u, false, false);
  return fminf(asf(r2[0]), asf(r2[1]));
}
constexpr float L2E = 1.4426950408889634f;
DI u32x4 hedgehog(f32x4 p) {
  float mx = redg_max(fmaxf(fmaxf(p[0], p[1]), fmaxf(p[2], p[3])));
  float mn = redg_min(fminf(fminf(p[0], p[1]), fminf(p[2], p[3])));
  float ep[4], en[4];
#pragma unroll
  for (int r = 0; r < 4; ++r) {
    ep[r] = __builtin_amdgcn_exp2f((p[r] - mx) * L2E);
    en[r] = __builtin_amdgcn_exp2f((mn - p[r]) * L2E);
  }
  float sp = redg_add((ep[0] + ep[1]) + (ep[2] + ep[3]));
  float sn = redg_add((en[0] + en[1]) + (en[2] + en[3]));
  float ip = __builtin_amdgcn_rcpf(sp), in = __builtin_amdgcn_rcpf(sn);
  u32x4 o;
  o[0] = pk2(ep[0] * ip, ep[1] * ip);
  o[1] = pk2(ep[2] * ip, ep[3] * ip);
  o[2] = pk2(en[0] * in, en[1] * in);
  o[3] = pk2(en[2] * in, en[3] * in);
  return o;
}
DI float gelu_tanh(float x) {
  const float c1 = -2.0f * 0.7978845608028654f * L2E;
  const float c2 = c1 * 0.044715f;
  float t = x * fmaf(x * x, c2, c1);
  return x * __builtin_amdgcn_rcpf(1.0f + __builtin_amdgcn_exp2f(t));
}
#define WAITV(n) asm volatile("s_waitcnt vmcnt(" #n ")" ::: "memory")
#define BAR() __builtin_amdgcn_s_barrier()
#define WAITL0() asm volatile("s_waitcnt lgkmcnt(0)" ::: "memory")

constexpr int K1_SLOT = 49152;
constexpr int K1_MISC = 3 * K1_SLOT;
constexpr int K1_XST = K1_MISC;
constexpr int K1_CS = K1_MISC + 4096;
constexpr int K1_ZS = K1_MISC + 6144;
constexpr int K1_LDS = 163840;

__global__ __launch_bounds__(512, 2) void k1(const char* __restrict__ x, const char* __restrict__ wqkv,
                                             const unsigned short* __restrict__ wphi, const char* __restrict__ win,
                                             char* ws) {
  extern __shared__ __attribute__((aligned(16))) char smem[];
  const int tid = threadIdx.x, lane = tid & 63;
  const int w = __builtin_amdgcn_readfirstlane(tid >> 6);
  const int wm = w >> 2, wn = w & 3;
  const int r16 = lane & 15, g = lane >> 4;
  const int xcd = blockIdx.x & 7, jj = blockIdx.x >> 3;
  const int rt = (xcd >> 2) * 8 + (jj >> 2);
  const int cg = (xcd & 3) * 4 + (jj & 3);
  const bool isqkv = cg < 8;
  const int zg = cg & 3;
  const int ft0 = zg * 11;
  const int nft = (zg == 3) ? 10 : 11;
  const int rot = ((jj & 3) * 2 + (jj >> 2)) & 7;

  __amdgpu_buffer_rsrc_t wsr = __builtin_amdgcn_make_buffer_rsrc(ws, 0, (int)WS_END, 0x00020000);
  const u32x4 wsd = mkdesc(ws, (unsigned)WS_END);
  unsigned tag = 0;
  if (cg >= 12) tag = __builtin_amdgcn_raw_buffer_load_b32(wsr, (int)(WS_EP + rt * 128), 0, 16) + 1u;

  const u32x4 xd = mkdesc(x, 2048u * 1024u);
  const u32x4 wd = isqkv ? mkdesc(wqkv, 512u * 3072u) : mkdesc(win, 512u * 2752u);
  int xsrc[2];
#pragma unroll
  for (int u = 0; u < 2; ++u) {
    int row = 16 * w + 8 * u + (lane >> 3);
    int c = (lane & 7) ^ ((row >> 1) & 7);
    xsrc[u] = (rt * 128 + row) * 1024 + c * 16;
  }
  int wsrc[4];
  const int colB = (cg >= 12 ? 1376 : 0) + ft0 * 32;
  const int wstride = isqkv ? 64 * 3072 : 64 * 2752;
#pragma unroll
  for (int u = 0; u < 4; ++u) {
    int k = 8 * w + 2 * u + (lane >> 5);
    int h3 = (k & 3) | (((k >> 3) & 1) << 2);
    int s = (lane & 31) ^ (h3 << 1);
    if (isqkv) {
      int L = min(s >> 3, 2);
      wsrc[u] = k * 3072 + L * 1024 + cg * 128 + (s & 7) * 16;
    } else {
      int rb = k * 2752 + colB;
      int sh = rb & 127;
      int off = min(max(s * 16, sh), sh + nft * 32 - 16);
      wsrc[u] = (rb - sh) + off;
    }
  }
  auto issue = [&](int i) {
    int st = (i + rot) & 7;
    int slot = (i % 3) * K1_SLOT;
#pragma unroll
    for (int u = 0; u < 2; ++u) dma16(xd, xsrc[u], st * 128, slot + (16 * w + 8 * u) * 128);
#pragma unroll
    for (int u = 0; u < 4; ++u) dma16(wd, wsrc[u], st * wstride, slot + 16384 + (8 * w + 2 * u) * 512);
  };
  issue(0);
  issue(1);

  int aoff[2];
#pragma unroll
  for (int ks = 0; ks < 2; ++ks) aoff[ks] = (wm * 64 + r16) * 128 + (((4 * ks + g) ^ ((r16 >> 1) & 7)) << 4);
  int boff[3];
  {
    int q = r16 >> 2, p = r16 & 3;
    int krow = 8 * g + q;
    int h3 = q | ((g & 1) << 2);
    int sh = isqkv ? 0 : ((colB + (q & 1) * 64) & 127);
#pragma unroll
    for (int i = 0; i < 3; ++i) {
      int bc = isqkv ? (i * 128 + wn * 32 + 8 * p) : (sh + (3 * wn + i) * 32 + 8 * p);
      boff[i] = 16384 + krow * 512 + ((((bc >> 4) ^ (h3 << 1)) & 31) << 4) + (bc & 8);
    }
  }
  f32x4 acc[4][3];
#pragma unroll
  for (int m = 0; m < 4; ++m)
#pragma unroll
    for (int i = 0; i < 3; ++i) acc[m][i] = f32x4{0.f, 0.f, 0.f, 0.f};
  float xs[4] = {0.f, 0.f, 0.f, 0.f}, xq[4] = {0.f, 0.f, 0.f, 0.f}, cs[3] = {0.f, 0.f, 0.f};
  const unsigned ONES = 0x3f803f80u;

#pragma unroll
  for (int i = 0; i < 8; ++i) {
    if (i < 7) { WAITV(6); } else { WAITV(0); }
    BAR();
    if (i + 2 < 8) issue(i + 2);
    const char* slot = smem + (i % 3) * K1_SLOT;
#pragma unroll
    for (int ks = 0; ks < 2; ++ks) {
      bf16x8 a[4], b[3];
#pragma unroll
      for (int m = 0; m < 4; ++m) a[m] = *(const bf16x8*)(slot + aoff[ks] + m * 2048);
#pragma unroll
      for (int n = 0; n < 3; ++n)
        b[n] = cat8(trread(slot + boff[n] + ks * 16384), trread(slot + boff[n] + ks * 16384 + 2048));
#pragma unroll
      for (int m = 0; m < 4; ++m)
#pragma unroll
        for (int n = 0; n < 3; ++n) acc[m][n] = mfma32(b[n], a[m], acc[m][n]);
      if (((2 * i + ks) & 3) == wn) {
#pragma unroll
        for (int m = 0; m < 4; ++m) {
          u32x4 v = __builtin_bit_cast(u32x4, a[m]);
#pragma unroll
          for (int e = 0; e < 4; ++e) { xs[m] = dot2(v[e], ONES, xs[m]); xq[m] = dot2(v[e], v[e], xq[m]); }
        }
      }
      if (ks == wm) {
#pragma unroll
        for (int n = 0; n < 3; ++n) {
          u32x4 v = __builtin_bit_cast(u32x4, b[n]);
#pragma unroll
          for (int e = 0; e < 4; ++e) cs[n] = dot2(v[e], ONES, cs[n]);
        }
      }
    }
    WAITL0();
  }
#pragma unroll
  for (int m = 0; m < 4; ++m) {
    float a = redg_add(xs[m]), b = redg_add(xq[m]);
    if (g == 0) *(f32x2*)(smem + K1_XST + (wn * 128 + wm * 64 + m * 16 + r16) * 8) = f32x2{a, b};
  }
#pragma unroll
  for (int n = 0; n < 3; ++n) {
    float a = redg_add(cs[n]);
    if (g == 0) *(float*)(smem + K1_CS + (wm * 192 + (wn * 3 + n) * 16 + r16) * 4) = a;
  }
  __syncthreads();
  float rs[4], rm[4];
#pragma unroll
  for (int m = 0; m < 4; ++m) {
    float s = 0.f, q = 0.f;
#pragma unroll
    for (int j = 0; j < 4; ++j) {
      f32x2 v = *(const f32x2*)(smem + K1_XST + (j * 128 + wm * 64 + m * 16 + r16) * 8);
      s += v[0]; q += v[1];
    }
    float mean = s * (1.0f / 512.0f);
    float var = fmaxf(q * (1.0f / 512.0f) - mean * mean, 0.f);
    rs[m] = __builtin_amdgcn_rsqf(var + 1e-5f);
    rm[m] = mean * rs[m];
  }
#pragma unroll
  for (int n = 0; n < 3; ++n) {
    f32x4 c0 = *(const f32x4*)(smem + K1_CS + ((wn * 3 + n) * 16 + 4 * g) * 4);
    f32x4 c1 = *(const f32x4*)(smem + K1_CS + (192 + (wn * 3 + n) * 16 + 4 * g) * 4);
    f32x4 c = c0 + c1;
#pragma unroll
    for (int m = 0; m < 4; ++m)
#pragma unroll
      for (int r = 0; r < 4; ++r) acc[m][n][r] = acc[m][n][r] * rs[m] - rm[m] * c[r];
  }
  char* scr = smem + w * 8192;
  const int tq = r16 >> 2, tp = r16 & 3;
  if (isqkv) {
    const int h = cg * 4 + wn;
    s16x4 wph;
#pragma unroll
    for (int j = 0; j < 4; ++j) wph[j] = (short)wphi[h * 256 + (4 * g + j) * 16 + r16];
    const f32x4 zero4 = {0.f, 0.f, 0.f, 0.f};
#pragma unroll
    for (int m = 0; m < 4; ++m) {
      u32x2 qf = {pk2(acc[m][0][0], acc[m][0][1]), pk2(acc[m][0][2], acc[m][0][3])};
      u32x2 kf = {pk2(acc[m][1][0], acc[m][1][1]), pk2(acc[m][1][2], acc[m][1][3])};
      u32x2 vf = {pk2(acc[m][2][0], acc[m][2][1]), pk2(acc[m][2][2], acc[m][2][3])};
      u32x4 pq = hedgehog(mfma16(wph, __builtin_bit_cast(s16x4, qf), zero4));
      u32x4 pk = hedgehog(mfma16(wph, __builtin_bit_cast(s16x4, kf), zero4));
      *(u32x4*)(ws + WS_PQF + ((size_t)h * 128 + rt * 8 + wm * 4 + m) * 1024 + lane * 16) = pq;
      *(u32x4*)(scr + (16 * m + r16) * 64 + g * 16) = pk;
      *(u32x2*)(scr + 4096 + (16 * m + r16) * 32 + g * 8) = vf;
    }
    f32x4 kvp = zero4, kvn = zero4, zp = zero4, zn = zero4;
    u32x4 o4 = {ONES, ONES, ONES, ONES};
    bf16x8 ones8 = __builtin_bit_cast(bf16x8, o4);
#pragma unroll
    for (int mp = 0; mp < 2; ++mp) {
      int row = 32 * mp + 8 * g + tq;
      bf16x8 ap = cat8(trread(scr + row * 64 + tp * 16), trread(scr + (row + 4) * 64 + tp * 16));
      bf16x8 an = cat8(trread(scr + row * 64 + tp * 16 + 8), trread(scr + (row + 4) * 64 + tp * 16 + 8));
      bf16x8 bv = cat8(trread(scr + 4096 + row * 32 + tp * 8), trread(scr + 4096 + (row + 4) * 32 + tp * 8));
      kvp = mfma32(ap, bv, kvp);
      kvn = mfma32(an, bv, kvn);
      zp = mfma32(ap, ones8, zp);
      zn = mfma32(an, ones8, zn);
    }
    char* red = smem + 65536 + wn * 4096;
    if (wm == 1) {
      *(f32x4*)(red + lane * 16) = kvp;
      *(f32x4*)(red + 1024 + lane * 16) = kvn;
      *(f32x4*)(red + 2048 + lane * 16) = zp;
      *(f32x4*)(red + 3072 + lane * 16) = zn;
    }
    __syncthreads();
    if (wm == 0) {
      kvp += *(const f32x4*)(red + lane * 16);
      kvn += *(const f32x4*)(red + 1024 + lane * 16);
      zp += *(const f32x4*)(red + 2048 + lane * 16);
      zn += *(const f32x4*)(red + 3072 + lane * 16);
      char* dst = ws + WS_KVP + ((size_t)h * 16 + rt) * 2048 + lane * 32;
      *(f32x4*)dst = kvp;
      *(f32x4*)(dst + 16) = kvn;
      if (r16 == 0) {
        char* dz = ws + WS_KVZ + ((size_t)h * 16 + rt) * 128 + g * 32;
        *(f32x4*)dz = zp;
        *(f32x4*)(dz + 16) = zn;
      }
    }
  } else {
    u32x2 zb[4][3];
#pragma unroll
    for (int m = 0; m < 4; ++m)
#pragma unroll
      for (int n = 0; n < 3; ++n) {
        zb[m][n][0] = pk2(gelu_tanh(acc[m][n][0]), gelu_tanh(acc[m][n][1]));
        zb[m][n][1] = pk2(gelu_tanh(acc[m][n][2]), gelu_tanh(acc[m][n][3]));
      }
    if (cg < 12) {
#pragma unroll
      for (int n = 0; n < 3; ++n)
        if (3 * wn + n < nft) {
#pragma unroll
          for (int m = 0; m < 4; ++m)
            *(u32x2*)(ws + WS_Z1F + ((size_t)(rt * 8 + wm * 4 + m) * 43 + ft0 + 3 * wn + n) * 512 + lane * 8) = zb[m][n];
        }
    } else {
#pragma unroll
      for (int m = 0; m < 4; ++m) {
        float s = 0.f, q = 0.f;
#pragma unroll
        for (int n = 0; n < 3; ++n)
          if (3 * wn + n < nft) {
            s = dot2(zb[m][n][0], ONES, s); s = dot2(zb[m][n][1], ONES, s);
            q = dot2(zb[m][n][0], zb[m][n][0], q); q = dot2(zb[m][n][1], zb[m][n][1], q);
          }
        s = redg_add(s); q = redg_add(q);
        if (g == 0) *(f32x2*)(smem + K1_ZS + (wn * 128 + wm * 64 + m * 16 + r16) * 8) = f32x2{s, q};
      }
      __syncthreads();
      if (tid < 128) {
        float s = 0.f, q = 0.f;
#pragma unroll
        for (int j = 0; j < 4; ++j) {
          f32x2 v = *(const f32x2*)(smem + K1_ZS + (j * 128 + tid) * 8);
          s += v[0]; q += v[1];
        }
        u32x4 gr = {__builtin_bit_cast(unsigned, s), tag, __builtin_bit_cast(unsigned, q), tag};
        __builtin_amdgcn_raw_buffer_store_b128(gr, wsr, (int)(WS_ZST + ((rt * 4 + zg) * 128 + tid) * 16), 0, 16);
      }
      {
        int off = (int)(WS_ZST + (rt * 4 * 128 + tid) * 16);
        u32x4 gr;
        for (int spin = 0; spin < (1 << 20); ++spin) {
          gr = poll128(wsd, off);
          bool ok = (gr[1] == tag) && (gr[3] == tag);
          if (__all(ok)) break;
          __builtin_amdgcn_s_sleep(1);
        }
        *(f32x2*)(smem + K1_XST + tid * 8) = f32x2{asf(gr[0]), asf(gr[2])};
      }
      __syncthreads();
      if (zg == 0 && tid == 0) __builtin_amdgcn_raw_buffer_store_b32(tag, wsr, (int)(WS_EP + rt * 128), 0, 16);
#pragma unroll
      for (int m = 0; m < 4; ++m) {
        float s = 0.f, q = 0.f;
#pragma unroll
        for (int j = 0; j < 4; ++j) {
          f32x2 v = *(const f32x2*)(smem + K1_XST + (j * 128 + wm * 64 + m * 16 + r16) * 8);
          s += v[0]; q += v[1];
        }
        float mean = s * (1.0f / 688.0f);
        float var = fmaxf(q * (1.0f / 688.0f) - mean * mean, 0.f);
        float r = __builtin_amdgcn_rsqf(var + 1e-5f);
        float c = -mean * r;
#pragma unroll
        for (int n = 0; n < 3; ++n) {
          u32x2 o;
          o[0] = pk2(fmaf(bflo(zb[m][n][0]), r, c), fmaf(bfhi(zb[m][n][0]), r, c));
          o[1] = pk2(fmaf(bflo(zb[m][n][1]), r, c), fmaf(bfhi(zb[m][n][1]), r, c));
          *(u32x2*)(scr + n * 2048 + (16 * m + r16) * 32 + g * 8) = o;
        }
      }
#pragma unroll
      for (int n = 0; n < 3; ++n)
#pragma unroll
        for (int kk = 0; kk < 2; ++kk) {
          int row = 32 * kk + 8 * g + tq;
          bf16x8 f = cat8(trread(scr + n * 2048 + row * 32 + tp * 8), trread(scr + n * 2048 + (row + 4) * 32 + tp * 8));
          if (3 * wn + n < nft)
            *(bf16x8*)(ws + WS_Z2F + (((size_t)rt * 44 + ft0 + 3 * wn + n) * 4 + 2 * wm + kk) * 1024 + lane * 16) = f;
        }
    }
  }
}

constexpr int K2_SLOT = 32768;
constexpr int K2_NSLOT = 4;
constexpr int K2_MISC = K2_NSLOT * K2_SLOT;
constexpr int K2_LDS = 163840;

__global__ __launch_bounds__(512, 2) void k2(const char* __restrict__ wsp, const unsigned short* __restrict__ bsp,
                                             char* ws) {
  extern __shared__ __attribute__((aligned(16))) char smem[];
  const int tid = threadIdx.x, lane = tid & 63;
  const int w = __builtin_amdgcn_readfirstlane(tid >> 6);
  const int th = w & 1, kq = w >> 1;
  const int r16 = lane & 15, g = lane >> 4;
  const int xcd = blockIdx.x & 7, jj = blockIdx.x >> 3;
  const int rt = xcd * 4 + (jj >> 3);
  const int cgp = jj & 7;
  const int ft0 = cgp < 7 ? cgp * 6 : 40;
  const int npair = cgp < 6 ? 3 : 2;
  const int rot = (cgp + 2 * (jj >> 3)) & 7;

  const u32x4 ad = mkdesc(wsp, 2048u * 4096u);
  const u32x4 wsd = mkdesc(ws, (unsigned)WS_END);
  int asrc;
  {
    int row = 8 * w + (lane >> 3);
    int c = (lane & 7) ^ ((row >> 1) & 7);
    asrc = (rt * 64 + row) * 4096 + c * 16;
  }
  const int bsrc = (int)WS_Z2F + ft0 * 4096 + w * 1024 + lane * 16;
  auto issue = [&](int i) {
    int slot = (i % K2_NSLOT) * K2_SLOT;
    if (i < 8) {
      int c = (i + rot) & 7;
#pragma unroll
      for (int u = 0; u < 4; ++u) dma16(ad, asrc, c * 512 + u * 128, slot + u * 8192 + w * 1024);
    } else {
      int st = (i - 8 + 2 * rot) & 15;
#pragma unroll
      for (int u = 0; u < 3; ++u) {
        int uu = u < npair ? u : 0;
        dma16(wsd, bsrc, st * (44 * 4096) + uu * 8192, slot + u * 8192 + w * 1024);
      }
    }
  };
  const int h = blockIdx.x & 31, rb = blockIdx.x >> 5;
  {
    const char* kp = ws + WS_KVP + ((size_t)h * 16 + 2 * w) * 2048 + lane * 32;
    f32x4 a0 = *(const f32x4*)kp, a1 = *(const f32x4*)(kp + 16);
    f32x4 b0 = *(const f32x4*)(kp + 2048), b1 = *(const f32x4*)(kp + 2048 + 16);
    const char* zp = ws + WS_KVZ + ((size_t)h * 16 + 2 * w) * 128 + g * 32;
    f32x4 c0 = *(const f32x4*)zp, c1 = *(const f32x4*)(zp + 16);
    f32x4 d0 = *(const f32x4*)(zp + 128), d1 = *(const f32x4*)(zp + 128 + 16);
    bf16x8 pq[2];
#pragma unroll
    for (int t = 0; t < 2; ++t)
      pq[t] = *(const bf16x8*)(ws + WS_PQF + ((size_t)h * 128 + rb * 16 + 2 * w + t) * 1024 + lane * 16);
    char* red = smem + w * 4096;
    *(f32x4*)(red + lane * 16) = a0 + b0;
    *(f32x4*)(red + 1024 + lane * 16) = a1 + b1;
    *(f32x4*)(red + 2048 + lane * 16) = c0 + d0;
    *(f32x4*)(red + 3072 + lane * 16) = c1 + d1;
    __syncthreads();
    f32x4 kp4 = {0, 0, 0, 0}, kn4 = kp4, zp4 = kp4, zn4 = kp4;
#pragma unroll
    for (int j = 0; j < 8; ++j) {
      kp4 += *(const f32x4*)(smem + j * 4096 + lane * 16);
      kn4 += *(const f32x4*)(smem + j * 4096 + 1024 + lane * 16);
      zp4 += *(const f32x4*)(smem + j * 4096 + 2048 + lane * 16);
      zn4 += *(const f32x4*)(smem + j * 4096 + 3072 + lane * 16);
    }
    u32x4 kvo = {pk2(kp4[0], kp4[1]), pk2(kp4[2], kp4[3]), pk2(kn4[0], kn4[1]), pk2(kn4[2], kn4[3])};
    u32x4 zo = {pk2(zp4[0], zp4[1]), pk2(zp4[2], zp4[3]), pk2(zn4[0], zn4[1]), pk2(zn4[2], zn4[3])};
    const f32x4 zero4 = {0.f, 0.f, 0.f, 0.f};
#pragma unroll
    for (int t = 0; t < 2; ++t) {
      f32x4 num = mfma32(__builtin_bit_cast(bf16x8, kvo), pq[t], zero4);
      f32x4 den = mfma32(__builtin_bit_cast(bf16x8, zo), pq[t], zero4);
      float inv = 1.0f / (den[0] + 1e-6f);
      u32x2 o = {pk2(num[0] * inv, num[1] * inv), pk2(num[2] * inv, num[3] * inv)};
      *(u32x2*)(ws + WS_A3F + ((size_t)(rb * 16 + 2 * w + t) * 38 + (h >> 1)) * 1024 + lane * 16 + (h & 1) * 8) = o;
    }
    WAITV(0);
    __syncthreads();
  }
  issue(0); issue(1); issue(2);

  bf16x8 af[8][2][2];
  int aoff;
  {
    int row = th * 32 + r16;
    int chunk = (kq & 1) * 4 + g;
    aoff = (kq >> 1) * 8192 + row * 128 + ((chunk ^ ((r16 >> 1) & 7)) << 4);
  }
#pragma unroll
  for (int i = 0; i < 8; ++i) {
    if (i < 6) { WAITV(8); } else if (i == 6) { WAITV(7); } else { WAITV(6); }
    BAR();
    issue(i + 3);
    const char* slot = smem + (i % K2_NSLOT) * K2_SLOT;
#pragma unroll
    for (int hh = 0; hh < 2; ++hh)
#pragma unroll
      for (int t = 0; t < 2; ++t) af[i][hh][t] = *(const bf16x8*)(slot + aoff + hh * 16384 + t * 2048);
    WAITL0();
  }
  f32x4 acc[6][2];
#pragma unroll
  for (int n = 0; n < 6; ++n)
#pragma unroll
    for (int t = 0; t < 2; ++t) acc[n][t] = f32x4{0.f, 0.f, 0.f, 0.f};
#pragma unroll
  for (int i = 8; i < 24; ++i) {
    if (i < 22) { WAITV(6); } else if (i == 22) { WAITV(3); } else { WAITV(0); }
    BAR();
    if (i + 3 < 24) issue(i + 3);
    const char* slot = smem + (i % K2_NSLOT) * K2_SLOT;
    const int c = (i - 8) >> 1, hh = (i - 8) & 1;
    bf16x8 b[6];
#pragma unroll
    for (int n = 0; n < 6; ++n) b[n] = *(const bf16x8*)(slot + n * 4096 + kq * 1024 + lane * 16);
#pragma unroll
    for (int n = 0; n < 6; ++n)
#pragma unroll
      for (int t = 0; t < 2; ++t) acc[n][t] = mfma32(b[n], af[c][hh][t], acc[n][t]);
    WAITL0();
  }
  __syncthreads();
#pragma unroll
  for (int n = 0; n < 6; ++n)
#pragma unroll
    for (int t = 0; t < 2; ++t) *(f32x4*)(smem + ((th * 4 + kq) * 12 + n * 2 + t) * 1024 + lane * 16) = acc[n][t];
  __syncthreads();
#pragma unroll
  for (int e = 0; e < 2; ++e) {
    int un = kq + 4 * e;
    if (un < 2 * npair) {
      int t = un & 1, p = un >> 1;
      int tt = rt * 4 + th * 2 + t;
      float bias = __builtin_bit_cast(float, (unsigned)bsp[tt * 16 + r16] << 16);
      u32x4 o;
#pragma unroll
      for (int hf = 0; hf < 2; ++hf) {
        int n = 2 * p + hf;
        f32x4 s = {0.f, 0.f, 0.f, 0.f};
#pragma unroll
        for (int j = 0; j < 4; ++j) s += *(const f32x4*)(smem + ((th * 4 + j) * 12 + n * 2 + t) * 1024 + lane * 16);
        int ft = ft0 + n;
        if (ft < 43) {
          u32x2 z1 = *(const u32x2*)(ws + WS_Z1F + ((size_t)tt * 43 + ft) * 512 + lane * 8);
          o[2 * hf] = pk2(bflo(z1[0]) * (s[0] + bias), bfhi(z1[0]) * (s[1] + bias));
          o[2 * hf + 1] = pk2(bflo(z1[1]) * (s[2] + bias), bfhi(z1[1]) * (s[3] + bias));
        } else {
          o[2 * hf] = 0; o[2 * hf + 1] = 0;
        }
      }
      *(u32x4*)(ws + WS_A3F + ((size_t)tt * 38 + 16 + (ft0 >> 1) + p) * 1024 + lane * 16) = o;
    }
  }
}

constexpr int K3_LDS = 1216 * 128;

__global__ __launch_bounds__(512, 2) void k3(const char* __restrict__ x, const char* __restrict__ wo,
                                             const char* __restrict__ wout, const char* ws,
                                             char* __restrict__ out) {
  extern __shared__ __attribute__((aligned(16))) char smem[];
  const int tid = threadIdx.x, lane = tid & 63;
  const int w = __builtin_amdgcn_readfirstlane(tid >> 6);
  const int mh = w & 1, kq = w >> 1;
  const int r16 = lane & 15, g = lane >> 4;
  const int xcd = blockIdx.x & 7, jj = blockIdx.x >> 3;
  const int rt = xcd * 4 + (jj >> 3);
  const int nc = jj & 7;
  bf16x8 af[10][2];
#pragma unroll
  for (int i = 0; i < 10; ++i) {
    int ks = min(4 * i + kq, 37);
#pragma unroll
    for (int t = 0; t < 2; ++t)
      af[i][t] = *(const bf16x8*)(ws + WS_A3F + ((size_t)(rt * 4 + mh * 2 + t) * 38 + ks) * 1024 + lane * 16);
  }
  {
    const u32x4 od = mkdesc(wo, 512u * 1024u), ud = mkdesc(wout, 688u * 1024u);
    int rr = lane >> 3;
    int h2 = ((rr >> 1) & 1) | (((rr >> 2) & 1) << 1);
    int voff = rr * 1024 + nc * 128 + (((lane & 7) ^ (h2 << 1)) << 4);
#pragma unroll
    for (int i = 0; i < 19; ++i) {
      if (i < 8) dma16(od, voff, (i * 8 + w) * 8192, (i * 8 + w) * 1024);
      else dma16(ud, voff, ((i - 8) * 8 + w) * 8192, (i * 8 + w) * 1024);
    }
  }
  f32x4 acc[4][2];
#pragma unroll
  for (int n = 0; n < 4; ++n)
#pragma unroll
    for (int t = 0; t < 2; ++t) acc[n][t] = f32x4{0.f, 0.f, 0.f, 0.f};
  int boff[4];
  {
    int q = r16 >> 2, p = r16 & 3;
    int k = 4 * g + q;
    int h2 = ((k >> 1) & 1) | (((k >> 2) & 1) << 1);
#pragma unroll
    for (int n = 0; n < 4; ++n) {
      int bc = n * 32 + 8 * p;
      boff[n] = k * 128 + (((bc >> 4) ^ (h2 << 1)) << 4) + (bc & 8) + kq * 4096;
    }
  }
  WAITV(0);
  __syncthreads();
#pragma unroll
  for (int i = 0; i < 10; ++i) {
    if (4 * i + kq < 38) {
      bf16x8 b[4];
#pragma unroll
      for (int n = 0; n < 4; ++n)
        b[n] = cat8(trread(smem + boff[n] + i * 16384), trread(smem + boff[n] + i * 16384 + 2048));
#pragma unroll
      for (int n = 0; n < 4; ++n)
#pragma unroll
        for (int t = 0; t < 2; ++t) acc[n][t] = mfma32(b[n], af[i][t], acc[n][t]);
    }
  }
  __syncthreads();
#pragma unroll
  for (int n = 0; n < 4; ++n)
#pragma unroll
    for (int t = 0; t < 2; ++t)
      *(f32x4*)(smem + kq * 17408 + (mh * 32 + t * 16 + r16) * 272 + (n * 16 + 4 * g) * 4) = acc[n][t];
  __syncthreads();
  {
    int row = tid >> 3, c8 = tid & 7;
    f32x4 s0 = {0.f, 0.f, 0.f, 0.f}, s1 = s0;
#pragma unroll
    for (int j = 0; j < 4; ++j) {
      s0 += *(const f32x4*)(smem + j * 17408 + row * 272 + c8 * 32);
      s1 += *(const f32x4*)(smem + j * 17408 + row * 272 + c8 * 32 + 16);
    }
    size_t go = (size_t)(rt * 64 + row) * 1024 + nc * 128 + c8 * 16;
    u32x4 xv = *(const u32x4*)(x + go);
    u32x4 o;
    o[0] = pk2(s0[0] + bflo(xv[0]), s0[1] + bfhi(xv[0]));
    o[1] = pk2(s0[2] + bflo(xv[1]), s0[3] + bfhi(xv[1]));
    o[2] = pk2(s1[0] + bflo(xv[2]), s1[1] + bfhi(xv[2]));
    o[3] = pk2(s1[2] + bflo(xv[3]), s1[3] + bfhi(xv[3]));
    *(u32x4*)(out + go) = o;
  }
}

extern "C" void kernel_launch(void* const* d_in, const int* in_sizes, int n_in, void* d_out, int out_size, void* d_ws,
                              size_t ws_size, hipStream_t stream) {
  const char* x = (const char*)d_in[0];
  const char* wqkv = (const char*)d_in[1];
  const unsigned short* wphi = (const unsigned short*)d_in[2];
  const char* wo = (const char*)d_in[3];
  const char* win = (const char*)d_in[5];
  const char* wsp = (const char*)d_in[6];
  const unsigned short* bsp = (const unsigned short*)d_in[7];
  const char* wout = (const char*)d_in[8];
  char* ws = (char*)d_ws;
  hipFuncSetAttribute((const void*)k1, hipFuncAttributeMaxDynamicSharedMemorySize, K1_LDS);
  hipFuncSetAttribute((const void*)k2, hipFuncAttributeMaxDynamicSharedMemorySize, K2_LDS);
  hipFuncSetAttribute((const void*)k3, hipFuncAttributeMaxDynamicSharedMemorySize, K3_LDS);
  k1<<<256, 512, K1_LDS, stream>>>(x, wqkv, wphi, win, ws);
  k2<<<256, 512, K2_LDS, stream>>>(wsp, bsp, ws);
  k3<<<256, 512, K3_LDS, stream>>>(x, wo, wout, ws, (char*)d_out);
}
```

```cpp
#include <hip/hip_runtime.h>
#include <hip/hip_bf16.h>
#include <stdint.h>

typedef __bf16 bf16x8 __attribute__((ext_vector_type(8)));
typedef __bf16 bf16x2 __attribute__((ext_vector_type(2)));
typedef short s16x4 __attribute__((ext_vector_type(4)));
typedef short s16x8 __attribute__((ext_vector_type(8)));
typedef float f32x4 __attribute__((ext_vector_type(4)));
typedef float f32x2 __attribute__((ext_vector_type(2)));
typedef unsigned u32x4 __attribute__((ext_vector_type(4)));
typedef unsigned u32x2 __attribute__((ext_vector_type(2)));
#define LAS __attribute__((address_space(3)))
#define DI __device__ __forceinline__

constexpr int S_ = 2048;
constexpr size_t WS_EP   = 0;
constexpr size_t WS_ZST  = 4096;
constexpr size_t WS_KVZ  = WS_ZST + 16 * 4 * 128 * 16;
constexpr size_t WS_KVP  = WS_KVZ + 32 * 16 * 128;
constexpr size_t WS_Z2F  = WS_KVP + 32 * 16 * 2048;
constexpr size_t WS_Z1F  = WS_Z2F + 16 * 44 * 4096;
constexpr size_t WS_A3F  = WS_Z1F + 128 * 43 * 512;
constexpr size_t WS_PQF  = WS_A3F + 128 * 38 * 1024;
constexpr size_t WS_END  = WS_PQF + 32 * 128 * 1024;

DI float asf(unsigned u) { return __builtin_bit_cast(float, u); }
DI unsigned asu(float f) { return __builtin_bit_cast(unsigned, f); }
DI u32x4 mkdesc(const void* p, unsigned bytes) {
  unsigned long long a = (unsigned long long)p;
  u32x4 d = {(unsigned)a, (unsigned)(a >> 32) & 0xffffu, bytes, 0x00020000u};
  return d;
}
DI void dma16(u32x4 desc, int voff, int soff, int lds) {
  asm volatile("s_mov_b32 m0, %3\n\ts_nop 0\n\tbuffer_load_dwordx4 %0, %1, %2 offen lds"
               :: "v"(voff), "s"(desc), "s"(soff), "s"(lds) : "memory");
}
DI u32x4 poll128(u32x4 desc, int voff) {
  u32x4 r;
  asm volatile("buffer_load_dwordx4 %0, %1, %2, 0 offen sc1\n\ts_waitcnt vmcnt(0)" : "=&v"(r) : "v"(voff), "s"(desc) : "memory");
  return r;
}
DI s16x4 trread(const char* p) { return __builtin_amdgcn_ds_read_tr16_b64_v4i16((LAS s16x4*)p); }
DI bf16x8 cat8(s16x4 a, s16x4 b) {
  s16x8 r = __builtin_shufflevector(a, b, 0, 1, 2, 3, 4, 5, 6, 7);
  return __builtin_bit_cast(bf16x8, r);
}
DI f32x4 mfma32(bf16x8 a, bf16x8 b, f32x4 c) { return __builtin_amdgcn_mfma_f32_16x16x32_bf16(a, b, c, 0, 0, 0); }
DI f32x4 mfma16(s16x4 a, s16x4 b, f32x4 c) { return __builtin_amdgcn_mfma_f32_16x16x16bf16_1k(a, b, c, 0, 0, 0); }
DI unsigned pk2(float a, float b) {
  f32x2 f = {a, b};
  return __builtin_bit_cast(unsigned, __builtin_convertvector(f, bf16x2));
}
DI float bflo(unsigned u) { return __builtin_bit_cast(float, u << 16); }
DI float bfhi(unsigned u) { return __builtin_bit_cast(float, u & 0xffff0000u); }
DI float dot2(unsigned a, unsigned b, float c) {
  return __builtin_amdgcn_fdot2_f32_bf16(__builtin_bit_cast(bf16x2, a), __builtin_bit_cast(bf16x2, b), c, false);
}
DI float redg_add(float v) {
  unsigned u = __builtin_bit_cast(unsigned, v);
  auto r = __builtin_amdgcn_permlane16_swap(u, u, false, false);
  v = asf(r[0]) + asf(r[1]);
  u = __builtin_bit_cast(unsigned, v);
  auto r2 = __builtin_amdgcn_permlane32_swap(u, u, false, false);
  return asf(r2[0]) + asf(r2[1]);
}
DI float redg_max(float v) {
  unsigned u = __builtin_bit_cast(unsigned, v);
  auto r = __builtin_amdgcn_permlane16_swap(u, u, false, false);
  v = fmaxf(asf(r[0]), asf(r[1]));
  u = __builtin_bit_cast(unsigned, v);
  auto r2 = __builtin_amdgcn_permlane32_swap(u, u, false, false);
  return fmaxf(asf(r2[0]), asf(r2[1]));
}
DI float redg_min(float v) {
  unsigned u = __builtin_bit_cast(unsigned, v);
  auto r = __builtin_amdgcn_permlane16_swap(u, u, false, false);
  v = fminf(asf(r[0]), asf(r[1]));
  u = __builtin_bit_cast(unsigned, v);
  auto r2 = __builtin_amdgcn_permlane32_swap(u, u, false, false);
  return fminf(asf(r2[0]), asf(r2[1]));
}
constexpr float L2E = 1.4426950408889634f;
DI u32x4 hedgehog(f32x4 p) {
  float mx = redg_max(fmaxf(fmaxf(p[0], p[1]), fmaxf(p[2], p[3])));
  float mn = redg_min(fminf(fminf(p[0], p[1]), fminf(p[2], p[3])));
  float ep[4], en[4];
#pragma unroll
  for (int r = 0; r < 4; ++r) {
    ep[r] = __builtin_amdgcn_exp2f((p[r] - mx) * L2E);
    en[r] = __builtin_amdgcn_exp2f((mn - p[r]) * L2E);
  }
  float sp = redg_add((ep[0] + ep[1]) + (ep[2] + ep[3]));
  float sn = redg_add((en[0] + en[1]) + (en[2] + en[3]));
  float ip = __builtin_amdgcn_rcpf(sp), in = __builtin_amdgcn_rcpf(sn);
  u32x4 o;
  o[0] = pk2(ep[0] * ip, ep[1] * ip);
  o[1] = pk2(ep[2] * ip, ep[3] * ip);
  o[2] = pk2(en[0] * in, en[1] * in);
  o[3] = pk2(en[2] * in, en[3] * in);
  return o;
}
DI float gelu_tanh(float x) {
  const float c1 = -2.0f * 0.7978845608028654f * L2E;
  const float c2 = c1 * 0.044715f;
  float t = x * fmaf(x * x, c2, c1);
  return x * __builtin_amdgcn_rcpf(1.0f + __builtin_amdgcn_exp2f(t));
}
#define STAMP(kid, i)
#define WAITV(n) asm volatile("s_waitcnt vmcnt(" #n ")" ::: "memory")
#define BAR() __builtin_amdgcn_s_barrier()
#define WAITL0() asm volatile("s_waitcnt lgkmcnt(0)" ::: "memory")

constexpr int K1_SLOT = 49152;
constexpr int K1_MISC = 3 * K1_SLOT;
constexpr int K1_XST = K1_MISC;
constexpr int K1_CS = K1_MISC + 4096;
constexpr int K1_ZS = K1_MISC + 6144;
constexpr int K1_LDS = 163840;

__global__ __launch_bounds__(512, 2) void k1(const char* __restrict__ x, const char* __restrict__ wqkv,
                                             const unsigned short* __restrict__ wphi, const char* __restrict__ win,
                                             char* ws) {
  extern __shared__ __attribute__((aligned(16))) char smem[];
  const int tid = threadIdx.x, lane = tid & 63;
  const int w = __builtin_amdgcn_readfirstlane(tid >> 6);
  const int wm = w >> 2, wn = w & 3;
  const int r16 = lane & 15, g = lane >> 4;
  const int xcd = blockIdx.x & 7, jj = blockIdx.x >> 3;
  const int rt = (xcd >> 2) * 8 + (jj >> 2);
  const int cg = (xcd & 3) * 4 + (jj & 3);
  const bool isqkv = cg < 8;
  const int zg = cg & 3;
  const int ft0 = zg * 11;
  const int nft = (zg == 3) ? 10 : 11;
  const int rot = ((jj & 3) * 2 + (jj >> 2)) & 7;

  __amdgpu_buffer_rsrc_t wsr = __builtin_amdgcn_make_buffer_rsrc(ws, 0, (int)WS_END, 0x00020000);
  const u32x4 wsd = mkdesc(ws, (unsigned)WS_END);
  unsigned tag = 0;
  if (cg >= 12) tag = __builtin_amdgcn_raw_buffer_load_b32(wsr, (int)(WS_EP + rt * 128), 0, 16) + 1u;

  const u32x4 xd = mkdesc(x, 2048u * 1024u);
  const u32x4 wd = isqkv ? mkdesc(wqkv, 512u * 3072u) : mkdesc(win, 512u * 2752u);
  int xsrc[2];
#pragma unroll
  for (int u = 0; u < 2; ++u) {
    int row = 16 * w + 8 * u + (lane >> 3);
    int c = (lane & 7) ^ ((row >> 1) & 7);
    xsrc[u] = (rt * 128 + row) * 1024 + c * 16;
  }
  int wsrc[4];
  const int colB = (cg >= 12 ? 1376 : 0) + ft0 * 32;
  const int wstride = isqkv ? 64 * 3072 : 64 * 2752;
  {
    int k = 8 * w + (lane >> 3);
    int h2 = ((k >> 1) & 1) | (((k >> 3) & 1) << 1);
    int c = (lane & 7) ^ (h2 << 1);
#pragma unroll
    for (int u = 0; u < 4; ++u) {
      if (isqkv) {
        wsrc[u] = k * 3072 + u * 1024 + cg * 128 + c * 16;
      } else {
        int rb = k * 2752 + colB;
        int sh = rb & 127;
        int off = min(max(u * 128 + c * 16, sh), sh + nft * 32 - 16);
        wsrc[u] = (rb - sh) + off;
      }
    }
  }
  auto issue1 = [&](int i, int u) {
    int st = (i + rot) & 7;
    int slot = (i % 3) * K1_SLOT;
    if (u < 2) dma16(xd, xsrc[u], st * 128, slot + (16 * w + 8 * u) * 128);
    else if (u < 5 || !isqkv) dma16(wd, wsrc[u - 2], st * wstride, slot + 16384 + (u - 2) * 8192 + w * 1024);
  };
  auto issue = [&](int i) {
#pragma unroll
    for (int u = 0; u < 6; ++u) issue1(i, u);
  };
#define WAITS() do { if (isqkv) { WAITV(5); } else { WAITV(6); } } while (0)
  STAMP(0,0);
  issue(0);
  issue(1);

  int aoff[2];
#pragma unroll
  for (int ks = 0; ks < 2; ++ks) aoff[ks] = (wm * 64 + r16) * 128 + (((4 * ks + g) ^ ((r16 >> 1) & 7)) << 4);
  int boff[3];
  {
    int q = r16 >> 2, p = r16 & 3;
    int krow = 8 * g + q;
    int h2 = ((q >> 1) & 1) | ((g & 1) << 1);
    int sh = isqkv ? 0 : ((colB + (q & 1) * 64) & 127);
#pragma unroll
    for (int i = 0; i < 3; ++i) {
      int bc = isqkv ? (i * 128 + wn * 32 + 8 * p) : (sh + (3 * wn + i) * 32 + 8 * p);
      boff[i] = 16384 + (bc >> 7) * 8192 + krow * 128 + (((((bc >> 4) & 7) ^ (h2 << 1))) << 4) + (bc & 8);
    }
  }
  f32x4 acc[4][3];
#pragma unroll
  for (int m = 0; m < 4; ++m)
#pragma unroll
    for (int i = 0; i < 3; ++i) acc[m][i] = f32x4{0.f, 0.f, 0.f, 0.f};
  float xs[4] = {0.f, 0.f, 0.f, 0.f}, xq[4] = {0.f, 0.f, 0.f, 0.f}, cs[3] = {0.f, 0.f, 0.f};
  const unsigned ONES = 0x3f803f80u;

#define SEG(acc_)
  WAITS();
  BAR();
  STAMP(0,1);
  if (wm == 1) BAR();
  SEG(tb);
#pragma unroll
  for (int i = 0; i < 8; ++i) {
    const char* slot = smem + (i % 3) * K1_SLOT;
    bf16x8 a[2][4], b[2][3];
#pragma unroll
    for (int ks = 0; ks < 2; ++ks) {
      if (i + 2 < 8) issue1(i + 2, 3 * ks);
#pragma unroll
      for (int n = 0; n < 2; ++n)
        b[ks][n] = cat8(trread(slot + boff[n] + ks * 4096), trread(slot + boff[n] + ks * 4096 + 512));
      if (i + 2 < 8) issue1(i + 2, 3 * ks + 1);
      b[ks][2] = cat8(trread(slot + boff[2] + ks * 4096), trread(slot + boff[2] + ks * 4096 + 512));
      a[ks][0] = *(const bf16x8*)(slot + aoff[ks]);
      if (i + 2 < 8) issue1(i + 2, 3 * ks + 2);
#pragma unroll
      for (int m = 1; m < 4; ++m) a[ks][m] = *(const bf16x8*)(slot + aoff[ks] + m * 2048);
    }
    WAITL0();
    SEG(tw);
    if (i < 6) { WAITS(); } else { WAITV(0); }
    SEG(ti);
    __builtin_amdgcn_sched_barrier(0);
    BAR();
    __builtin_amdgcn_sched_barrier(0);
    SEG(tb);
    __builtin_amdgcn_s_setprio(1);
#pragma unroll
    for (int ks = 0; ks < 2; ++ks) {
#pragma unroll
      for (int m = 0; m < 4; ++m)
#pragma unroll
        for (int n = 0; n < 3; ++n) acc[m][n] = mfma32(b[ks][n], a[ks][m], acc[m][n]);
      if (((2 * i + ks) & 3) == wn) {
#pragma unroll
        for (int m = 0; m < 4; ++m) {
          u32x4 v = __builtin_bit_cast(u32x4, a[ks][m]);
#pragma unroll
          for (int e = 0; e < 4; ++e) { xs[m] = dot2(v[e], ONES, xs[m]); xq[m] = dot2(v[e], v[e], xq[m]); }
        }
      }
      if (ks == wm) {
#pragma unroll
        for (int n = 0; n < 3; ++n) {
          u32x4 v = __builtin_bit_cast(u32x4, b[ks][n]);
#pragma unroll
          for (int e = 0; e < 4; ++e) cs[n] = dot2(v[e], ONES, cs[n]);
        }
      }
    }
    __builtin_amdgcn_s_setprio(0);
    SEG(tc);
    __builtin_amdgcn_sched_barrier(0);
    BAR();
    __builtin_amdgcn_sched_barrier(0);
    SEG(tb);
  }
  if (wm == 0) BAR();
  STAMP(0,2);
#pragma unroll
  for (int m = 0; m < 4; ++m) {
    float a = redg_add(xs[m]), b = redg_add(xq[m]);
    if (g == 0) *(f32x2*)(smem + K1_XST + (wn * 128 + wm * 64 + m * 16 + r16) * 8) = f32x2{a, b};
  }
#pragma unroll
  for (int n = 0; n < 3; ++n) {
    float a = redg_add(cs[n]);
    if (g == 0) *(float*)(smem + K1_CS + (wm * 192 + (wn * 3 + n) * 16 + r16) * 4) = a;
  }
  __syncthreads();
  float rs[4], rm[4];
#pragma unroll
  for (int m = 0; m < 4; ++m) {
    float s = 0.f, q = 0.f;
#pragma unroll
    for (int j = 0; j < 4; ++j) {
      f32x2 v = *(const f32x2*)(smem + K1_XST + (j * 128 + wm * 64 + m * 16 + r16) * 8);
      s += v[0]; q += v[1];
    }
    float mean = s * (1.0f / 512.0f);
    float var = fmaxf(q * (1.0f / 512.0f) - mean * mean, 0.f);
    rs[m] = __builtin_amdgcn_rsqf(var + 1e-5f);
    rm[m] = mean * rs[m];
  }
#pragma unroll
  for (int n = 0; n < 3; ++n) {
    f32x4 c0 = *(const f32x4*)(smem + K1_CS + ((wn * 3 + n) * 16 + 4 * g) * 4);
    f32x4 c1 = *(const f32x4*)(smem + K1_CS + (192 + (wn * 3 + n) * 16 + 4 * g) * 4);
    f32x4 c = c0 + c1;
#pragma unroll
    for (int m = 0; m < 4; ++m)
#pragma unroll
      for (int r = 0; r < 4; ++r) acc[m][n][r] = acc[m][n][r] * rs[m] - rm[m] * c[r];
  }
  STAMP(0,3);
  char* scr = smem + w * 8192;
  const int tq = r16 >> 2, tp = r16 & 3;
  if (isqkv) {
    const int h = cg * 4 + wn;
    s16x4 wph;
#pragma unroll
    for (int j = 0; j < 4; ++j) wph[j] = (short)wphi[h * 256 + (4 * g + j) * 16 + r16];
    const f32x4 zero4 = {0.f, 0.f, 0.f, 0.f};
#pragma unroll
    for (int m = 0; m < 4; ++m) {
      u32x2 qf = {pk2(acc[m][0][0], acc[m][0][1]), pk2(acc[m][0][2], acc[m][0][3])};
      u32x2 kf = {pk2(acc[m][1][0], acc[m][1][1]), pk2(acc[m][1][2], acc[m][1][3])};
      u32x2 vf = {pk2(acc[m][2][0], acc[m][2][1]), pk2(acc[m][2][2], acc[m][2][3])};
      u32x4 pq = hedgehog(mfma16(wph, __builtin_bit_cast(s16x4, qf), zero4));
      u32x4 pk = hedgehog(mfma16(wph, __builtin_bit_cast(s16x4, kf), zero4));
      *(u32x4*)(ws + WS_PQF + ((size_t)h * 128 + rt * 8 + wm * 4 + m) * 1024 + lane * 16) = pq;
      *(u32x4*)(scr + (16 * m + r16) * 64 + g * 16) = pk;
      *(u32x2*)(scr + 4096 + (16 * m + r16) * 32 + g * 8) = vf;
    }
    f32x4 kvp = zero4, kvn = zero4, zp = zero4, zn = zero4;
    u32x4 o4 = {ONES, ONES, ONES, ONES};
    bf16x8 ones8 = __builtin_bit_cast(bf16x8, o4);
#pragma unroll
    for (int mp = 0; mp < 2; ++mp) {
      int row = 32 * mp + 8 * g + tq;
      bf16x8 ap = cat8(trread(scr + row * 64 + tp * 16), trread(scr + (row + 4) * 64 + tp * 16));
      bf16x8 an = cat8(trread(scr + row * 64 + tp * 16 + 8), trread(scr + (row + 4) * 64 + tp * 16 + 8));
      bf16x8 bv = cat8(trread(scr + 4096 + row * 32 + tp * 8), trread(scr + 4096 + (row + 4) * 32 + tp * 8));
      kvp = mfma32(ap, bv, kvp);
      kvn = mfma32(an, bv, kvn);
      zp = mfma32(ap, ones8, zp);
      zn = mfma32(an, ones8, zn);
    }
    char* red = smem + 65536 + wn * 4096;
    if (wm == 1) {
      *(f32x4*)(red + lane * 16) = kvp;
      *(f32x4*)(red + 1024 + lane * 16) = kvn;
      *(f32x4*)(red + 2048 + lane * 16) = zp;
      *(f32x4*)(red + 3072 + lane * 16) = zn;
    }
    __syncthreads();
    if (wm == 0) {
      kvp += *(const f32x4*)(red + lane * 16);
      kvn += *(const f32x4*)(red + 1024 + lane * 16);
      zp += *(const f32x4*)(red + 2048 + lane * 16);
      zn += *(const f32x4*)(red + 3072 + lane * 16);
      char* dst = ws + WS_KVP + ((size_t)h * 16 + rt) * 2048 + lane * 32;
      *(f32x4*)dst = kvp;
      *(f32x4*)(dst + 16) = kvn;
      if (r16 == 0) {
        char* dz = ws + WS_KVZ + ((size_t)h * 16 + rt) * 128 + g * 32;
        *(f32x4*)dz = zp;
        *(f32x4*)(dz + 16) = zn;
      }
    }
  } else {
    u32x2 zb[4][3];
#pragma unroll
    for (int m = 0; m < 4; ++m)
#pragma unroll
      for (int n = 0; n < 3; ++n) {
        zb[m][n][0] = pk2(gelu_tanh(acc[m][n][0]), gelu_tanh(acc[m][n][1]));
        zb[m][n][1] = pk2(gelu_tanh(acc[m][n][2]), gelu_tanh(acc[m][n][3]));
      }
    if (cg < 12) {
#pragma unroll
      for (int n = 0; n < 3; ++n)
        if (3 * wn + n < nft) {
#pragma unroll
          for (int m = 0; m < 4; ++m)
            *(u32x2*)(ws + WS_Z1F + ((size_t)(rt * 8 + wm * 4 + m) * 43 + ft0 + 3 * wn + n) * 512 + lane * 8) = zb[m][n];
        }
    } else {
#pragma unroll
      for (int m = 0; m < 4; ++m) {
        float s = 0.f, q = 0.f;
#pragma unroll
        for (int n = 0; n < 3; ++n)
          if (3 * wn + n < nft) {
            s = dot2(zb[m][n][0], ONES, s); s = dot2(zb[m][n][1], ONES, s);
            q = dot2(zb[m][n][0], zb[m][n][0], q); q = dot2(zb[m][n][1], zb[m][n][1], q);
          }
        s = redg_add(s); q = redg_add(q);
        if (g == 0) *(f32x2*)(smem + K1_ZS + (wn * 128 + wm * 64 + m * 16 + r16) * 8) = f32x2{s, q};
      }
      __syncthreads();
      if (tid < 128) {
        float s = 0.f, q = 0.f;
#pragma unroll
        for (int j = 0; j < 4; ++j) {
          f32x2 v = *(const f32x2*)(smem + K1_ZS + (j * 128 + tid) * 8);
          s += v[0]; q += v[1];
        }
        u32x4 gr = {__builtin_bit_cast(unsigned, s), tag, __builtin_bit_cast(unsigned, q), tag};
        __builtin_amdgcn_raw_buffer_store_b128(gr, wsr, (int)(WS_ZST + ((rt * 4 + zg) * 128 + tid) * 16), 0, 16);
      }
      STAMP(0,4);
      {
        int off = (int)(WS_ZST + (rt * 4 * 128 + tid) * 16);
        u32x4 gr;
        for (int spin = 0; spin < (1 << 20); ++spin) {
          gr = poll128(wsd, off);
          bool ok = (gr[1] == tag) && (gr[3] == tag);
          if (__all(ok)) break;
          __builtin_amdgcn_s_sleep(1);
        }
        *(f32x2*)(smem + K1_XST + tid * 8) = f32x2{asf(gr[0]), asf(gr[2])};
      }
      __syncthreads();
      STAMP(0,5);
      if (zg == 0 && tid == 0) __builtin_amdgcn_raw_buffer_store_b32(tag, wsr, (int)(WS_EP + rt * 128), 0, 16);
#pragma unroll
      for (int m = 0; m < 4; ++m) {
        float s = 0.f, q = 0.f;
#pragma unroll
        for (int j = 0; j < 4; ++j) {
          f32x2 v = *(const f32x2*)(smem + K1_XST + (j * 128 + wm * 64 + m * 16 + r16) * 8);
          s += v[0]; q += v[1];
        }
        float mean = s * (1.0f / 688.0f);
        float var = fmaxf(q * (1.0f / 688.0f) - mean * mean, 0.f);
        float r = __builtin_amdgcn_rsqf(var + 1e-5f);
        float c = -mean * r;
#pragma unroll
        for (int n = 0; n < 3; ++n) {
          u32x2 o;
          o[0] = pk2(fmaf(bflo(zb[m][n][0]), r, c), fmaf(bfhi(zb[m][n][0]), r, c));
          o[1] = pk2(fmaf(bflo(zb[m][n][1]), r, c), fmaf(bfhi(zb[m][n][1]), r, c));
          *(u32x2*)(scr + n * 2048 + (16 * m + r16) * 32 + g * 8) = o;
        }
      }
#pragma unroll
      for (int n = 0; n < 3; ++n)
#pragma unroll
        for (int kk = 0; kk < 2; ++kk) {
          int row = 32 * kk + 8 * g + tq;
          bf16x8 f = cat8(trread(scr + n * 2048 + row * 32 + tp * 8), trread(scr + n * 2048 + (row + 4) * 32 + tp * 8));
          if (3 * wn + n < nft)
            *(bf16x8*)(ws + WS_Z2F + (((size_t)rt * 44 + ft0 + 3 * wn + n) * 4 + 2 * wm + kk) * 1024 + lane * 16) = f;
        }
    }
  }
  STAMP(0,6);
}

constexpr int K2_SLOT = 32768;
constexpr int K2_NSLOT = 4;
constexpr int K2_MISC = K2_NSLOT * K2_SLOT;
constexpr int K2_LDS = 163840;

__global__ __launch_bounds__(512, 2) void k2(const char* __restrict__ wsp, const unsigned short* __restrict__ bsp,
                                             char* ws) {
  extern __shared__ __attribute__((aligned(16))) char smem[];
  const int tid = threadIdx.x, lane = tid & 63;
  const int w = __builtin_amdgcn_readfirstlane(tid >> 6);
  const int th = w & 1, kq = w >> 1;
  const int r16 = lane & 15, g = lane >> 4;
  const int xcd = blockIdx.x & 7, jj = blockIdx.x >> 3;
  const int rt = xcd * 4 + (jj >> 3);
  const int cgp = jj & 7;
  const int ft0 = cgp < 7 ? cgp * 6 : 40;
  const int npair = cgp < 6 ? 3 : 2;
  const int rot = 0;

  const u32x4 ad = mkdesc(wsp, 2048u * 4096u);
  const u32x4 wsd = mkdesc(ws, (unsigned)WS_END);
  int asrc;
  {
    int row = 8 * w + (lane >> 3);
    int c = (lane & 7) ^ ((row >> 1) & 7);
    asrc = (rt * 64 + row) * 4096 + c * 16;
  }
  const int bsrc = (int)WS_Z2F + ft0 * 4096 + w * 1024 + lane * 16;
  auto issue = [&](int i) {
    int slot = (i % K2_NSLOT) * K2_SLOT;
    if (i < 8) {
      int c = (i + rot) & 7;
#pragma unroll
      for (int u = 0; u < 4; ++u) dma16(ad, asrc, c * 512 + u * 128, slot + u * 8192 + w * 1024);
    } else {
      int st = (i - 8 + 2 * rot) & 15;
#pragma unroll
      for (int u = 0; u < 3; ++u) {
        int uu = u < npair ? u : 0;
        dma16(wsd, bsrc, st * (44 * 4096) + uu * 8192, slot + u * 8192 + w * 1024);
      }
    }
  };
  STAMP(1,0);
  const int h = blockIdx.x & 31, rb = blockIdx.x >> 5;
  {
    const char* kp = ws + WS_KVP + ((size_t)h * 16 + 2 * w) * 2048 + lane * 32;
    f32x4 a0 = *(const f32x4*)kp, a1 = *(const f32x4*)(kp + 16);
    f32x4 b0 = *(const f32x4*)(kp + 2048), b1 = *(const f32x4*)(kp + 2048 + 16);
    const char* zp = ws + WS_KVZ + ((size_t)h * 16 + 2 * w) * 128 + g * 32;
    f32x4 c0 = *(const f32x4*)zp, c1 = *(const f32x4*)(zp + 16);
    f32x4 d0 = *(const f32x4*)(zp + 128), d1 = *(const f32x4*)(zp + 128 + 16);
    bf16x8 pq[2];
#pragma unroll
    for (int t = 0; t < 2; ++t)
      pq[t] = *(const bf16x8*)(ws + WS_PQF + ((size_t)h * 128 + rb * 16 + 2 * w + t) * 1024 + lane * 16);
    char* red = smem + w * 4096;
    *(f32x4*)(red + lane * 16) = a0 + b0;
    *(f32x4*)(red + 1024 + lane * 16) = a1 + b1;
    *(f32x4*)(red + 2048 + lane * 16) = c0 + d0;
    *(f32x4*)(red + 3072 + lane * 16) = c1 + d1;
    __syncthreads();
    f32x4 kp4 = {0, 0, 0, 0}, kn4 = kp4, zp4 = kp4, zn4 = kp4;
#pragma unroll
    for (int j = 0; j < 8; ++j) {
      kp4 += *(const f32x4*)(smem + j * 4096 + lane * 16);
      kn4 += *(const f32x4*)(smem + j * 4096 + 1024 + lane * 16);
      zp4 += *(const f32x4*)(smem + j * 4096 + 2048 + lane * 16);
      zn4 += *(const f32x4*)(smem + j * 4096 + 3072 + lane * 16);
    }
    u32x4 kvo = {pk2(kp4[0], kp4[1]), pk2(kp4[2], kp4[3]), pk2(kn4[0], kn4[1]), pk2(kn4[2], kn4[3])};
    u32x4 zo = {pk2(zp4[0], zp4[1]), pk2(zp4[2], zp4[3]), pk2(zn4[0], zn4[1]), pk2(zn4[2], zn4[3])};
    const f32x4 zero4 = {0.f, 0.f, 0.f, 0.f};
#pragma unroll
    for (int t = 0; t < 2; ++t) {
      f32x4 num = mfma32(__builtin_bit_cast(bf16x8, kvo), pq[t], zero4);
      f32x4 den = mfma32(__builtin_bit_cast(bf16x8, zo), pq[t], zero4);
      float inv = 1.0f / (den[0] + 1e-6f);
      u32x2 o = {pk2(num[0] * inv, num[1] * inv), pk2(num[2] * inv, num[3] * inv)};
      *(u32x2*)(ws + WS_A3F + ((size_t)(rb * 16 + 2 * w + t) * 38 + (h >> 1)) * 1024 + lane * 16 + (h & 1) * 8) = o;
    }
    WAITV(0);
    __syncthreads();
  }
  STAMP(1,1);
  issue(0); issue(1); issue(2);

  bf16x8 af[8][2][2];
  int aoff;
  {
    int row = th * 32 + r16;
    int chunk = (kq & 1) * 4 + g;
    aoff = (kq >> 1) * 8192 + row * 128 + ((chunk ^ ((r16 >> 1) & 7)) << 4);
  }
#pragma unroll
  for (int i = 0; i < 8; ++i) {
    if (i < 6) { WAITV(8); } else if (i == 6) { WAITV(7); } else { WAITV(6); }
    BAR();
    issue(i + 3);
    const char* slot = smem + (i % K2_NSLOT) * K2_SLOT;
#pragma unroll
    for (int hh = 0; hh < 2; ++hh)
#pragma unroll
      for (int t = 0; t < 2; ++t) af[i][hh][t] = *(const bf16x8*)(slot + aoff + hh * 16384 + t * 2048);
    WAITL0();
  }
  STAMP(1,2);
  f32x4 acc[6][2];
#pragma unroll
  for (int n = 0; n < 6; ++n)
#pragma unroll
    for (int t = 0; t < 2; ++t) acc[n][t] = f32x4{0.f, 0.f, 0.f, 0.f};
#pragma unroll
  for (int i = 8; i < 24; ++i) {
    if (i < 22) { WAITV(6); } else if (i == 22) { WAITV(3); } else { WAITV(0); }
    BAR();
    if (i + 3 < 24) issue(i + 3);
    const char* slot = smem + (i % K2_NSLOT) * K2_SLOT;
    const int c = (i - 8) >> 1, hh = (i - 8) & 1;
    bf16x8 b[6];
#pragma unroll
    for (int n = 0; n < 6; ++n) b[n] = *(const bf16x8*)(slot + n * 4096 + kq * 1024 + lane * 16);
#pragma unroll
    for (int n = 0; n < 6; ++n)
#pragma unroll
      for (int t = 0; t < 2; ++t) acc[n][t] = mfma32(b[n], af[c][hh][t], acc[n][t]);
    WAITL0();
  }
  __syncthreads();
  STAMP(1,3);
#pragma unroll
  for (int n = 0; n < 6; ++n)
#pragma unroll
    for (int t = 0; t < 2; ++t) *(f32x4*)(smem + ((th * 4 + kq) * 12 + n * 2 + t) * 1024 + lane * 16) = acc[n][t];
  __syncthreads();
#pragma unroll
  for (int e = 0; e < 2; ++e) {
    int un = kq + 4 * e;
    if (un < 2 * npair) {
      int t = un & 1, p = un >> 1;
      int tt = rt * 4 + th * 2 + t;
      float bias = __builtin_bit_cast(float, (unsigned)bsp[tt * 16 + r16] << 16);
      u32x4 o;
#pragma unroll
      for (int hf = 0; hf < 2; ++hf) {
        int n = 2 * p + hf;
        f32x4 s = {0.f, 0.f, 0.f, 0.f};
#pragma unroll
        for (int j = 0; j < 4; ++j) s += *(const f32x4*)(smem + ((th * 4 + j) * 12 + n * 2 + t) * 1024 + lane * 16);
        int ft = ft0 + n;
        if (ft < 43) {
          u32x2 z1 = *(const u32x2*)(ws + WS_Z1F + ((size_t)tt * 43 + ft) * 512 + lane * 8);
          o[2 * hf] = pk2(bflo(z1[0]) * (s[0] + bias), bfhi(z1[0]) * (s[1] + bias));
          o[2 * hf + 1] = pk2(bflo(z1[1]) * (s[2] + bias), bfhi(z1[1]) * (s[3] + bias));
        } else {
          o[2 * hf] = 0; o[2 * hf + 1] = 0;
        }
      }
      *(u32x4*)(ws + WS_A3F + ((size_t)tt * 38 + 16 + (ft0 >> 1) + p) * 1024 + lane * 16) = o;
    }
  }
  STAMP(1,4);
}

constexpr int K3_LDS = 1216 * 128;

__global__ __launch_bounds__(512, 2) void k3(const char* __restrict__ x, const char* __restrict__ wo,
                                             const char* __restrict__ wout, const char* ws,
                                             char* __restrict__ out) {
  extern __shared__ __attribute__((aligned(16))) char smem[];
  const int tid = threadIdx.x, lane = tid & 63;
  const int w = __builtin_amdgcn_readfirstlane(tid >> 6);
  const int mh = w & 1, kq = w >> 1;
  const int r16 = lane & 15, g = lane >> 4;
  const int xcd = blockIdx.x & 7, jj = blockIdx.x >> 3;
  const int rt = xcd * 4 + (jj >> 3);
  const int nc = jj & 7;
  STAMP(2,0);
  bf16x8 af[10][2];
#pragma unroll
  for (int i = 0; i < 10; ++i) {
    int ks = min(4 * i + kq, 37);
#pragma unroll
    for (int t = 0; t < 2; ++t)
      af[i][t] = *(const bf16x8*)(ws + WS_A3F + ((size_t)(rt * 4 + mh * 2 + t) * 38 + ks) * 1024 + lane * 16);
  }
  {
    const u32x4 od = mkdesc(wo, 512u * 1024u), ud = mkdesc(wout, 688u * 1024u);
    int rr = lane >> 3;
    int h2 = ((rr >> 1) & 1) | (((rr >> 2) & 1) << 1);
    int voff = rr * 1024 + nc * 128 + (((lane & 7) ^ (h2 << 1)) << 4);
#pragma unroll
    for (int i = 0; i < 19; ++i) {
      if (i < 8) dma16(od, voff, (i * 8 + w) * 8192, (i * 8 + w) * 1024);
      else dma16(ud, voff, ((i - 8) * 8 + w) * 8192, (i * 8 + w) * 1024);
    }
  }
  f32x4 acc[4][2];
#pragma unroll
  for (int n = 0; n < 4; ++n)
#pragma unroll
    for (int t = 0; t < 2; ++t) acc[n][t] = f32x4{0.f, 0.f, 0.f, 0.f};
  int boff[4];
  {
    int q = r16 >> 2, p = r16 & 3;
    int k = 4 * g + q;
    int h2 = ((k >> 1) & 1) | (((k >> 2) & 1) << 1);
#pragma unroll
    for (int n = 0; n < 4; ++n) {
      int bc = n * 32 + 8 * p;
      boff[n] = k * 128 + (((bc >> 4) ^ (h2 << 1)) << 4) + (bc & 8) + kq * 4096;
    }
  }
  WAITV(0);
  __syncthreads();
  STAMP(2,1);
#pragma unroll
  for (int i = 0; i < 10; ++i) {
    if (4 * i + kq < 38) {
      bf16x8 b[4];
#pragma unroll
      for (int n = 0; n < 4; ++n)
        b[n] = cat8(trread(smem + boff[n] + i * 16384), trread(smem + boff[n] + i * 16384 + 2048));
#pragma unroll
      for (int n = 0; n < 4; ++n)
#pragma unroll
        for (int t = 0; t < 2; ++t) acc[n][t] = mfma32(b[n], af[i][t], acc[n][t]);
    }
  }
  __syncthreads();
  STAMP(2,2);
#pragma unroll
  for (int n = 0; n < 4; ++n)
#pragma unroll
    for (int t = 0; t < 2; ++t)
      *(f32x4*)(smem + kq * 17408 + (mh * 32 + t * 16 + r16) * 272 + (n * 16 + 4 * g) * 4) = acc[n][t];
  __syncthreads();
  {
    int row = tid >> 3, c8 = tid & 7;
    f32x4 s0 = {0.f, 0.f, 0.f, 0.f}, s1 = s0;
#pragma unroll
    for (int j = 0; j < 4; ++j) {
      s0 += *(const f32x4*)(smem + j * 17408 + row * 272 + c8 * 32);
      s1 += *(const f32x4*)(smem + j * 17408 + row * 272 + c8 * 32 + 16);
    }
    size_t go = (size_t)(rt * 64 + row) * 1024 + nc * 128 + c8 * 16;
    u32x4 xv = *(const u32x4*)(x + go);
    u32x4 o;
    o[0] = pk2(s0[0] + bflo(xv[0]), s0[1] + bfhi(xv[0]));
    o[1] = pk2(s0[2] + bflo(xv[1]), s0[3] + bfhi(xv[1]));
    o[2] = pk2(s1[0] + bflo(xv[2]), s1[1] + bfhi(xv[2]));
    o[3] = pk2(s1[2] + bflo(xv[3]), s1[3] + bfhi(xv[3]));
    *(u32x4*)(out + go) = o;
  }
  STAMP(2,3);
}

extern "C" void kernel_launch(void* const* d_in, const int* in_sizes, int n_in, void* d_out, int out_size, void* d_ws,
                              size_t ws_size, hipStream_t stream) {
  const char* x = (const char*)d_in[0];
  const char* wqkv = (const char*)d_in[1];
  const unsigned short* wphi = (const unsigned short*)d_in[2];
  const char* wo = (const char*)d_in[3];
  const char* win = (const char*)d_in[5];
  const char* wsp = (const char*)d_in[6];
  const unsigned short* bsp = (const unsigned short*)d_in[7];
  const char* wout = (const char*)d_in[8];
  char* ws = (char*)d_ws;
  hipFuncSetAttribute((const void*)k1, hipFuncAttributeMaxDynamicSharedMemorySize, K1_LDS);
  hipFuncSetAttribute((const void*)k2, hipFuncAttributeMaxDynamicSharedMemorySize, K2_LDS);
  hipFuncSetAttribute((const void*)k3, hipFuncAttributeMaxDynamicSharedMemorySize, K3_LDS);
  k1<<<256, 512, K1_LDS, stream>>>(x, wqkv, wphi, win, ws);
  k2<<<256, 512, K2_LDS, stream>>>(wsp, bsp, ws);
  k3<<<256, 512, K3_LDS, stream>>>(x, wo, wout, ws, (char*)d_out);
}
```

```cpp
#include <hip/hip_runtime.h>
#include <hip/hip_bf16.h>
#include <stdint.h>

typedef __bf16 bf16x8 __attribute__((ext_vector_type(8)));
typedef __bf16 bf16x2 __attribute__((ext_vector_type(2)));
typedef short s16x4 __attribute__((ext_vector_type(4)));
typedef short s16x8 __attribute__((ext_vector_type(8)));
typedef float f32x4 __attribute__((ext_vector_type(4)));
typedef float f32x2 __attribute__((ext_vector_type(2)));
typedef unsigned u32x4 __attribute__((ext_vector_type(4)));
typedef unsigned u32x2 __attribute__((ext_vector_type(2)));
#define LAS __attribute__((address_space(3)))
#define DI __device__ __forceinline__

constexpr int WS_EP  = 0;
constexpr int WS_FLZ = 1024;
constexpr int WS_FLO = WS_FLZ + 8192;
constexpr int WS_FLU = WS_FLO + 8192;
constexpr int WS_RST = WS_FLU + 16384;
constexpr int WS_ZST = WS_RST + 2048 * 16;
constexpr int KVP_SZ = 1088 * 16;
constexpr int WS_KVP = WS_ZST + 16 * 8 * 128 * 16;
constexpr int WS_KVR = WS_KVP + 8 * 16 * KVP_SZ;
constexpr int WS_Z2F = WS_KVR + 8 * 1088 * 8;
constexpr int WS_Z1F = WS_Z2F + 16 * 44 * 4096;
constexpr int WS_A3F = WS_Z1F + 128 * 44 * 512;
constexpr int WS_END = WS_A3F + 128 * 38 * 1024;
static_assert(WS_Z2F % 128 == 0 && WS_KVP % 128 == 0 && WS_KVR % 128 == 0, "alignment");

template <int V> struct IC { static constexpr int value = V; constexpr operator int() const { return V; } };
template <int I, int N, class F> DI void sfor_(F&& f) { if constexpr (I < N) { f(IC<I>{}); sfor_<I + 1, N>(f); } }
template <int N, class F> DI void sfor(F&& f) { sfor_<0, N>(f); }
template <int N> DI void waitv() { asm volatile("s_waitcnt vmcnt(%0)" :: "n"(N) : "memory"); }

DI float asf(unsigned u) { return __builtin_bit_cast(float, u); }
DI unsigned asu(float f) { return __builtin_bit_cast(unsigned, f); }
DI u32x4 mkdesc(const void* p, unsigned bytes) {
  unsigned long long a = (unsigned long long)p;
  u32x4 d = {(unsigned)a, (unsigned)(a >> 32) & 0xffffu, bytes, 0x00020000u};
  return d;
}
DI void dma16(u32x4 desc, int voff, int soff, int lds) {
  asm volatile("s_mov_b32 m0, %3\n\ts_nop 0\n\tbuffer_load_dwordx4 %0, %1, %2 offen lds"
               :: "v"(voff), "s"(desc), "s"(soff), "s"(lds) : "memory");
}
DI void dma16d(u32x4 desc, int voff, int soff, int lds) {
  asm volatile("s_mov_b32 m0, %3\n\ts_nop 0\n\tbuffer_load_dwordx4 %0, %1, %2 offen sc1 lds"
               :: "v"(voff), "s"(desc), "s"(soff), "s"(lds) : "memory");
}
DI u32x2 poll64(u32x4 desc, int voff) {
  u32x2 r;
  asm volatile("buffer_load_dwordx2 %0, %1, %2, 0 offen sc1\n\ts_waitcnt vmcnt(0)" : "=&v"(r) : "v"(voff), "s"(desc) : "memory");
  return r;
}
DI void ld128d(u32x4& r, u32x4 desc, int voff, int soff) {
  asm volatile("buffer_load_dwordx4 %0, %1, %2, %3 offen sc1" : "=v"(r) : "v"(voff), "s"(desc), "s"(soff) : "memory");
}
DI void ld64d(u32x2& r, u32x4 desc, int voff) {
  asm volatile("buffer_load_dwordx2 %0, %1, %2, 0 offen sc1" : "=v"(r) : "v"(voff), "s"(desc) : "memory");
}
DI s16x4 trread(const char* p) { return __builtin_amdgcn_ds_read_tr16_b64_v4i16((LAS s16x4*)p); }
DI bf16x8 cat8(s16x4 a, s16x4 b) {
  s16x8 r = __builtin_shufflevector(a, b, 0, 1, 2, 3, 4, 5, 6, 7);
  return __builtin_bit_cast(bf16x8, r);
}
DI void keep(bf16x8 v) { asm volatile("" :: "v"(v)); }
DI bf16x8 asb8(u32x4 v) { return __builtin_bit_cast(bf16x8, v); }
DI u32x4 asu4(f32x4 v) { return __builtin_bit_cast(u32x4, v); }
DI f32x4 asf4(u32x4 v) { return __builtin_bit_cast(f32x4, v); }
DI f32x4 mfma32(bf16x8 a, bf16x8 b, f32x4 c) { return __builtin_amdgcn_mfma_f32_16x16x32_bf16(a, b, c, 0, 0, 0); }
DI f32x4 mfma16(u32x2 a, u32x2 b, f32x4 c) {
  return __builtin_amdgcn_mfma_f32_16x16x16bf16_1k(__builtin_bit_cast(s16x4, a), __builtin_bit_cast(s16x4, b), c, 0, 0, 0);
}
template <int CTRL> DI float dppadd(float v) { return v + asf(__builtin_amdgcn_update_dpp(0u, asu(v), CTRL, 0xf, 0xf, true)); }
DI unsigned pk2(float a, float b) {
  f32x2 f = {a, b};
  return __builtin_bit_cast(unsigned, __builtin_convertvector(f, bf16x2));
}
DI u32x2 pk4(f32x4 v) { return u32x2{pk2(v[0], v[1]), pk2(v[2], v[3])}; }
DI float bflo(unsigned u) { return asf(u << 16); }
DI float bfhi(unsigned u) { return asf(u & 0xffff0000u); }
DI float dot2(unsigned a, unsigned b, float c) {
  return __builtin_amdgcn_fdot2_f32_bf16(__builtin_bit_cast(bf16x2, a), __builtin_bit_cast(bf16x2, b), c, false);
}
template <bool MAX> DI void redg2(float& a, float& b) {
  auto r = __builtin_amdgcn_permlane16_swap(asu(a), asu(b), false, false);
  float m = MAX ? fmaxf(asf(r[0]), asf(r[1])) : asf(r[0]) + asf(r[1]);
  auto r2 = __builtin_amdgcn_permlane32_swap(asu(m), asu(m), false, false);
  m = MAX ? fmaxf(asf(r2[0]), asf(r2[1])) : asf(r2[0]) + asf(r2[1]);
  auto r3 = __builtin_amdgcn_permlane16_swap(asu(m), asu(m), false, false);
  a = asf(r3[0]); b = asf(r3[1]);
}
constexpr float L2E = 1.4426950408889634f;
DI u32x4 hedgehog(f32x4 p) {
  float mx = fmaxf(fmaxf(p[0], p[1]), fmaxf(p[2], p[3]));
  float mn = -fminf(fminf(p[0], p[1]), fminf(p[2], p[3]));
  redg2<true>(mx, mn);
  const float a = -mx * L2E, b = -mn * L2E;
  float ep[4], en[4];
#pragma unroll
  for (int r = 0; r < 4; ++r) {
    ep[r] = __builtin_amdgcn_exp2f(fmaf(p[r], L2E, a));
    en[r] = __builtin_amdgcn_exp2f(fmaf(p[r], -L2E, b));
  }
  float sp = (ep[0] + ep[1]) + (ep[2] + ep[3]);
  float sn = (en[0] + en[1]) + (en[2] + en[3]);
  redg2<false>(sp, sn);
  float ip = __builtin_amdgcn_rcpf(sp), in = __builtin_amdgcn_rcpf(sn);
  return u32x4{pk2(ep[0] * ip, ep[1] * ip), pk2(ep[2] * ip, ep[3] * ip), pk2(en[0] * in, en[1] * in), pk2(en[2] * in, en[3] * in)};
}
DI float gelu_tanh(float x) {
  const float c1 = -2.0f * 0.7978845608028654f * L2E;
  const float c2 = c1 * 0.044715f;
  float t = x * fmaf(x * x, c2, c1);
  return x * __builtin_amdgcn_rcpf(1.0f + __builtin_amdgcn_exp2f(t));
}
#define STAMP(i)
#define WAITV(n) asm volatile("s_waitcnt vmcnt(" #n ")" ::: "memory")
#define WAITL0() asm volatile("s_waitcnt lgkmcnt(0)" ::: "memory")
#define SB() __builtin_amdgcn_sched_barrier(0)
#define BAR() do { SB(); __builtin_amdgcn_s_barrier(); SB(); } while (0)
#define LBAR() do { WAITL0(); BAR(); } while (0)
constexpr int SPIN_CAP = 1 << 18;
constexpr unsigned TAGX = 0x5bd1e995u;

constexpr int L_SLOT = 40960;
constexpr int L_SCR = 114688;
constexpr int L_MISC = 32768;
constexpr int L_MISC2 = 73728;
constexpr int L_RING = 24576;
constexpr int L_WO = 98304;
constexpr int LDS_BYTES = 163840;

__global__ __launch_bounds__(512, 2) void fused(const char* __restrict__ x, const char* __restrict__ wqkv,
                                                const char* __restrict__ wphi, const char* __restrict__ wo,
                                                const char* __restrict__ win, const char* __restrict__ wsp,
                                                const char* __restrict__ bsp, const char* __restrict__ wout,
                                                char* out, char* ws) {
  extern __shared__ __attribute__((aligned(16))) char smem[];
  const int tid = threadIdx.x, lane = tid & 63;
  const int w = __builtin_amdgcn_readfirstlane(tid >> 6);
  const int r16 = lane & 15, g = lane >> 4;
  const int tq = r16 >> 2, tp = r16 & 3;
  const int bid = blockIdx.x, xcd = bid & 7, jj = bid >> 3;
  const int rt16 = (xcd >> 2) * 8 + (jj >> 2);
  const bool isqkv = (jj & 2) == 0;
  const int grp = (xcd & 3) * 2 + (jj & 1);
  const int zft0 = grp < 7 ? grp * 6 : 40;
  const int znft = grp < 6 ? 6 : (grp == 6 ? 4 : 3);
  const int wm = w >> 1, wn = w & 1;
  const int rt64 = xcd * 4 + (jj >> 3);
  const int cgp = jj & 7;
  const int ft0 = cgp < 7 ? cgp * 6 : 40;
  const int npair = cgp < 6 ? 3 : 2;
  const int th = w & 1, kq = w >> 1;

#define ROT1 0
#define ROT2 0
  const int rot1 = ROT1, rot2 = ROT2;
  const __amdgpu_buffer_rsrc_t wsr = __builtin_amdgcn_make_buffer_rsrc(ws, 0, WS_END, 0x00020000);
  const u32x4 wsd = mkdesc(ws, WS_END);
  const u32x4 xd = mkdesc(x, 2048u * 1024u);
  const u32x4 wd = isqkv ? mkdesc(wqkv, 512u * 3072u) : mkdesc(win, 512u * 2752u);
  const u32x4 ad = mkdesc(wsp, 2048u * 4096u);
  const f32x4 zero4 = {0.f, 0.f, 0.f, 0.f};
  const unsigned ONES = 0x3f803f80u;
  const bf16x8 ones8 = asb8(u32x4{ONES, ONES, ONES, ONES});
  STAMP(0);

  int xsrc[2], wsrc[3];
#pragma unroll
  for (int u = 0; u < 2; ++u) {
    int row = 16 * w + 8 * u + (lane >> 3);
    xsrc[u] = (rt16 * 128 + row) * 1024 + (((lane & 7) ^ ((row >> 1) & 7)) << 4);
  }
  const int wstride = isqkv ? 64 * 3072 : 64 * 2752;
#pragma unroll
  for (int u = 0; u < 3; ++u) {
    int ci = (3 * w + u) * 64 + lane;
    int row = ci / 24, c = ci - row * 24;
    int h2 = ((row >> 1) & 1) | (((row >> 3) & 1) << 1);
    int cs = c ^ (h2 << 1);
    int vq = row * 3072 + (cs >> 3) * 1024 + grp * 128 + (cs & 7) * 16;
    int cz = min(cs < 12 ? cs : cs - 12, znft * 2 - 1);
    int vz = row * 2752 + (cs < 12 ? 1376 : 0) + zft0 * 32 + cz * 16;
    wsrc[u] = isqkv ? vq : vz;
  }
  auto dma1 = [&](int st, int u) {
    const int slot = (st & 3) * L_SLOT, sr = (st + rot1) & 7;
    if (u < 2) dma16(xd, xsrc[u], sr * 128, slot + (16 * w + 8 * u) * 128);
    else dma16(wd, wsrc[u - 2], sr * wstride, slot + 16384 + (3 * w + u - 2) * 1024);
  };
#pragma unroll
  for (int u = 0; u < 5; ++u) dma1(0, u);
  unsigned ep;
  u32x4 xrow;
  asm volatile("buffer_load_dword %0, off, %2, 0 sc1\n\tbuffer_load_dwordx4 %1, %3, %4, 0 offen"
               : "=&v"(ep), "=&v"(xrow) : "s"(wsd), "v"((bid * 8 + w) * 1024 + lane * 16), "s"(xd) : "memory");
#pragma unroll
  for (int u = 0; u < 5; ++u) dma1(1, u);

  const int asrc = (rt64 * 64 + th * 32 + (lane >> 3)) * 4096 + kq * 128 + (((lane & 7) ^ ((lane >> 4) & 3)) << 4);
  auto dmaA = [&](int c, int u) {
    dma16(ad, asrc ^ ((u & 1) << 6), ((c + rot2) & 7) * 512 + u * 8 * 4096, (c % 3) * L_SLOT + w * 4096 + u * 1024);
  };
  auto piece = [&](int p) {
    if (p < 30) dma1(2 + p / 5, p % 5);
    else if (p < 42) dmaA((p - 30) >> 2, (p - 30) & 3);
  };

  int aoff[2], boff[6];
#pragma unroll
  for (int ks = 0; ks < 2; ++ks) aoff[ks] = (wm * 32 + r16) * 128 + (((4 * ks + g) ^ ((r16 >> 1) & 7)) << 4);
  {
    int h2 = ((tq >> 1) & 1) | ((g & 1) << 1);
#pragma unroll
    for (int i = 0; i < 6; ++i) {
      int sg = isqkv ? ((i >> 1) * 4 + 2 * wn + (i & 1)) : ((i / 3) * 6 + 3 * wn + (i % 3));
      int chunk = 2 * sg + (tp >> 1);
      boff[i] = 16384 + (8 * g + tq) * 384 + ((chunk ^ (h2 << 1)) << 4) + (tp & 1) * 8;
    }
  }
  f32x4 acc[2][6], csum[6];
#pragma unroll
  for (int i = 0; i < 6; ++i) { acc[0][i] = zero4; acc[1][i] = zero4; csum[i] = zero4; }
  bf16x8 fa[2][2], fb[2][6];
  u32x4 rst[2];
  unsigned tag = 0;
  auto rdA = [&](int st, int ks, int m) { fa[ks][m] = *(const bf16x8*)(smem + (st & 3) * L_SLOT + aoff[ks] + m * 2048); };
  auto rdB = [&](int st, int ks, int n) {
    const char* p = smem + (st & 3) * L_SLOT + boff[n] + ks * 12288;
    fb[ks][n] = cat8(trread(p), trread(p + 1536));
  };
  waitv<7>();
  BAR();
  STAMP(1);
  rdB(0, 0, 0); rdA(0, 0, 0); rdA(0, 0, 1);
#pragma unroll
  for (int n = 1; n < 6; ++n) rdB(0, 0, n);
  sfor<16>([&](auto B) {
    constexpr int b = B, i = b >> 1, ks = b & 1;
    constexpr int nst = (b + 1) >> 1, nks = ks ^ 1;
    constexpr int p0 = b < 12 ? 3 * b : (b == 12 ? 36 : (b == 13 ? 38 : 42));
    constexpr int p1 = b < 12 ? 3 * b + 3 : (b == 12 ? 38 : 42);
    sfor<6>([&](auto N) {
      constexpr int n = N;
      SB();
      acc[0][n] = mfma32(fb[ks][n], fa[ks][0], acc[0][n]);
      acc[1][n] = mfma32(fb[ks][n], fa[ks][1], acc[1][n]);
      SB();
      if constexpr (nst < 8 && n < 3) {
        rdB(nst, nks, 2 * n);
        if constexpr (n < 2) rdA(nst, nks, n);
        rdB(nst, nks, 2 * n + 1);
      }
      if constexpr (n >= 2 && p0 + n - 2 < p1) piece(p0 + n - 2);
    });
    if ((b & 3) == wm) {
#pragma unroll
      for (int n = 0; n < 6; ++n) csum[n] = mfma32(ones8, fb[ks][n], csum[n]);
    }
    if constexpr (ks == 0 && i < 7) {
      WAITL0();
      waitv<(i < 5 ? i + 3 : (i == 5 ? 10 : 8))>();
      BAR();
    }
    if constexpr (b == 0) {
      asm volatile("" : "+v"(ep), "+v"(xrow));
      tag = __builtin_amdgcn_readfirstlane(ep) + 1u;
      float s = 0.f, q = 0.f;
#pragma unroll
      for (int e = 0; e < 4; ++e) { s = dot2(xrow[e], ONES, s); q = dot2(xrow[e], xrow[e], q); }
      redg2<false>(s, q);
      s = dppadd<0x128>(s); q = dppadd<0x128>(q); s = dppadd<0x124>(s); q = dppadd<0x124>(q);
      s = dppadd<0x122>(s); q = dppadd<0x122>(q); s = dppadd<0x121>(s); q = dppadd<0x121>(q);
      float mean = s * (1.0f / 512.0f);
      float var = fmaxf(q * (1.0f / 512.0f) - mean * mean, 0.f);
      float rs = __builtin_amdgcn_rsqf(var + 1e-5f);
      if (lane == 0)
        __builtin_amdgcn_raw_buffer_store_b128(u32x4{asu(rs), tag, asu(mean), tag}, wsr, WS_RST + (bid * 8 + w) * 16, 0, 16);
    }
    if constexpr (b == 9) {
      if (w < 2) dma16(mkdesc(wphi, 32u * 512u), lane * 16, grp * 2048 + w * 1024, L_MISC + w * 1024);
    }
    if constexpr (b == 9) {
      ld128d(rst[0], wsd, WS_RST + (rt16 * 128 + wm * 32 + r16) * 16, 0);
      ld128d(rst[1], wsd, WS_RST + (rt16 * 128 + wm * 32 + r16) * 16, 256);
    }
  });
  STAMP(2);
  if (g == 0) {
#pragma unroll
    for (int n = 0; n < 6; ++n) {
      float c = csum[n][0];
      unsigned hl = pk2(c, 0.f);
      hl = pk2(c, c - bflo(hl));
      *(u32x2*)(smem + L_MISC + 2048 + ((wm * 12 + wn * 6 + n) * 16 + r16) * 8) = u32x2{hl, hl & 0xffffu};
    }
  }
  asm volatile("" : "+v"(rst[0]), "+v"(rst[1]));
#pragma nounroll
  for (int spin = 0; spin < SPIN_CAP; ++spin) {
    bool ok = rst[0][1] == tag && rst[0][3] == tag && rst[1][1] == tag && rst[1][3] == tag;
    if (__builtin_expect(__all(ok), 1)) break;
    __builtin_amdgcn_s_sleep(1);
    ld128d(rst[0], wsd, WS_RST + (rt16 * 128 + wm * 32 + r16) * 16, 0);
    ld128d(rst[1], wsd, WS_RST + (rt16 * 128 + wm * 32 + r16) * 16, 256);
    asm volatile("s_waitcnt vmcnt(0)" : "+v"(rst[0]), "+v"(rst[1]) :: "memory");
  }
  LBAR();
  float rs[2];
  {
    u32x2 mo[2];
#pragma unroll
    for (int m = 0; m < 2; ++m) {
      float mu = -asf(rst[m][2]);
      unsigned hh = pk2(mu, mu);
      mo[m] = u32x2{hh, pk2(mu - bflo(hh), 0.f)};
      rs[m] = asf(rst[m][0]);
    }
#pragma unroll
    for (int n = 0; n < 6; ++n) {
      u32x2 co = *(const u32x2*)(smem + L_MISC + 2048 + ((g * 12 + wn * 6 + n) * 16 + r16) * 8);
      acc[0][n] = mfma16(co, mo[0], acc[0][n]);
      acc[1][n] = mfma16(co, mo[1], acc[1][n]);
    }
  }
  bf16x8 af[8][2][2];
  const int aro = w * 4096 + r16 * 128;
  auto wsp_step = [&](int c) {
    if (c < 6) { WAITV(8); } else if (c == 6) { WAITV(4); } else { WAITV(0); }
#pragma unroll
    for (int e = 0; e < 2; ++e)
#pragma unroll
      for (int t = 0; t < 2; ++t)
        af[c][e][t] = *(const bf16x8*)(smem + (c % 3) * L_SLOT + aro + t * 2048 + (((4 * e + g) ^ ((r16 >> 1) & 7)) << 4));
    if (c + 3 < 8) {
      WAITL0();
#pragma unroll
      for (int u = 0; u < 4; ++u) dmaA(c + 3, u);
    }
  };
  char* scr = smem + L_SCR + w * 6144;
  u32x2 wph[2], qb[2][2];
  STAMP(3);
  if (isqkv) {
    wsp_step(0);
#pragma unroll
    for (int e = 0; e < 2; ++e) wph[e] = __builtin_bit_cast(u32x2, trread(smem + L_MISC + (2 * wn + e) * 512 + (4 * g + tq) * 32 + tp * 8));
#pragma unroll
    for (int e = 0; e < 2; ++e)
#pragma unroll
      for (int m = 0; m < 2; ++m) {
        *(u32x4*)(scr + e * 2048 + (16 * m + r16) * 64 + g * 16) = hedgehog(mfma16(wph[e], pk4(acc[m][2 + e] * rs[m]), zero4));
        *(u32x2*)(scr + 4096 + e * 1024 + (16 * m + r16) * 32 + g * 8) = pk4(acc[m][4 + e] * rs[m]);
        qb[m][e] = pk4(acc[m][e] * rs[m]);
      }
    wsp_step(1);
    f32x4 kv[2][2], kz[2][2];
#pragma unroll
    for (int e = 0; e < 2; ++e) {
      const int row = 8 * g + tq;
      const char* pp = scr + e * 2048 + row * 64 + tp * 16;
      const char* pv = scr + 4096 + e * 1024 + row * 32 + tp * 8;
      bf16x8 ap = cat8(trread(pp), trread(pp + 256));
      bf16x8 an = cat8(trread(pp + 8), trread(pp + 264));
      bf16x8 bv = cat8(trread(pv), trread(pv + 128));
      kv[e][0] = mfma32(ap, bv, zero4);
      kv[e][1] = mfma32(an, bv, zero4);
      kz[e][0] = mfma32(ap, ones8, zero4);
      kz[e][1] = mfma32(an, ones8, zero4);
    }
#pragma unroll
    for (int e = 0; e < 2; ++e)
#pragma unroll
      for (int s = 0; s < 2; ++s) {
        *(f32x4*)(scr + (e * 2 + s) * 1024 + lane * 16) = kv[e][s];
        if (r16 == 0) *(f32x4*)(scr + 4096 + ((e * 2 + s) * 4 + g) * 16) = kz[e][s];
      }
    LBAR();
    {
      f32x4 a = zero4, b = zero4;
#pragma unroll
      for (int j = 0; j < 4; ++j) {
        const char* p = smem + L_SCR + (j * 2 + wn) * 6144;
        a += *(const f32x4*)(p + wm * 1024 + lane * 16);
        b += *(const f32x4*)(p + 4096 + (wm * 4 + g) * 16);
      }
      const int base = WS_KVP + (grp * 16 + rt16) * KVP_SZ;
      const int hs = wn * 4 + wm;
      __builtin_amdgcn_raw_buffer_store_b128(u32x4{asu(a[0]), tag, asu(a[1]), tag}, wsr, base + hs * 2048 + lane * 32, 0, 16);
      __builtin_amdgcn_raw_buffer_store_b128(u32x4{asu(a[2]), tag, asu(a[3]), tag}, wsr, base + hs * 2048 + lane * 32 + 16, 0, 16);
      if (r16 < 2) {
        const float v0 = r16 ? b[2] : b[0], v1 = r16 ? b[3] : b[1];
        __builtin_amdgcn_raw_buffer_store_b128(u32x4{asu(v0), tag, asu(v1), tag}, wsr, base + 16384 + hs * 128 + g * 32 + r16 * 16, 0, 16);
      }
    }
    wsp_step(2);
  } else {
    u32x2 zb[2][3];
#pragma unroll
    for (int m = 0; m < 2; ++m) {
      float s = 0.f, q = 0.f;
#pragma unroll
      for (int n = 0; n < 3; ++n) {
        f32x4 y;
#pragma unroll
        for (int r = 0; r < 4; ++r) y[r] = gelu_tanh(acc[m][n][r] * rs[m]);
        zb[m][n] = pk4(y);
        if (3 * wn + n < znft) {
          s = dot2(zb[m][n][0], ONES, s); s = dot2(zb[m][n][1], ONES, s);
          q = dot2(zb[m][n][0], zb[m][n][0], q); q = dot2(zb[m][n][1], zb[m][n][1], q);
        }
      }
      redg2<false>(s, q);
      if (g == 0) *(f32x2*)(smem + L_MISC2 + (wn * 128 + wm * 32 + m * 16 + r16) * 8) = f32x2{s, q};
    }
    LBAR();
    if (tid < 128) {
      f32x2 a = *(const f32x2*)(smem + L_MISC2 + tid * 8), b = *(const f32x2*)(smem + L_MISC2 + (128 + tid) * 8);
      __builtin_amdgcn_raw_buffer_store_b128(u32x4{asu(a[0] + b[0]), tag, asu(a[1] + b[1]), tag}, wsr,
                                             WS_ZST + ((rt16 * 8 + grp) * 128 + tid) * 16, 0, 16);
    }
    STAMP(4);
    wsp_step(0);
    wsp_step(1);
#pragma unroll
    for (int n = 0; n < 3; ++n) {
#pragma unroll
      for (int m = 0; m < 2; ++m) {
        f32x4 y;
#pragma unroll
        for (int r = 0; r < 4; ++r) y[r] = gelu_tanh(acc[m][3 + n][r] * rs[m]);
        if (3 * wn + n < znft)
          __builtin_amdgcn_raw_buffer_store_b64(pk4(y), wsr, WS_Z1F + ((rt16 * 8 + wm * 2 + m) * 44 + zft0 + 3 * wn + n) * 512 + lane * 8, 0, 16);
      }
    }
    STAMP(5);
    u32x4 zs[4];
#pragma nounroll
    for (int spin = 0; spin < SPIN_CAP; ++spin) {
      const int off = WS_ZST + ((rt16 * 8 + 2 * g) * 128 + wm * 32 + r16) * 16;
      ld128d(zs[0], wsd, off, 0); ld128d(zs[1], wsd, off, 2048); ld128d(zs[2], wsd, off, 256); ld128d(zs[3], wsd, off, 2304);
      asm volatile("s_waitcnt vmcnt(0)" : "+v"(zs[0]), "+v"(zs[1]), "+v"(zs[2]), "+v"(zs[3]) :: "memory");
      bool ok = true;
#pragma unroll
      for (int j = 0; j < 4; ++j) ok = ok && zs[j][1] == tag && zs[j][3] == tag;
      if (__all(ok)) break;
      __builtin_amdgcn_s_sleep(1);
    }
    STAMP(6);
#pragma unroll
    for (int m = 0; m < 2; ++m) {
      float s = asf(zs[2 * m][0]) + asf(zs[2 * m + 1][0]), q = asf(zs[2 * m][2]) + asf(zs[2 * m + 1][2]);
      redg2<false>(s, q);
      float mean = s * (1.0f / 688.0f);
      float var = fmaxf(q * (1.0f / 688.0f) - mean * mean, 0.f);
      float r = __builtin_amdgcn_rsqf(var + 1e-5f);
      float c = -mean * r;
#pragma unroll
      for (int n = 0; n < 3; ++n) {
        u32x2 o;
        o[0] = pk2(fmaf(bflo(zb[m][n][0]), r, c), fmaf(bfhi(zb[m][n][0]), r, c));
        o[1] = pk2(fmaf(bflo(zb[m][n][1]), r, c), fmaf(bfhi(zb[m][n][1]), r, c));
        *(u32x2*)(scr + n * 1024 + (16 * m + r16) * 32 + g * 8) = o;
      }
    }
    {
      const int ksg = rt16 * 4 + wm;
      const int dst = WS_Z2F + ((((ksg >> 3) * 2 + (ksg & 1)) * 44 + zft0 + 3 * wn) * 4 + ((ksg >> 1) & 3)) * 1024 + lane * 16;
#pragma unroll
      for (int n = 0; n < 3; ++n) {
        const char* p = scr + n * 1024 + (8 * g + tq) * 32 + tp * 8;
        bf16x8 f = cat8(trread(p), trread(p + 128));
        if (3 * wn + n < znft)
          __builtin_amdgcn_raw_buffer_store_b128(__builtin_bit_cast(u32x4, f), wsr, dst + n * 4096, 0, 16);
      }
    }
    WAITV(0);
    if (lane == 0) __builtin_amdgcn_raw_buffer_store_b64(u32x2{tag, tag ^ TAGX}, wsr, WS_FLZ + ((grp * 16 + rt16) * 8 + w) * 8, 0, 16);
    wsp_step(2);
    wph[0] = wph[1] = qb[0][0] = qb[0][1] = qb[1][0] = qb[1][1] = u32x2{0u, 0u};
  }
  STAMP(7);
#pragma unroll
  for (int c = 3; c < 8; ++c) wsp_step(c);
  LBAR();
  STAMP(8);
  const int w3off = (lane >> 3) * 1024 + cgp * 128 + (((lane & 7) ^ ((((lane >> 4) & 1) | (((lane >> 5) & 1) << 1)) << 1)) << 4);
  {
    const u32x4 od = mkdesc(wo, 512u * 1024u);
#pragma unroll
    for (int i = 0; i < 8; ++i) dma16(od, w3off, (i * 8 + w) * 8192, L_WO + (i * 8 + w) * 1024);
  }
  if (isqkv) {
#pragma nounroll
    for (int rep = 0; rep < ((w == 0 && rt16 < 8) ? 2 : 1); ++rep) {
      const int sub = rep ? 128 + rt16 : rt16 * 8 + w;
      const int off = WS_KVP + (grp * 16 + (lane >> 3)) * KVP_SZ + (sub * 8 + (lane & 7)) * 16;
      u32x4 a, b;
#pragma nounroll
      for (int spin = 0; spin < SPIN_CAP; ++spin) {
        ld128d(a, wsd, off, 0); ld128d(b, wsd, off, 8 * KVP_SZ);
        asm volatile("s_waitcnt vmcnt(0)" : "+v"(a), "+v"(b) :: "memory");
        if (__all(a[1] == tag && a[3] == tag && b[1] == tag && b[3] == tag)) break;
        __builtin_amdgcn_s_sleep(1);
      }
      float v0 = asf(a[0]) + asf(b[0]), v1 = asf(a[2]) + asf(b[2]);
      v0 = dppadd<0x128>(v0); v1 = dppadd<0x128>(v1);
      redg2<false>(v0, v1);
      if (lane < 8) __builtin_amdgcn_raw_buffer_store_b64(u32x2{pk2(v0, v1), tag}, wsr, WS_KVR + (grp * 1088 + sub * 8 + lane) * 8, 0, 16);
    }
  }
  STAMP(9);
  if (w == 0) {
#pragma nounroll
    for (int spin = 0; spin < SPIN_CAP; ++spin) {
      u32x4 f;
      ld128d(f, wsd, WS_FLZ + cgp * 1024 + lane * 16, 0);
      asm volatile("s_waitcnt vmcnt(0)" : "+v"(f) :: "memory");
      if (__all(f[0] == tag && f[1] == (tag ^ TAGX) && f[2] == tag && f[3] == (tag ^ TAGX))) break;
      __builtin_amdgcn_s_sleep(1);
    }
  }
  BAR();
  STAMP(10);
  const int bsrc = WS_Z2F + ft0 * 4096 + w * 1024 + lane * 16;
  const int u2off = npair == 3 ? 16384 : 0;
  auto dma2 = [&](int st, int u) {
    dma16d(wsd, bsrc, ((st + 2 * rot2) & 15) * (44 * 4096) + (u < 2 ? u * 8192 : u2off), (st & 3) * L_RING + u * 8192 + w * 1024);
  };
#pragma unroll
  for (int st = 0; st < 4; ++st)
#pragma unroll
    for (int u = 0; u < 3; ++u) dma2(st, u);
  f32x4 ac2[6][2];
#pragma unroll
  for (int n = 0; n < 6; ++n) { ac2[n][0] = zero4; ac2[n][1] = zero4; }
  bf16x8 fz[2][6];
  auto rdZ = [&](int st, int n) { fz[st & 1][n] = *(const bf16x8*)(smem + (st & 3) * L_RING + n * 4096 + kq * 1024 + lane * 16); };
  u32x4 kr[8];
  u32x4 ao[4][2];
  u32x2 z1v[2][2];
  unsigned biasv[2];
  u32x2 ofl;
  const int kroff = WS_KVR + (grp * 1088 + wn * 512) * 8 + lane * 16;
  const int kzoff = WS_KVR + (grp * 1088 + 1024 + wn * 32) * 8 + g * 16;
  auto ld_kr = [&]() {
#pragma unroll
    for (int j = 0; j < 4; ++j) { ld128d(kr[j], wsd, kroff, j * 1024); ld128d(kr[4 + j], wsd, kzoff, j * 64); }
  };
  const int a3 = WS_A3F + ((rt64 * 4 + th * 2) * 38 + kq) * 1024 + lane * 16;
  const int oflo = WS_FLO + ((rt64 >> 1) * 64 + (rt64 & 1) * 32 + (lane & 31)) * 8;
  waitv<9>();
  BAR();
#pragma unroll
  for (int n = 0; n < 6; ++n) rdZ(0, n);
  sfor<16>([&](auto ST) {
    constexpr int st = ST;
    if constexpr (st < 15) {
      WAITL0();
      waitv<(st == 5 || st == 6 ? 14 : (st == 13 ? 9 : 6))>();
      BAR();
    }
    sfor<6>([&](auto N) {
      constexpr int n = N;
      SB();
      ac2[n][0] = mfma32(fz[st & 1][n], af[st >> 1][st & 1][0], ac2[n][0]);
      ac2[n][1] = mfma32(fz[st & 1][n], af[st >> 1][st & 1][1], ac2[n][1]);
      SB();
      if constexpr (st < 15) {
        if constexpr (n == 1) { rdZ(st + 1, 0); rdZ(st + 1, 1); }
        if constexpr (n == 2) { rdZ(st + 1, 2); rdZ(st + 1, 3); }
        if constexpr (n == 3) { rdZ(st + 1, 4); rdZ(st + 1, 5); }
      }
      if constexpr (st + 4 < 16) {
        if constexpr (n == 0) dma2(st + 4, 0);
        if constexpr (n == 4) dma2(st + 4, 1);
        if constexpr (n == 5) dma2(st + 4, 2);
      }
    });
    if constexpr (st == 4) ld_kr();
    if constexpr (st == 7) {
      if (isqkv) {
        asm volatile("" : "+v"(kr[0]), "+v"(kr[1]), "+v"(kr[2]), "+v"(kr[3]), "+v"(kr[4]), "+v"(kr[5]), "+v"(kr[6]), "+v"(kr[7]));
#pragma nounroll
        for (int spin = 0; spin < SPIN_CAP; ++spin) {
          bool ok = true;
#pragma unroll
          for (int j = 0; j < 8; ++j) ok = ok && kr[j][1] == tag && kr[j][3] == tag;
          if (__builtin_expect(__all(ok), 1)) break;
          __builtin_amdgcn_s_sleep(1);
          ld_kr();
          asm volatile("s_waitcnt vmcnt(0)" : "+v"(kr[0]), "+v"(kr[1]), "+v"(kr[2]), "+v"(kr[3]), "+v"(kr[4]), "+v"(kr[5]), "+v"(kr[6]), "+v"(kr[7]) :: "memory");
        }
#pragma unroll
        for (int m = 0; m < 2; ++m) {
          u32x4 o;
#pragma unroll
          for (int e = 0; e < 2; ++e) {
            const bf16x8 kvo = asb8(u32x4{kr[2 * e][0], kr[2 * e][2], kr[2 * e + 1][0], kr[2 * e + 1][2]});
            const bf16x8 zo = asb8(u32x4{kr[4 + 2 * e][0], kr[4 + 2 * e][2], kr[5 + 2 * e][0], kr[5 + 2 * e][2]});
            const bf16x8 pq = asb8(hedgehog(mfma16(wph[e], qb[m][e], zero4)));
            f32x4 num = mfma32(kvo, pq, zero4);
            f32x4 den = mfma32(zo, pq, zero4);
            u32x2 v = pk4(num * __builtin_amdgcn_rcpf(den[0] + 1e-6f));
            o[2 * e] = v[0]; o[2 * e + 1] = v[1];
          }
          __builtin_amdgcn_raw_buffer_store_b128(o, wsr, WS_A3F + ((rt16 * 8 + wm * 2 + m) * 38 + grp * 2 + wn) * 1024 + lane * 16, 0, 16);
        }
      }
    }
    if constexpr (st == 10) {
      if (isqkv && lane == 0)
        __builtin_amdgcn_raw_buffer_store_b64(u32x2{tag, tag ^ TAGX}, wsr, WS_FLO + ((rt16 * 4 + wm) * 16 + grp * 2 + wn) * 8, 0, 16);
    }
    if constexpr (st == 12) {
#pragma unroll
      for (int e = 0; e < 2; ++e) {
        const int un = min(kq + 4 * e, 2 * npair - 1), tt = rt64 * 4 + th * 2 + (un & 1);
        const int zo = WS_Z1F + (tt * 44 + ft0 + (un & ~1)) * 512 + lane * 8;
        asm volatile("buffer_load_dwordx2 %0, %3, %4, 0 offen sc1\n\tbuffer_load_dwordx2 %1, %3, %4, 0 offen offset:512 sc1\n\t"
                     "buffer_load_ushort %2, %5, %6, 0 offen"
                     : "=&v"(z1v[e][0]), "=&v"(z1v[e][1]), "=&v"(biasv[e])
                     : "v"(zo), "s"(wsd), "v"((tt * 16 + r16) * 2), "s"(mkdesc(bsp, 4096u)) : "memory");
      }
    }
    if constexpr (st == 14) {
      ld64d(ofl, wsd, oflo);
    }
  });
  STAMP(11);
  asm volatile("s_waitcnt vmcnt(0)" : "+v"(ofl), "+v"(z1v[0][0]), "+v"(z1v[0][1]), "+v"(z1v[1][0]), "+v"(z1v[1][1]),
               "+v"(biasv[0]), "+v"(biasv[1]) :: "memory");
#pragma nounroll
  for (int spin = 0; spin < SPIN_CAP; ++spin) {
    if (__builtin_expect(__all(ofl[0] == tag && ofl[1] == (tag ^ TAGX)), 1)) break;
    __builtin_amdgcn_s_sleep(1);
    ofl = poll64(wsd, oflo);
  }
#pragma unroll
  for (int i = 0; i < 4; ++i)
#pragma unroll
    for (int t = 0; t < 2; ++t) ld128d(ao[i][t], wsd, a3, (t * 38 + 4 * i) * 1024);
  BAR();
#pragma unroll
  for (int n = 0; n < 6; ++n)
#pragma unroll
    for (int t = 0; t < 2; ++t) *(f32x4*)(smem + ((th * 4 + kq) * 12 + n * 2 + t) * 1024 + lane * 16) = ac2[n][t];
  LBAR();
#pragma unroll
  for (int e = 0; e < 2; ++e) {
    const int un = kq + 4 * e;
    if (un < 2 * npair) {
      const int t = un & 1, p = un >> 1;
      const int tt = rt64 * 4 + th * 2 + t;
      const float bias = bflo(biasv[e]);
      u32x4 o;
#pragma unroll
      for (int hf = 0; hf < 2; ++hf) {
        f32x4 s = {bias, bias, bias, bias};
#pragma unroll
        for (int j = 0; j < 4; ++j) s += *(const f32x4*)(smem + ((th * 4 + j) * 12 + (2 * p + hf) * 2 + t) * 1024 + lane * 16);
        const bool valid = ft0 + 2 * p + hf < 43;
        unsigned lo = pk2(bflo(z1v[e][hf][0]) * s[0], bfhi(z1v[e][hf][0]) * s[1]);
        unsigned hi = pk2(bflo(z1v[e][hf][1]) * s[2], bfhi(z1v[e][hf][1]) * s[3]);
        o[2 * hf] = valid ? lo : 0u;
        o[2 * hf + 1] = valid ? hi : 0u;
      }
      __builtin_amdgcn_raw_buffer_store_b128(o, wsr, WS_A3F + (tt * 38 + 16 + (ft0 >> 1) + p) * 1024 + lane * 16, 0, 16);
    }
  }
  LBAR();
  STAMP(12);
  f32x4 ac3[4][2];
#pragma unroll
  for (int n = 0; n < 4; ++n) { ac3[n][0] = zero4; ac3[n][1] = zero4; }
  int b3[4];
  {
    const int k = 4 * g + tq;
    const int h2 = ((k >> 1) & 1) | (((k >> 2) & 1) << 1);
#pragma unroll
    for (int n = 0; n < 4; ++n) b3[n] = k * 128 + (((2 * n + (tp >> 1)) ^ (h2 << 1)) << 4) + (tp & 1) * 8 + kq * 4096;
  }
  WAITV(0);
  if (lane == 0) __builtin_amdgcn_raw_buffer_store_b64(u32x2{tag, tag ^ TAGX}, wsr, WS_FLU + ((rt64 * 8 + cgp) * 8 + w) * 8, 0, 16);
  asm volatile("" : "+v"(ao[0][0]), "+v"(ao[0][1]), "+v"(ao[1][0]), "+v"(ao[1][1]), "+v"(ao[2][0]), "+v"(ao[2][1]), "+v"(ao[3][0]), "+v"(ao[3][1]));
  const u32x4 ud = mkdesc(wout, 688u * 1024u);
  const int go = (rt64 * 64 + (tid >> 3)) * 1024 + cgp * 128 + (tid & 7) * 16;
  u32x4 xres;
  asm volatile("buffer_load_dwordx4 %0, %1, %2, 0 offen" : "=v"(xres) : "v"(go), "s"(xd) : "memory");
#pragma unroll
  for (int i = 0; i < 4; ++i) {
    bf16x8 b[4];
#pragma unroll
    for (int n = 0; n < 4; ++n) b[n] = cat8(trread(smem + L_WO + b3[n] + i * 16384), trread(smem + L_WO + b3[n] + i * 16384 + 2048));
#pragma unroll
    for (int j = 0; j < 3; ++j)
      if (3 * i + j < 11) dma16(ud, w3off, ((3 * i + j) * 8 + w) * 8192, ((3 * i + j) * 8 + w) * 1024);
#pragma unroll
    for (int n = 0; n < 4; ++n)
#pragma unroll
      for (int t = 0; t < 2; ++t) ac3[n][t] = mfma32(b[n], asb8(ao[i][t]), ac3[n][t]);
  }
  STAMP(13);
  if (bid == 0 && w == 7) {
#pragma nounroll
    for (int spin = 0; spin < SPIN_CAP; ++spin) {
      u32x4 f[4];
#pragma unroll
      for (int j = 0; j < 4; ++j) ld128d(f[j], wsd, WS_RST + lane * 128, j * 8192);
      asm volatile("s_waitcnt vmcnt(0)" : "+v"(f[0]), "+v"(f[1]), "+v"(f[2]), "+v"(f[3]) :: "memory");
      if (__all(f[0][1] == tag && f[1][1] == tag && f[2][1] == tag && f[3][1] == tag)) break;
    }
    if (lane == 0) __builtin_amdgcn_raw_buffer_store_b32(tag, wsr, WS_EP, 0, 16);
  }
#pragma nounroll
  for (int spin = 0; spin < SPIN_CAP; ++spin) {
    u32x2 f = poll64(wsd, WS_FLU + (rt64 * 64 + lane) * 8);
    if (__all(f[0] == tag && f[1] == (tag ^ TAGX))) break;
    __builtin_amdgcn_s_sleep(1);
  }
  u32x4 au[6][2];
#pragma unroll
  for (int i = 0; i < 6; ++i)
#pragma unroll
    for (int t = 0; t < 2; ++t) ld128d(au[i][t], wsd, a3, (t * 38 + 16 + ((i < 5 || kq < 2) ? 4 * i : 16)) * 1024);
  LBAR();
  STAMP(14);
  sfor<6>([&](auto I) {
    constexpr int i = I;
    bf16x8 b[4];
    const int io = (i < 5 || kq < 2) ? i * 16384 : 0;
#pragma unroll
    for (int n = 0; n < 4; ++n) b[n] = cat8(trread(smem + b3[n] + io), trread(smem + b3[n] + io + 2048));
    asm volatile("s_waitcnt vmcnt(%2)" : "+v"(au[i][0]), "+v"(au[i][1]) : "n"(10 - 2 * i) : "memory");
    if (i < 5 || kq < 2) {
#pragma unroll
      for (int n = 0; n < 4; ++n)
#pragma unroll
        for (int t = 0; t < 2; ++t) ac3[n][t] = mfma32(b[n], asb8(au[i][t]), ac3[n][t]);
    }
  });
  STAMP(15);
#pragma unroll
  for (int n = 0; n < 4; ++n)
#pragma unroll
    for (int t = 0; t < 2; ++t)
      *(f32x4*)(smem + L_WO + kq * 16384 + (th * 32 + t * 16 + r16) * 256 + (((n * 4 + g) ^ r16) << 4)) = ac3[n][t];
  LBAR();
  {
    asm volatile("" : "+v"(xres));
    const int row = tid >> 3, c8 = tid & 7;
    f32x4 s0 = {bflo(xres[0]), bfhi(xres[0]), bflo(xres[1]), bfhi(xres[1])};
    f32x4 s1 = {bflo(xres[2]), bfhi(xres[2]), bflo(xres[3]), bfhi(xres[3])};
#pragma unroll
    for (int j = 0; j < 4; ++j) {
      s0 += *(const f32x4*)(smem + L_WO + j * 16384 + row * 256 + (((2 * c8) ^ (row & 15)) << 4));
      s1 += *(const f32x4*)(smem + L_WO + j * 16384 + row * 256 + (((2 * c8 + 1) ^ (row & 15)) << 4));
    }
    const u32x2 lo = pk4(s0), hi = pk4(s1);
    *(u32x4*)(out + go) = u32x4{lo[0], lo[1], hi[0], hi[1]};
  }
  STAMP(16);
}

extern "C" void kernel_launch(void* const* d_in, const int* in_sizes, int n_in, void* d_out, int out_size, void* d_ws,
                              size_t ws_size, hipStream_t stream) {
  (void)hipFuncSetAttribute((const void*)fused, hipFuncAttributeMaxDynamicSharedMemorySize, LDS_BYTES);
  fused<<<256, 512, LDS_BYTES, stream>>>((const char*)d_in[0], (const char*)d_in[1], (const char*)d_in[2],
                                         (const char*)d_in[3], (const char*)d_in[5], (const char*)d_in[6],
                                         (const char*)d_in[7], (const char*)d_in[8], (char*)d_out, (char*)d_ws);
}
```

```cpp
#define OPT_KARG
#define OPT_CODE
#include <hip/hip_runtime.h>
#include <hip/hip_bf16.h>
#include <stdint.h>

typedef __bf16 bf16x8 __attribute__((ext_vector_type(8)));
typedef __bf16 bf16x2 __attribute__((ext_vector_type(2)));
typedef short s16x4 __attribute__((ext_vector_type(4)));
typedef short s16x8 __attribute__((ext_vector_type(8)));
typedef float f32x4 __attribute__((ext_vector_type(4)));
typedef float f32x2 __attribute__((ext_vector_type(2)));
typedef unsigned u32x4 __attribute__((ext_vector_type(4)));
typedef unsigned u32x2 __attribute__((ext_vector_type(2)));
#define LAS __attribute__((address_space(3)))
#define DI __device__ __forceinline__

constexpr int WS_EP  = 0;
constexpr int WS_FLZ = 1024;
constexpr int WS_FLO = WS_FLZ + 8192;
constexpr int WS_FLU = WS_FLO + 8192;
constexpr int WS_RST = WS_FLU + 16384;
constexpr int WS_ZST = WS_RST + 2048 * 16;
constexpr int KVP_SZ = 1088 * 16;
constexpr int WS_KVP = WS_ZST + 16 * 8 * 128 * 16;
constexpr int WS_KVR = WS_KVP + 8 * 16 * KVP_SZ;
constexpr int WS_Z2F = WS_KVR + 8 * 1088 * 8;
constexpr int WS_Z1F = WS_Z2F + 16 * 44 * 4096;
constexpr int WS_A3F = WS_Z1F + 128 * 44 * 512;
constexpr int WS_END = WS_A3F + 128 * 38 * 1024;
static_assert(WS_Z2F % 128 == 0 && WS_KVP % 128 == 0 && WS_KVR % 128 == 0, "alignment");

template <int V> struct IC { static constexpr int value = V; constexpr operator int() const { return V; } };
template <int I, int N, class F> DI void sfor_(F&& f) { if constexpr (I < N) { f(IC<I>{}); sfor_<I + 1, N>(f); } }
template <int N, class F> DI void sfor(F&& f) { sfor_<0, N>(f); }
template <int N> DI void waitv() { asm volatile("s_waitcnt vmcnt(%0)" :: "n"(N) : "memory"); }

DI float asf(unsigned u) { return __builtin_bit_cast(float, u); }
DI unsigned asu(float f) { return __builtin_bit_cast(unsigned, f); }
DI u32x4 mkdesc(const void* p, unsigned bytes) {
  unsigned long long a = (unsigned long long)p;
  u32x4 d = {(unsigned)a, (unsigned)(a >> 32) & 0xffffu, bytes, 0x00020000u};
  return d;
}
DI void dma16(u32x4 desc, int voff, int soff, int lds) {
  asm volatile("s_mov_b32 m0, %3\n\ts_nop 0\n\tbuffer_load_dwordx4 %0, %1, %2 offen lds"
               :: "v"(voff), "s"(desc), "s"(soff), "s"(lds) : "memory");
}
DI void dma16d(u32x4 desc, int voff, int soff, int lds) {
  asm volatile("s_mov_b32 m0, %3\n\ts_nop 0\n\tbuffer_load_dwordx4 %0, %1, %2 offen sc1 lds"
               :: "v"(voff), "s"(desc), "s"(soff), "s"(lds) : "memory");
}
DI u32x2 poll64(u32x4 desc, int voff) {
  u32x2 r;
  asm volatile("buffer_load_dwordx2 %0, %1, %2, 0 offen sc1\n\ts_waitcnt vmcnt(0)" : "=&v"(r) : "v"(voff), "s"(desc) : "memory");
  return r;
}
DI void ld128d(u32x4& r, u32x4 desc, int voff, int soff) {
  asm volatile("buffer_load_dwordx4 %0, %1, %2, %3 offen sc1" : "=v"(r) : "v"(voff), "s"(desc), "s"(soff) : "memory");
}
DI void ld64d(u32x2& r, u32x4 desc, int voff) {
  asm volatile("buffer_load_dwordx2 %0, %1, %2, 0 offen sc1" : "=v"(r) : "v"(voff), "s"(desc) : "memory");
}
DI void gld32(unsigned& r, const char* p) { asm volatile("global_load_dword %0, %1, off" : "=&v"(r) : "v"(p) : "memory"); }
DI void keepu(unsigned v) { asm volatile("" :: "v"(v)); }
DI s16x4 trread(const char* p) { return __builtin_amdgcn_ds_read_tr16_b64_v4i16((LAS s16x4*)p); }
DI bf16x8 cat8(s16x4 a, s16x4 b) {
  s16x8 r = __builtin_shufflevector(a, b, 0, 1, 2, 3, 4, 5, 6, 7);
  return __builtin_bit_cast(bf16x8, r);
}
DI void keep(bf16x8 v) { asm volatile("" :: "v"(v)); }
DI bf16x8 asb8(u32x4 v) { return __builtin_bit_cast(bf16x8, v); }
DI u32x4 asu4(f32x4 v) { return __builtin_bit_cast(u32x4, v); }
DI f32x4 asf4(u32x4 v) { return __builtin_bit_cast(f32x4, v); }
DI f32x4 mfma32(bf16x8 a, bf16x8 b, f32x4 c) { return __builtin_amdgcn_mfma_f32_16x16x32_bf16(a, b, c, 0, 0, 0); }
DI f32x4 mfma16(u32x2 a, u32x2 b, f32x4 c) {
  return __builtin_amdgcn_mfma_f32_16x16x16bf16_1k(__builtin_bit_cast(s16x4, a), __builtin_bit_cast(s16x4, b), c, 0, 0, 0);
}
template <int CTRL> DI float dppadd(float v) { return v + asf(__builtin_amdgcn_update_dpp(0u, asu(v), CTRL, 0xf, 0xf, true)); }
DI unsigned pk2(float a, float b) {
  f32x2 f = {a, b};
  return __builtin_bit_cast(unsigned, __builtin_convertvector(f, bf16x2));
}
DI u32x2 pk4(f32x4 v) { return u32x2{pk2(v[0], v[1]), pk2(v[2], v[3])}; }
DI float bflo(unsigned u) { return asf(u << 16); }
DI float bfhi(unsigned u) { return asf(u & 0xffff0000u); }
DI float dot2(unsigned a, unsigned b, float c) {
  return __builtin_amdgcn_fdot2_f32_bf16(__builtin_bit_cast(bf16x2, a), __builtin_bit_cast(bf16x2, b), c, false);
}
template <bool MAX> DI void redg2(float& a, float& b) {
  auto r = __builtin_amdgcn_permlane16_swap(asu(a), asu(b), false, false);
  float m = MAX ? fmaxf(asf(r[0]), asf(r[1])) : asf(r[0]) + asf(r[1]);
  auto r2 = __builtin_amdgcn_permlane32_swap(asu(m), asu(m), false, false);
  m = MAX ? fmaxf(asf(r2[0]), asf(r2[1])) : asf(r2[0]) + asf(r2[1]);
  auto r3 = __builtin_amdgcn_permlane16_swap(asu(m), asu(m), false, false);
  a = asf(r3[0]); b = asf(r3[1]);
}
constexpr float L2E = 1.4426950408889634f;
DI u32x4 hedgehog(f32x4 p) {
  float mx = fmaxf(fmaxf(p[0], p[1]), fmaxf(p[2], p[3]));
  float mn = -fminf(fminf(p[0], p[1]), fminf(p[2], p[3]));
  redg2<true>(mx, mn);
  const float a = -mx * L2E, b = -mn * L2E;
  float ep[4], en[4];
#pragma unroll
  for (int r = 0; r < 4; ++r) {
    ep[r] = __builtin_amdgcn_exp2f(fmaf(p[r], L2E, a));
    en[r] = __builtin_amdgcn_exp2f(fmaf(p[r], -L2E, b));
  }
  float sp = (ep[0] + ep[1]) + (ep[2] + ep[3]);
  float sn = (en[0] + en[1]) + (en[2] + en[3]);
  redg2<false>(sp, sn);
  float ip = __builtin_amdgcn_rcpf(sp), in = __builtin_amdgcn_rcpf(sn);
  return u32x4{pk2(ep[0] * ip, ep[1] * ip), pk2(ep[2] * ip, ep[3] * ip), pk2(en[0] * in, en[1] * in), pk2(en[2] * in, en[3] * in)};
}
DI float gelu_tanh(float x) {
  const float c1 = -2.0f * 0.7978845608028654f * L2E;
  const float c2 = c1 * 0.044715f;
  float t = x * fmaf(x * x, c2, c1);
  return x * __builtin_amdgcn_rcpf(1.0f + __builtin_amdgcn_exp2f(t));
}
#define STAMP(i)
#define WAITV(n) asm volatile("s_waitcnt vmcnt(" #n ")" ::: "memory")
#define WAITL0() asm volatile("s_waitcnt lgkmcnt(0)" ::: "memory")
#define SB() __builtin_amdgcn_sched_barrier(0)
#define BAR() do { SB(); __builtin_amdgcn_s_barrier(); SB(); } while (0)
#define LBAR() do { WAITL0(); BAR(); } while (0)
constexpr int SPIN_CAP = 1 << 18;
constexpr unsigned TAGX = 0x5bd1e995u;

constexpr int L_SLOT = 40960;
constexpr int L_SCR = 114688;
constexpr int L_MISC = 32768;
constexpr int L_MISC2 = 73728;
constexpr int L_RING = 24576;
constexpr int L_WO = 98304;
constexpr int LDS_BYTES = 163840;

__global__ __launch_bounds__(512, 2) void fused(const char* __restrict__ x, const char* __restrict__ wqkv,
                                                const char* __restrict__ wphi, const char* __restrict__ wo,
                                                const char* __restrict__ win, const char* __restrict__ wsp,
                                                const char* __restrict__ bsp, const char* __restrict__ wout,
                                                char* out, char* ws) {
  extern __shared__ __attribute__((aligned(16))) char smem[];
  unsigned dummy0 = 0, dummy1 = 0, dummy2 = 0;
  asm volatile("" :: "s"(x), "s"(wqkv), "s"(wphi), "s"(wo), "s"(win), "s"(wsp), "s"(bsp), "s"(wout), "s"(out), "s"(ws));
  {
    unsigned long long pc = __builtin_amdgcn_s_getpc() & ~127ull;
    const int li = min((blockIdx.x >> 3) * 9 + min(threadIdx.x & 63, 8), 275);
    if (threadIdx.x < 64) asm volatile("global_load_dword %0, %1, %2" : "=&v"(dummy0) : "v"(li * 128), "s"(pc) : "memory");
  }
  const int tid = threadIdx.x, lane = tid & 63;
  const int w = __builtin_amdgcn_readfirstlane(tid >> 6);
  const int r16 = lane & 15, g = lane >> 4;
  const int tq = r16 >> 2, tp = r16 & 3;
  const int bid = blockIdx.x, xcd = bid & 7, jj = bid >> 3;
  const int rt16 = (xcd >> 2) * 8 + (jj >> 2);
  const bool isqkv = (jj & 2) == 0;
  const int grp = (xcd & 3) * 2 + (jj & 1);
  const int zft0 = grp < 7 ? grp * 6 : 40;
  const int znft = grp < 6 ? 6 : (grp == 6 ? 4 : 3);
  const int wm = w >> 1, wn = w & 1;
  const int rt64 = xcd * 4 + (jj >> 3);
  const int cgp = jj & 7;
  const int ft0 = cgp < 7 ? cgp * 6 : 40;
  const int npair = cgp < 6 ? 3 : 2;
  const int th = w & 1, kq = w >> 1;

#define ROT1 0
#define ROT2 0
  const int rot1 = ROT1, rot2 = ROT2;
  const __amdgpu_buffer_rsrc_t wsr = __builtin_amdgcn_make_buffer_rsrc(ws, 0, WS_END, 0x00020000);
  const u32x4 wsd = mkdesc(ws, WS_END);
  const u32x4 xd = mkdesc(x, 2048u * 1024u);
  const u32x4 wd = isqkv ? mkdesc(wqkv, 512u * 3072u) : mkdesc(win, 512u * 2752u);
  const u32x4 ad = mkdesc(wsp, 2048u * 4096u);
  const f32x4 zero4 = {0.f, 0.f, 0.f, 0.f};
  const unsigned ONES = 0x3f803f80u;
  const bf16x8 ones8 = asb8(u32x4{ONES, ONES, ONES, ONES});
  STAMP(0);

  int xsrc[2], wsrc[3];
#pragma unroll
  for (int u = 0; u < 2; ++u) {
    int row = 16 * w + 8 * u + (lane >> 3);
    xsrc[u] = (rt16 * 128 + row) * 1024 + (((lane & 7) ^ ((row >> 1) & 7)) << 4);
  }
  const int wstride = isqkv ? 64 * 3072 : 64 * 2752;
#pragma unroll
  for (int u = 0; u < 3; ++u) {
    int ci = (3 * w + u) * 64 + lane;
    int row = ci / 24, c = ci - row * 24;
    int h2 = ((row >> 1) & 1) | (((row >> 3) & 1) << 1);
    int cs = c ^ (h2 << 1);
    int vq = row * 3072 + (cs >> 3) * 1024 + grp * 128 + (cs & 7) * 16;
    int cz = min(cs < 12 ? cs : cs - 12, znft * 2 - 1);
    int vz = row * 2752 + (cs < 12 ? 1376 : 0) + zft0 * 32 + cz * 16;
    wsrc[u] = isqkv ? vq : vz;
  }
  auto dma1 = [&](int st, int u) {
    const int slot = (st & 3) * L_SLOT, sr = (st + rot1) & 7;
    if (u < 2) dma16(xd, xsrc[u], sr * 128, slot + (16 * w + 8 * u) * 128);
    else dma16(wd, wsrc[u - 2], sr * wstride, slot + 16384 + (3 * w + u - 2) * 1024);
  };
#pragma unroll
  for (int u = 0; u < 5; ++u) dma1(0, u);
  unsigned ep;
  u32x4 xrow;
  asm volatile("buffer_load_dword %0, off, %2, 0 sc1\n\tbuffer_load_dwordx4 %1, %3, %4, 0 offen"
               : "=&v"(ep), "=&v"(xrow) : "s"(wsd), "v"((bid * 8 + w) * 1024 + lane * 16), "s"(xd) : "memory");
#pragma unroll
  for (int u = 0; u < 5; ++u) dma1(1, u);

  const int asrc = (rt64 * 64 + th * 32 + (lane >> 3)) * 4096 + kq * 128 + (((lane & 7) ^ ((lane >> 4) & 3)) << 4);
  auto dmaA = [&](int c, int u) {
    dma16(ad, asrc ^ ((u & 1) << 6), ((c + rot2) & 7) * 512 + u * 8 * 4096, (c % 3) * L_SLOT + w * 4096 + u * 1024);
  };
  auto piece = [&](int p) {
    if (p < 30) dma1(2 + p / 5, p % 5);
    else if (p < 42) dmaA((p - 30) >> 2, (p - 30) & 3);
  };

  int aoff[2], boff[6];
#pragma unroll
  for (int ks = 0; ks < 2; ++ks) aoff[ks] = (wm * 32 + r16) * 128 + (((4 * ks + g) ^ ((r16 >> 1) & 7)) << 4);
  {
    int h2 = ((tq >> 1) & 1) | ((g & 1) << 1);
#pragma unroll
    for (int i = 0; i < 6; ++i) {
      int sg = isqkv ? ((i >> 1) * 4 + 2 * wn + (i & 1)) : ((i / 3) * 6 + 3 * wn + (i % 3));
      int chunk = 2 * sg + (tp >> 1);
      boff[i] = 16384 + (8 * g + tq) * 384 + ((chunk ^ (h2 << 1)) << 4) + (tp & 1) * 8;
    }
  }
  f32x4 acc[2][6], csum[6];
#pragma unroll
  for (int i = 0; i < 6; ++i) { acc[0][i] = zero4; acc[1][i] = zero4; csum[i] = zero4; }
  bf16x8 fa[2][2], fb[2][6];
  u32x4 rst[2];
  unsigned tag = 0;
  auto rdA = [&](int st, int ks, int m) { fa[ks][m] = *(const bf16x8*)(smem + (st & 3) * L_SLOT + aoff[ks] + m * 2048); };
  auto rdB = [&](int st, int ks, int n) {
    const char* p = smem + (st & 3) * L_SLOT + boff[n] + ks * 12288;
    fb[ks][n] = cat8(trread(p), trread(p + 1536));
  };
  waitv<7>();
  BAR();
  STAMP(1);
  rdB(0, 0, 0); rdA(0, 0, 0); rdA(0, 0, 1);
#pragma unroll
  for (int n = 1; n < 6; ++n) rdB(0, 0, n);
  sfor<16>([&](auto B) {
    constexpr int b = B, i = b >> 1, ks = b & 1;
    constexpr int nst = (b + 1) >> 1, nks = ks ^ 1;
    constexpr int p0 = b < 12 ? 3 * b : (b == 12 ? 36 : (b == 13 ? 38 : 42));
    constexpr int p1 = b < 12 ? 3 * b + 3 : (b == 12 ? 38 : 42);
    sfor<6>([&](auto N) {
      constexpr int n = N;
      SB();
      acc[0][n] = mfma32(fb[ks][n], fa[ks][0], acc[0][n]);
      acc[1][n] = mfma32(fb[ks][n], fa[ks][1], acc[1][n]);
      SB();
      if constexpr (nst < 8 && n < 3) {
        rdB(nst, nks, 2 * n);
        if constexpr (n < 2) rdA(nst, nks, n);
        rdB(nst, nks, 2 * n + 1);
      }
      if constexpr (n >= 2 && p0 + n - 2 < p1) piece(p0 + n - 2);
    });
    if ((b & 3) == wm) {
#pragma unroll
      for (int n = 0; n < 6; ++n) csum[n] = mfma32(ones8, fb[ks][n], csum[n]);
    }
    if constexpr (ks == 0 && i < 7) {
      WAITL0();
      waitv<(i < 5 ? i + 3 : (i == 5 ? 10 : 8))>();
      BAR();
    }
    if constexpr (b == 0) {
      asm volatile("" : "+v"(ep), "+v"(xrow));
      tag = __builtin_amdgcn_readfirstlane(ep) + 1u;
      float s = 0.f, q = 0.f;
#pragma unroll
      for (int e = 0; e < 4; ++e) { s = dot2(xrow[e], ONES, s); q = dot2(xrow[e], xrow[e], q); }
      redg2<false>(s, q);
      s = dppadd<0x128>(s); q = dppadd<0x128>(q); s = dppadd<0x124>(s); q = dppadd<0x124>(q);
      s = dppadd<0x122>(s); q = dppadd<0x122>(q); s = dppadd<0x121>(s); q = dppadd<0x121>(q);
      float mean = s * (1.0f / 512.0f);
      float var = fmaxf(q * (1.0f / 512.0f) - mean * mean, 0.f);
      float rs = __builtin_amdgcn_rsqf(var + 1e-5f);
      if (lane == 0)
        __builtin_amdgcn_raw_buffer_store_b128(u32x4{asu(rs), tag, asu(mean), tag}, wsr, WS_RST + (bid * 8 + w) * 16, 0, 16);
    }
    if constexpr (b == 8) { keepu(dummy0); keepu(dummy1); keepu(dummy2); }
    if constexpr (b == 9) {
      if (w < 2) dma16(mkdesc(wphi, 32u * 512u), lane * 16, grp * 2048 + w * 1024, L_MISC + w * 1024);
    }
    if constexpr (b == 9) {
      ld128d(rst[0], wsd, WS_RST + (rt16 * 128 + wm * 32 + r16) * 16, 0);
      ld128d(rst[1], wsd, WS_RST + (rt16 * 128 + wm * 32 + r16) * 16, 256);
    }
  });
  STAMP(2);
  if (g == 0) {
#pragma unroll
    for (int n = 0; n < 6; ++n) {
      float c = csum[n][0];
      unsigned hl = pk2(c, 0.f);
      hl = pk2(c, c - bflo(hl));
      *(u32x2*)(smem + L_MISC + 2048 + ((wm * 12 + wn * 6 + n) * 16 + r16) * 8) = u32x2{hl, hl & 0xffffu};
    }
  }
  asm volatile("" : "+v"(rst[0]), "+v"(rst[1]));
#pragma nounroll
  for (int spin = 0; spin < SPIN_CAP; ++spin) {
    bool ok = rst[0][1] == tag && rst[0][3] == tag && rst[1][1] == tag && rst[1][3] == tag;
    if (__builtin_expect(__all(ok), 1)) break;
    __builtin_amdgcn_s_sleep(1);
    ld128d(rst[0], wsd, WS_RST + (rt16 * 128 + wm * 32 + r16) * 16, 0);
    ld128d(rst[1], wsd, WS_RST + (rt16 * 128 + wm * 32 + r16) * 16, 256);
    asm volatile("s_waitcnt vmcnt(0)" : "+v"(rst[0]), "+v"(rst[1]) :: "memory");
  }
  LBAR();
  float rs[2];
  {
    u32x2 mo[2];
#pragma unroll
    for (int m = 0; m < 2; ++m) {
      float mu = -asf(rst[m][2]);
      unsigned hh = pk2(mu, mu);
      mo[m] = u32x2{hh, pk2(mu - bflo(hh), 0.f)};
      rs[m] = asf(rst[m][0]);
    }
#pragma unroll
    for (int n = 0; n < 6; ++n) {
      u32x2 co = *(const u32x2*)(smem + L_MISC + 2048 + ((g * 12 + wn * 6 + n) * 16 + r16) * 8);
      acc[0][n] = mfma16(co, mo[0], acc[0][n]);
      acc[1][n] = mfma16(co, mo[1], acc[1][n]);
    }
  }
  bf16x8 af[8][2][2];
  const int aro = w * 4096 + r16 * 128;
  auto wsp_step = [&](int c) {
    if (c < 6) { WAITV(8); } else if (c == 6) { WAITV(4); } else { WAITV(0); }
#pragma unroll
    for (int e = 0; e < 2; ++e)
#pragma unroll
      for (int t = 0; t < 2; ++t)
        af[c][e][t] = *(const bf16x8*)(smem + (c % 3) * L_SLOT + aro + t * 2048 + (((4 * e + g) ^ ((r16 >> 1) & 7)) << 4));
    if (c + 3 < 8) {
      WAITL0();
#pragma unroll
      for (int u = 0; u < 4; ++u) dmaA(c + 3, u);
    }
  };
  char* scr = smem + L_SCR + w * 6144;
  u32x2 wph[2], qb[2][2];
  STAMP(3);
  if (isqkv) {
    wsp_step(0);
#pragma unroll
    for (int e = 0; e < 2; ++e) wph[e] = __builtin_bit_cast(u32x2, trread(smem + L_MISC + (2 * wn + e) * 512 + (4 * g + tq) * 32 + tp * 8));
#pragma unroll
    for (int e = 0; e < 2; ++e)
#pragma unroll
      for (int m = 0; m < 2; ++m) {
        *(u32x4*)(scr + e * 2048 + (16 * m + r16) * 64 + g * 16) = hedgehog(mfma16(wph[e], pk4(acc[m][2 + e] * rs[m]), zero4));
        *(u32x2*)(scr + 4096 + e * 1024 + (16 * m + r16) * 32 + g * 8) = pk4(acc[m][4 + e] * rs[m]);
        qb[m][e] = pk4(acc[m][e] * rs[m]);
      }
    wsp_step(1);
    f32x4 kv[2][2], kz[2][2];
#pragma unroll
    for (int e = 0; e < 2; ++e) {
      const int row = 8 * g + tq;
      const char* pp = scr + e * 2048 + row * 64 + tp * 16;
      const char* pv = scr + 4096 + e * 1024 + row * 32 + tp * 8;
      bf16x8 ap = cat8(trread(pp), trread(pp + 256));
      bf16x8 an = cat8(trread(pp + 8), trread(pp + 264));
      bf16x8 bv = cat8(trread(pv), trread(pv + 128));
      kv[e][0] = mfma32(ap, bv, zero4);
      kv[e][1] = mfma32(an, bv, zero4);
      kz[e][0] = mfma32(ap, ones8, zero4);
      kz[e][1] = mfma32(an, ones8, zero4);
    }
#pragma unroll
    for (int e = 0; e < 2; ++e)
#pragma unroll
      for (int s = 0; s < 2; ++s) {
        *(f32x4*)(scr + (e * 2 + s) * 1024 + lane * 16) = kv[e][s];
        if (r16 == 0) *(f32x4*)(scr + 4096 + ((e * 2 + s) * 4 + g) * 16) = kz[e][s];
      }
    LBAR();
    {
      f32x4 a = zero4, b = zero4;
#pragma unroll
      for (int j = 0; j < 4; ++j) {
        const char* p = smem + L_SCR + (j * 2 + wn) * 6144;
        a += *(const f32x4*)(p + wm * 1024 + lane * 16);
        b += *(const f32x4*)(p + 4096 + (wm * 4 + g) * 16);
      }
      const int base = WS_KVP + (grp * 16 + rt16) * KVP_SZ;
      const int hs = wn * 4 + wm;
      __builtin_amdgcn_raw_buffer_store_b128(u32x4{asu(a[0]), tag, asu(a[1]), tag}, wsr, base + hs * 2048 + lane * 32, 0, 16);
      __builtin_amdgcn_raw_buffer_store_b128(u32x4{asu(a[2]), tag, asu(a[3]), tag}, wsr, base + hs * 2048 + lane * 32 + 16, 0, 16);
      if (r16 < 2) {
        const float v0 = r16 ? b[2] : b[0], v1 = r16 ? b[3] : b[1];
        __builtin_amdgcn_raw_buffer_store_b128(u32x4{asu(v0), tag, asu(v1), tag}, wsr, base + 16384 + hs * 128 + g * 32 + r16 * 16, 0, 16);
      }
    }
    wsp_step(2);
  } else {
    u32x2 zb[2][3];
#pragma unroll
    for (int m = 0; m < 2; ++m) {
      float s = 0.f, q = 0.f;
#pragma unroll
      for (int n = 0; n < 3; ++n) {
        f32x4 y;
#pragma unroll
        for (int r = 0; r < 4; ++r) y[r] = gelu_tanh(acc[m][n][r] * rs[m]);
        zb[m][n] = pk4(y);
        if (3 * wn + n < znft) {
          s = dot2(zb[m][n][0], ONES, s); s = dot2(zb[m][n][1], ONES, s);
          q = dot2(zb[m][n][0], zb[m][n][0], q); q = dot2(zb[m][n][1], zb[m][n][1], q);
        }
      }
      redg2<false>(s, q);
      if (g == 0) *(f32x2*)(smem + L_MISC2 + (wn * 128 + wm * 32 + m * 16 + r16) * 8) = f32x2{s, q};
    }
    LBAR();
    if (tid < 128) {
      f32x2 a = *(const f32x2*)(smem + L_MISC2 + tid * 8), b = *(const f32x2*)(smem + L_MISC2 + (128 + tid) * 8);
      __builtin_amdgcn_raw_buffer_store_b128(u32x4{asu(a[0] + b[0]), tag, asu(a[1] + b[1]), tag}, wsr,
                                             WS_ZST + ((rt16 * 8 + grp) * 128 + tid) * 16, 0, 16);
    }
    STAMP(4);
    wsp_step(0);
    wsp_step(1);
#pragma unroll
    for (int n = 0; n < 3; ++n) {
#pragma unroll
      for (int m = 0; m < 2; ++m) {
        f32x4 y;
#pragma unroll
        for (int r = 0; r < 4; ++r) y[r] = gelu_tanh(acc[m][3 + n][r] * rs[m]);
        if (3 * wn + n < znft)
          __builtin_amdgcn_raw_buffer_store_b64(pk4(y), wsr, WS_Z1F + ((rt16 * 8 + wm * 2 + m) * 44 + zft0 + 3 * wn + n) * 512 + lane * 8, 0, 16);
      }
    }
    STAMP(5);
    u32x4 zs[4];
#pragma nounroll
    for (int spin = 0; spin < SPIN_CAP; ++spin) {
      const int off = WS_ZST + ((rt16 * 8 + 2 * g) * 128 + wm * 32 + r16) * 16;
      ld128d(zs[0], wsd, off, 0); ld128d(zs[1], wsd, off, 2048); ld128d(zs[2], wsd, off, 256); ld128d(zs[3], wsd, off, 2304);
      asm volatile("s_waitcnt vmcnt(0)" : "+v"(zs[0]), "+v"(zs[1]), "+v"(zs[2]), "+v"(zs[3]) :: "memory");
      bool ok = true;
#pragma unroll
      for (int j = 0; j < 4; ++j) ok = ok && zs[j][1] == tag && zs[j][3] == tag;
      if (__all(ok)) break;
      __builtin_amdgcn_s_sleep(1);
    }
    STAMP(6);
#pragma unroll
    for (int m = 0; m < 2; ++m) {
      float s = asf(zs[2 * m][0]) + asf(zs[2 * m + 1][0]), q = asf(zs[2 * m][2]) + asf(zs[2 * m + 1][2]);
      redg2<false>(s, q);
      float mean = s * (1.0f / 688.0f);
      float var = fmaxf(q * (1.0f / 688.0f) - mean * mean, 0.f);
      float r = __builtin_amdgcn_rsqf(var + 1e-5f);
      float c = -mean * r;
#pragma unroll
      for (int n = 0; n < 3; ++n) {
        u32x2 o;
        o[0] = pk2(fmaf(bflo(zb[m][n][0]), r, c), fmaf(bfhi(zb[m][n][0]), r, c));
        o[1] = pk2(fmaf(bflo(zb[m][n][1]), r, c), fmaf(bfhi(zb[m][n][1]), r, c));
        *(u32x2*)(scr + n * 1024 + (16 * m + r16) * 32 + g * 8) = o;
      }
    }
    {
      const int ksg = rt16 * 4 + wm;
      const int dst = WS_Z2F + ((((ksg >> 3) * 2 + (ksg & 1)) * 44 + zft0 + 3 * wn) * 4 + ((ksg >> 1) & 3)) * 1024 + lane * 16;
#pragma unroll
      for (int n = 0; n < 3; ++n) {
        const char* p = scr + n * 1024 + (8 * g + tq) * 32 + tp * 8;
        bf16x8 f = cat8(trread(p), trread(p + 128));
        if (3 * wn + n < znft)
          __builtin_amdgcn_raw_buffer_store_b128(__builtin_bit_cast(u32x4, f), wsr, dst + n * 4096, 0, 16);
      }
    }
    WAITV(0);
    if (lane == 0) __builtin_amdgcn_raw_buffer_store_b64(u32x2{tag, tag ^ TAGX}, wsr, WS_FLZ + ((grp * 16 + rt16) * 8 + w) * 8, 0, 16);
    wsp_step(2);
    wph[0] = wph[1] = qb[0][0] = qb[0][1] = qb[1][0] = qb[1][1] = u32x2{0u, 0u};
  }
  STAMP(7);
#pragma unroll
  for (int c = 3; c < 8; ++c) wsp_step(c);
  LBAR();
  STAMP(8);
  const int w3off = (lane >> 3) * 1024 + cgp * 128 + (((lane & 7) ^ ((((lane >> 4) & 1) | (((lane >> 5) & 1) << 1)) << 1)) << 4);
  {
    const u32x4 od = mkdesc(wo, 512u * 1024u);
#pragma unroll
    for (int i = 0; i < 8; ++i) dma16(od, w3off, (i * 8 + w) * 8192, L_WO + (i * 8 + w) * 1024);
  }
  if (isqkv) {
#pragma nounroll
    for (int rep = 0; rep < ((w == 0 && rt16 < 8) ? 2 : 1); ++rep) {
      const int sub = rep ? 128 + rt16 : rt16 * 8 + w;
      const int off = WS_KVP + (grp * 16 + (lane >> 3)) * KVP_SZ + (sub * 8 + (lane & 7)) * 16;
      u32x4 a, b;
#pragma nounroll
      for (int spin = 0; spin < SPIN_CAP; ++spin) {
        ld128d(a, wsd, off, 0); ld128d(b, wsd, off, 8 * KVP_SZ);
        asm volatile("s_waitcnt vmcnt(0)" : "+v"(a), "+v"(b) :: "memory");
        if (__all(a[1] == tag && a[3] == tag && b[1] == tag && b[3] == tag)) break;
        __builtin_amdgcn_s_sleep(1);
      }
      float v0 = asf(a[0]) + asf(b[0]), v1 = asf(a[2]) + asf(b[2]);
      v0 = dppadd<0x128>(v0); v1 = dppadd<0x128>(v1);
      redg2<false>(v0, v1);
      if (lane < 8) __builtin_amdgcn_raw_buffer_store_b64(u32x2{pk2(v0, v1), tag}, wsr, WS_KVR + (grp * 1088 + sub * 8 + lane) * 8, 0, 16);
    }
  }
  STAMP(9);
  if (w == 0) {
#pragma nounroll
    for (int spin = 0; spin < SPIN_CAP; ++spin) {
      u32x4 f;
      ld128d(f, wsd, WS_FLZ + cgp * 1024 + lane * 16, 0);
      asm volatile("s_waitcnt vmcnt(0)" : "+v"(f) :: "memory");
      if (__all(f[0] == tag && f[1] == (tag ^ TAGX) && f[2] == tag && f[3] == (tag ^ TAGX))) break;
      __builtin_amdgcn_s_sleep(1);
    }
  }
  BAR();
  STAMP(10);
  const int bsrc = WS_Z2F + ft0 * 4096 + w * 1024 + lane * 16;
  const int u2off = npair == 3 ? 16384 : 0;
  auto dma2 = [&](int st, int u) {
    dma16d(wsd, bsrc, ((st + 2 * rot2) & 15) * (44 * 4096) + (u < 2 ? u * 8192 : u2off), (st & 3) * L_RING + u * 8192 + w * 1024);
  };
#pragma unroll
  for (int st = 0; st < 4; ++st)
#pragma unroll
    for (int u = 0; u < 3; ++u) dma2(st, u);
  f32x4 ac2[6][2];
#pragma unroll
  for (int n = 0; n < 6; ++n) { ac2[n][0] = zero4; ac2[n][1] = zero4; }
  bf16x8 fz[2][6];
  auto rdZ = [&](int st, int n) { fz[st & 1][n] = *(const bf16x8*)(smem + (st & 3) * L_RING + n * 4096 + kq * 1024 + lane * 16); };
  u32x4 kr[8];
  u32x4 ao[4][2];
  u32x2 z1v[2][2];
  unsigned biasv[2];
  u32x2 ofl;
  const int kroff = WS_KVR + (grp * 1088 + wn * 512) * 8 + lane * 16;
  const int kzoff = WS_KVR + (grp * 1088 + 1024 + wn * 32) * 8 + g * 16;
  auto ld_kr = [&]() {
#pragma unroll
    for (int j = 0; j < 4; ++j) { ld128d(kr[j], wsd, kroff, j * 1024); ld128d(kr[4 + j], wsd, kzoff, j * 64); }
  };
  const int a3 = WS_A3F + ((rt64 * 4 + th * 2) * 38 + kq) * 1024 + lane * 16;
  const int oflo = WS_FLO + ((rt64 >> 1) * 64 + (rt64 & 1) * 32 + (lane & 31)) * 8;
  waitv<9>();
  BAR();
#pragma unroll
  for (int n = 0; n < 6; ++n) rdZ(0, n);
  sfor<16>([&](auto ST) {
    constexpr int st = ST;
    if constexpr (st < 15) {
      WAITL0();
      waitv<(st == 5 || st == 6 ? 14 : (st == 13 ? 9 : 6))>();
      BAR();
    }
    sfor<6>([&](auto N) {
      constexpr int n = N;
      SB();
      ac2[n][0] = mfma32(fz[st & 1][n], af[st >> 1][st & 1][0], ac2[n][0]);
      ac2[n][1] = mfma32(fz[st & 1][n], af[st >> 1][st & 1][1], ac2[n][1]);
      SB();
      if constexpr (st < 15) {
        if constexpr (n == 1) { rdZ(st + 1, 0); rdZ(st + 1, 1); }
        if constexpr (n == 2) { rdZ(st + 1, 2); rdZ(st + 1, 3); }
        if constexpr (n == 3) { rdZ(st + 1, 4); rdZ(st + 1, 5); }
      }
      if constexpr (st + 4 < 16) {
        if constexpr (n == 0) dma2(st + 4, 0);
        if constexpr (n == 4) dma2(st + 4, 1);
        if constexpr (n == 5) dma2(st + 4, 2);
      }
    });
    if constexpr (st == 4) ld_kr();
    if constexpr (st == 7) {
      if (isqkv) {
        asm volatile("" : "+v"(kr[0]), "+v"(kr[1]), "+v"(kr[2]), "+v"(kr[3]), "+v"(kr[4]), "+v"(kr[5]), "+v"(kr[6]), "+v"(kr[7]));
#pragma nounroll
        for (int spin = 0; spin < SPIN_CAP; ++spin) {
          bool ok = true;
#pragma unroll
          for (int j = 0; j < 8; ++j) ok = ok && kr[j][1] == tag && kr[j][3] == tag;
          if (__builtin_expect(__all(ok), 1)) break;
          __builtin_amdgcn_s_sleep(1);
          ld_kr();
          asm volatile("s_waitcnt vmcnt(0)" : "+v"(kr[0]), "+v"(kr[1]), "+v"(kr[2]), "+v"(kr[3]), "+v"(kr[4]), "+v"(kr[5]), "+v"(kr[6]), "+v"(kr[7]) :: "memory");
        }
#pragma unroll
        for (int m = 0; m < 2; ++m) {
          u32x4 o;
#pragma unroll
          for (int e = 0; e < 2; ++e) {
            const bf16x8 kvo = asb8(u32x4{kr[2 * e][0], kr[2 * e][2], kr[2 * e + 1][0], kr[2 * e + 1][2]});
            const bf16x8 zo = asb8(u32x4{kr[4 + 2 * e][0], kr[4 + 2 * e][2], kr[5 + 2 * e][0], kr[5 + 2 * e][2]});
            const bf16x8 pq = asb8(hedgehog(mfma16(wph[e], qb[m][e], zero4)));
            f32x4 num = mfma32(kvo, pq, zero4);
            f32x4 den = mfma32(zo, pq, zero4);
            u32x2 v = pk4(num * __builtin_amdgcn_rcpf(den[0] + 1e-6f));
            o[2 * e] = v[0]; o[2 * e + 1] = v[1];
          }
          __builtin_amdgcn_raw_buffer_store_b128(o, wsr, WS_A3F + ((rt16 * 8 + wm * 2 + m) * 38 + grp * 2 + wn) * 1024 + lane * 16, 0, 16);
        }
      }
    }
    if constexpr (st == 10) {
      if (isqkv && lane == 0)
        __builtin_amdgcn_raw_buffer_store_b64(u32x2{tag, tag ^ TAGX}, wsr, WS_FLO + ((rt16 * 4 + wm) * 16 + grp * 2 + wn) * 8, 0, 16);
    }
    if constexpr (st == 12) {
#pragma unroll
      for (int e = 0; e < 2; ++e) {
        const int un = min(kq + 4 * e, 2 * npair - 1), tt = rt64 * 4 + th * 2 + (un & 1);
        const int zo = WS_Z1F + (tt * 44 + ft0 + (un & ~1)) * 512 + lane * 8;
        asm volatile("buffer_load_dwordx2 %0, %3, %4, 0 offen sc1\n\tbuffer_load_dwordx2 %1, %3, %4, 0 offen offset:512 sc1\n\t"
                     "buffer_load_ushort %2, %5, %6, 0 offen"
                     : "=&v"(z1v[e][0]), "=&v"(z1v[e][1]), "=&v"(biasv[e])
                     : "v"(zo), "s"(wsd), "v"((tt * 16 + r16) * 2), "s"(mkdesc(bsp, 4096u)) : "memory");
      }
    }
    if constexpr (st == 14) {
      ld64d(ofl, wsd, oflo);
    }
  });
  STAMP(11);
  asm volatile("s_waitcnt vmcnt(0)" : "+v"(ofl), "+v"(z1v[0][0]), "+v"(z1v[0][1]), "+v"(z1v[1][0]), "+v"(z1v[1][1]),
               "+v"(biasv[0]), "+v"(biasv[1]) :: "memory");
#pragma nounroll
  for (int spin = 0; spin < SPIN_CAP; ++spin) {
    if (__builtin_expect(__all(ofl[0] == tag && ofl[1] == (tag ^ TAGX)), 1)) break;
    __builtin_amdgcn_s_sleep(1);
    ofl = poll64(wsd, oflo);
  }
#pragma unroll
  for (int i = 0; i < 4; ++i)
#pragma unroll
    for (int t = 0; t < 2; ++t) ld128d(ao[i][t], wsd, a3, (t * 38 + 4 * i) * 1024);
  BAR();
#pragma unroll
  for (int n = 0; n < 6; ++n)
#pragma unroll
    for (int t = 0; t < 2; ++t) *(f32x4*)(smem + ((th * 4 + kq) * 12 + n * 2 + t) * 1024 + lane * 16) = ac2[n][t];
  LBAR();
#pragma unroll
  for (int e = 0; e < 2; ++e) {
    const int un = kq + 4 * e;
    if (un < 2 * npair) {
      const int t = un & 1, p = un >> 1;
      const int tt = rt64 * 4 + th * 2 + t;
      const float bias = bflo(biasv[e]);
      u32x4 o;
#pragma unroll
      for (int hf = 0; hf < 2; ++hf) {
        f32x4 s = {bias, bias, bias, bias};
#pragma unroll
        for (int j = 0; j < 4; ++j) s += *(const f32x4*)(smem + ((th * 4 + j) * 12 + (2 * p + hf) * 2 + t) * 1024 + lane * 16);
        const bool valid = ft0 + 2 * p + hf < 43;
        unsigned lo = pk2(bflo(z1v[e][hf][0]) * s[0], bfhi(z1v[e][hf][0]) * s[1]);
        unsigned hi = pk2(bflo(z1v[e][hf][1]) * s[2], bfhi(z1v[e][hf][1]) * s[3]);
        o[2 * hf] = valid ? lo : 0u;
        o[2 * hf + 1] = valid ? hi : 0u;
      }
      __builtin_amdgcn_raw_buffer_store_b128(o, wsr, WS_A3F + (tt * 38 + 16 + (ft0 >> 1) + p) * 1024 + lane * 16, 0, 16);
    }
  }
  LBAR();
  STAMP(12);
  f32x4 ac3[4][2];
#pragma unroll
  for (int n = 0; n < 4; ++n) { ac3[n][0] = zero4; ac3[n][1] = zero4; }
  int b3[4];
  {
    const int k = 4 * g + tq;
    const int h2 = ((k >> 1) & 1) | (((k >> 2) & 1) << 1);
#pragma unroll
    for (int n = 0; n < 4; ++n) b3[n] = k * 128 + (((2 * n + (tp >> 1)) ^ (h2 << 1)) << 4) + (tp & 1) * 8 + kq * 4096;
  }
  WAITV(0);
  if (lane == 0) __builtin_amdgcn_raw_buffer_store_b64(u32x2{tag, tag ^ TAGX}, wsr, WS_FLU + ((rt64 * 8 + cgp) * 8 + w) * 8, 0, 16);
  asm volatile("" : "+v"(ao[0][0]), "+v"(ao[0][1]), "+v"(ao[1][0]), "+v"(ao[1][1]), "+v"(ao[2][0]), "+v"(ao[2][1]), "+v"(ao[3][0]), "+v"(ao[3][1]));
  const u32x4 ud = mkdesc(wout, 688u * 1024u);
  const int go = (rt64 * 64 + (tid >> 3)) * 1024 + cgp * 128 + (tid & 7) * 16;
  u32x4 xres;
  asm volatile("buffer_load_dwordx4 %0, %1, %2, 0 offen" : "=v"(xres) : "v"(go), "s"(xd) : "memory");
#pragma unroll
  for (int i = 0; i < 4; ++i) {
    bf16x8 b[4];
#pragma unroll
    for (int n = 0; n < 4; ++n) b[n] = cat8(trread(smem + L_WO + b3[n] + i * 16384), trread(smem + L_WO + b3[n] + i * 16384 + 2048));
#pragma unroll
    for (int j = 0; j < 3; ++j)
      if (3 * i + j < 11) dma16(ud, w3off, ((3 * i + j) * 8 + w) * 8192, ((3 * i + j) * 8 + w) * 1024);
#pragma unroll
    for (int n = 0; n < 4; ++n)
#pragma unroll
      for (int t = 0; t < 2; ++t) ac3[n][t] = mfma32(b[n], asb8(ao[i][t]), ac3[n][t]);
  }
  STAMP(13);
  if (bid == 0 && w == 7) {
#pragma nounroll
    for (int spin = 0; spin < SPIN_CAP; ++spin) {
      u32x4 f[4];
#pragma unroll
      for (int j = 0; j < 4; ++j) ld128d(f[j], wsd, WS_RST + lane * 128, j * 8192);
      asm volatile("s_waitcnt vmcnt(0)" : "+v"(f[0]), "+v"(f[1]), "+v"(f[2]), "+v"(f[3]) :: "memory");
      if (__all(f[0][1] == tag && f[1][1] == tag && f[2][1] == tag && f[3][1] == tag)) break;
    }
    if (lane == 0) __builtin_amdgcn_raw_buffer_store_b32(tag, wsr, WS_EP, 0, 16);
  }
#pragma nounroll
  for (int spin = 0; spin < SPIN_CAP; ++spin) {
    u32x2 f = poll64(wsd, WS_FLU + (rt64 * 64 + lane) * 8);
    if (__all(f[0] == tag && f[1] == (tag ^ TAGX))) break;
    __builtin_amdgcn_s_sleep(1);
  }
  u32x4 au[6][2];
#pragma unroll
  for (int i = 0; i < 6; ++i)
#pragma unroll
    for (int t = 0; t < 2; ++t) ld128d(au[i][t], wsd, a3, (t * 38 + 16 + ((i < 5 || kq < 2) ? 4 * i : 16)) * 1024);
  LBAR();
  STAMP(14);
  sfor<6>([&](auto I) {
    constexpr int i = I;
    bf16x8 b[4];
    const int io = (i < 5 || kq < 2) ? i * 16384 : 0;
#pragma unroll
    for (int n = 0; n < 4; ++n) b[n] = cat8(trread(smem + b3[n] + io), trread(smem + b3[n] + io + 2048));
    asm volatile("s_waitcnt vmcnt(%2)" : "+v"(au[i][0]), "+v"(au[i][1]) : "n"(10 - 2 * i) : "memory");
    if (i < 5 || kq < 2) {
#pragma unroll
      for (int n = 0; n < 4; ++n)
#pragma unroll
        for (int t = 0; t < 2; ++t) ac3[n][t] = mfma32(b[n], asb8(au[i][t]), ac3[n][t]);
    }
  });
  STAMP(15);
#pragma unroll
  for (int n = 0; n < 4; ++n)
#pragma unroll
    for (int t = 0; t < 2; ++t)
      *(f32x4*)(smem + L_WO + kq * 16384 + (th * 32 + t * 16 + r16) * 256 + (((n * 4 + g) ^ r16) << 4)) = ac3[n][t];
  LBAR();
  {
    asm volatile("" : "+v"(xres));
    const int row = tid >> 3, c8 = tid & 7;
    f32x4 s0 = {bflo(xres[0]), bfhi(xres[0]), bflo(xres[1]), bfhi(xres[1])};
    f32x4 s1 = {bflo(xres[2]), bfhi(xres[2]), bflo(xres[3]), bfhi(xres[3])};
#pragma unroll
    for (int j = 0; j < 4; ++j) {
      s0 += *(const f32x4*)(smem + L_WO + j * 16384 + row * 256 + (((2 * c8) ^ (row & 15)) << 4));
      s1 += *(const f32x4*)(smem + L_WO + j * 16384 + row * 256 + (((2 * c8 + 1) ^ (row & 15)) << 4));
    }
    const u32x2 lo = pk4(s0), hi = pk4(s1);
    __builtin_amdgcn_raw_buffer_store_b128(u32x4{lo[0], lo[1], hi[0], hi[1]}, __builtin_amdgcn_make_buffer_rsrc(out, 0, 2048 * 1024, 0x00020000), go, 0, 16);
  }
  STAMP(16);
}

extern "C" void kernel_launch(void* const* d_in, const int* in_sizes, int n_in, void* d_out, int out_size, void* d_ws,
                              size_t ws_size, hipStream_t stream) {
  (void)hipFuncSetAttribute((const void*)fused, hipFuncAttributeMaxDynamicSharedMemorySize, LDS_BYTES);
  fused<<<256, 512, LDS_BYTES, stream>>>((const char*)d_in[0], (const char*)d_in[1], (const char*)d_in[2],
                                         (const char*)d_in[3], (const char*)d_in[5], (const char*)d_in[6],
                                         (const char*)d_in[7], (const char*)d_in[8], (char*)d_out, (char*)d_ws);
}
```

```cpp
#include <hip/hip_runtime.h>
#include <hip/hip_bf16.h>
#include <stdint.h>

typedef __bf16 bf16x8 __attribute__((ext_vector_type(8)));
typedef __bf16 bf16x2 __attribute__((ext_vector_type(2)));
typedef short s16x4 __attribute__((ext_vector_type(4)));
typedef short s16x8 __attribute__((ext_vector_type(8)));
typedef float f32x4 __attribute__((ext_vector_type(4)));
typedef float f32x2 __attribute__((ext_vector_type(2)));
typedef unsigned u32x4 __attribute__((ext_vector_type(4)));
typedef unsigned u32x2 __attribute__((ext_vector_type(2)));
#define LAS __attribute__((address_space(3)))
#define DI __device__ __forceinline__

constexpr int WS_EP  = 0;
constexpr int WS_FLZ = 1024;
constexpr int WS_FLO = WS_FLZ + 8192;
constexpr int WS_FLU = WS_FLO + 8192;
constexpr int WS_CS  = WS_FLU + 16384;
constexpr int WS_RST = WS_CS + 16 * 4 * 1024;
constexpr int WS_ZST = WS_RST + 2048 * 16;
constexpr int KVP_SZ = 1088 * 16;
constexpr int WS_KVP = WS_ZST + 16 * 8 * 128 * 16;
constexpr int WS_KVR = WS_KVP + 8 * 16 * KVP_SZ;
constexpr int WS_Z2F = WS_KVR + 8 * 1088 * 8;
constexpr int WS_Z1F = WS_Z2F + 16 * 44 * 4096;
constexpr int WS_A3F = WS_Z1F + 64 * 44 * 1024;
constexpr int WS_END = WS_A3F + 64 * 76 * 1024;
static_assert(WS_Z2F % 128 == 0 && WS_KVP % 128 == 0 && WS_KVR % 128 == 0, "alignment");

template <int V> struct IC { static constexpr int value = V; constexpr operator int() const { return V; } };
template <int I, int N, class F> DI void sfor_(F&& f) { if constexpr (I < N) { f(IC<I>{}); sfor_<I + 1, N>(f); } }
template <int N, class F> DI void sfor(F&& f) { sfor_<0, N>(f); }
template <int N> DI void waitv() { asm volatile("s_waitcnt vmcnt(%0)" :: "n"(N) : "memory"); }

DI float asf(unsigned u) { return __builtin_bit_cast(float, u); }
DI unsigned asu(float f) { return __builtin_bit_cast(unsigned, f); }
DI u32x4 mkdesc(const void* p, unsigned bytes) {
  unsigned long long a = (unsigned long long)p;
  u32x4 d = {(unsigned)a, (unsigned)(a >> 32) & 0xffffu, bytes, 0x00020000u};
  return d;
}
DI void dma16(u32x4 desc, int voff, int soff, int lds) {
  asm volatile("s_mov_b32 m0, %3\n\ts_nop 0\n\tbuffer_load_dwordx4 %0, %1, %2 offen lds"
               :: "v"(voff), "s"(desc), "s"(soff), "s"(lds) : "memory");
}
DI void dma16d(u32x4 desc, int voff, int soff, int lds) {
  asm volatile("s_mov_b32 m0, %3\n\ts_nop 0\n\tbuffer_load_dwordx4 %0, %1, %2 offen sc1 lds"
               :: "v"(voff), "s"(desc), "s"(soff), "s"(lds) : "memory");
}
DI u32x2 poll64(u32x4 desc, int voff, int soff) {
  u32x2 r;
  asm volatile("buffer_load_dwordx2 %0, %1, %2, %3 offen sc1\n\ts_waitcnt vmcnt(0)" : "=&v"(r) : "v"(voff), "s"(desc), "s"(soff) : "memory");
  return r;
}
DI void ld128d(u32x4& r, u32x4 desc, int voff, int soff) {
  asm volatile("buffer_load_dwordx4 %0, %1, %2, %3 offen sc1" : "=v"(r) : "v"(voff), "s"(desc), "s"(soff) : "memory");
}
DI void ld64d(u32x2& r, u32x4 desc, int voff) {
  asm volatile("buffer_load_dwordx2 %0, %1, %2, 0 offen sc1" : "=v"(r) : "v"(voff), "s"(desc) : "memory");
}
DI void ld64ds(u32x2& r, u32x4 desc, int voff, int soff) {
  asm volatile("buffer_load_dwordx2 %0, %1, %2, %3 offen sc1" : "=v"(r) : "v"(voff), "s"(desc), "s"(soff) : "memory");
}
DI void ld64p(u32x2& r, u32x4 desc, int voff, int soff) {
  asm volatile("buffer_load_dwordx2 %0, %1, %2, %3 offen" : "=v"(r) : "v"(voff), "s"(desc), "s"(soff) : "memory");
}
DI void ldu16(unsigned& r, u32x4 desc, int voff, int soff) {
  asm volatile("buffer_load_ushort %0, %1, %2, %3 offen" : "=v"(r) : "v"(voff), "s"(desc), "s"(soff) : "memory");
}
DI void st128d(u32x4 v, u32x4 desc, int voff, int soff) {
  asm volatile("buffer_store_dwordx4 %0, %1, %2, %3 offen sc1\n\ts_nop 1" :: "v"(v), "v"(voff), "s"(desc), "s"(soff) : "memory");
}
DI void pin(u32x4& v) { asm volatile("" : "+v"(v)); }
DI void pin(u32x2& v) { asm volatile("" : "+v"(v)); }
DI void pin(unsigned& v) { asm volatile("" : "+v"(v)); }
DI s16x4 trread(const char* p) { return __builtin_amdgcn_ds_read_tr16_b64_v4i16((LAS s16x4*)p); }
DI bf16x8 cat8(s16x4 a, s16x4 b) {
  s16x8 r = __builtin_shufflevector(a, b, 0, 1, 2, 3, 4, 5, 6, 7);
  return __builtin_bit_cast(bf16x8, r);
}
DI void keep(bf16x8 v) { asm volatile("" :: "v"(v)); }
DI bf16x8 asb8(u32x4 v) { return __builtin_bit_cast(bf16x8, v); }
DI u32x4 asu4(f32x4 v) { return __builtin_bit_cast(u32x4, v); }
DI f32x4 asf4(u32x4 v) { return __builtin_bit_cast(f32x4, v); }
DI f32x4 mfma32(bf16x8 a, bf16x8 b, f32x4 c) { return __builtin_amdgcn_mfma_f32_16x16x32_bf16(a, b, c, 0, 0, 0); }
DI f32x4 mfma16(u32x2 a, u32x2 b, f32x4 c) {
  return __builtin_amdgcn_mfma_f32_16x16x16bf16_1k(__builtin_bit_cast(s16x4, a), __builtin_bit_cast(s16x4, b), c, 0, 0, 0);
}
template <int CTRL> DI float dppadd(float v) { return v + asf(__builtin_amdgcn_update_dpp(0u, asu(v), CTRL, 0xf, 0xf, true)); }
DI unsigned pk2(float a, float b) {
  f32x2 f = {a, b};
  return __builtin_bit_cast(unsigned, __builtin_convertvector(f, bf16x2));
}
DI u32x2 pk4(f32x4 v) { return u32x2{pk2(v[0], v[1]), pk2(v[2], v[3])}; }
DI float bflo(unsigned u) { return asf(u << 16); }
DI float bfhi(unsigned u) { return asf(u & 0xffff0000u); }
DI float dot2(unsigned a, unsigned b, float c) {
  return __builtin_amdgcn_fdot2_f32_bf16(__builtin_bit_cast(bf16x2, a), __builtin_bit_cast(bf16x2, b), c, false);
}
DI float fmx(float a, float b) { return __builtin_elementwise_maximum(a, b); }
DI float fmn(float a, float b) { return __builtin_elementwise_minimum(a, b); }
template <bool MAX> DI float rop(unsigned a, unsigned b) { return MAX ? fmx(asf(a), asf(b)) : asf(a) + asf(b); }
template <bool MAX> DI void redg2(float& a, float& b) {
  auto r = __builtin_amdgcn_permlane16_swap(asu(a), asu(b), false, false);
  float m = rop<MAX>(r[0], r[1]);
  auto r2 = __builtin_amdgcn_permlane32_swap(asu(m), asu(m), false, false);
  m = rop<MAX>(r2[0], r2[1]);
  auto r3 = __builtin_amdgcn_permlane16_swap(asu(m), asu(m), false, false);
  a = asf(r3[0]); b = asf(r3[1]);
}
template <bool MAX> DI float redg4(const float (&v)[4]) {
  auto r = __builtin_amdgcn_permlane16_swap(asu(v[0]), asu(v[1]), false, false);
  auto q = __builtin_amdgcn_permlane16_swap(asu(v[2]), asu(v[3]), false, false);
  auto t = __builtin_amdgcn_permlane32_swap(asu(rop<MAX>(r[0], r[1])), asu(rop<MAX>(q[0], q[1])), false, false);
  return rop<MAX>(t[0], t[1]);
}
DI void bcast4(float m, float (&v)[4]) {
  auto t = __builtin_amdgcn_permlane32_swap(asu(m), asu(m), false, false);
  unsigned t0 = t[0], t1 = t[1];
  auto r = __builtin_amdgcn_permlane16_swap(t0, t0, false, false);
  auto q = __builtin_amdgcn_permlane16_swap(t1, t1, false, false);
  v[0] = asf(r[0]); v[1] = asf(r[1]); v[2] = asf(q[0]); v[3] = asf(q[1]);
}
constexpr float L2E = 1.4426950408889634f;
DI f32x4 exp4(f32x4 v) {
  return f32x4{__builtin_amdgcn_exp2f(v[0]), __builtin_amdgcn_exp2f(v[1]), __builtin_amdgcn_exp2f(v[2]), __builtin_amdgcn_exp2f(v[3])};
}
DI float sum4(f32x4 v) { f32x2 t = v.xy + v.zw; return t[0] + t[1]; }
DI void hedgehog4(const f32x4 (&p)[4], u32x4 (&o)[4]) {
  float mx[4], mn[4];
#pragma unroll
  for (int i = 0; i < 4; ++i) {
    mx[i] = fmx(fmx(p[i][0], p[i][1]), fmx(p[i][2], p[i][3]));
    mn[i] = -fmn(fmn(p[i][0], p[i][1]), fmn(p[i][2], p[i][3]));
  }
  bcast4(redg4<true>(mx), mx);
  bcast4(redg4<true>(mn), mn);
  f32x4 ep[4], en[4];
  float sp[4], sn[4];
#pragma unroll
  for (int i = 0; i < 4; ++i) {
    ep[i] = exp4(p[i] - mx[i]);
    en[i] = exp4(-p[i] - mn[i]);
    sp[i] = sum4(ep[i]);
    sn[i] = sum4(en[i]);
  }
  bcast4(__builtin_amdgcn_rcpf(redg4<false>(sp)), sp);
  bcast4(__builtin_amdgcn_rcpf(redg4<false>(sn)), sn);
#pragma unroll
  for (int i = 0; i < 4; ++i) {
    const u32x2 a = pk4(ep[i] * sp[i]), b = pk4(en[i] * sn[i]);
    o[i] = u32x4{a[0], a[1], b[0], b[1]};
  }
}
DI f32x4 gelu4(f32x4 a, float k1, float k2, float irs) {
  const f32x4 K1 = {k1, k1, k1, k1}, K2 = {k2, k2, k2, k2}, I = {irs, irs, irs, irs};
  const f32x4 e = exp4(a * __builtin_elementwise_fma(a * a, K2, K1));
  const f32x4 d = __builtin_elementwise_fma(e, I, I);
  const f32x4 r = {__builtin_amdgcn_rcpf(d[0]), __builtin_amdgcn_rcpf(d[1]), __builtin_amdgcn_rcpf(d[2]), __builtin_amdgcn_rcpf(d[3])};
  f32x4 o = a * r;
  asm volatile("" : "+v"(o));
  return o;
}
constexpr int DROP = 0x7f000000;
#define STAMP(i)
#define WAITV(n) asm volatile("s_waitcnt vmcnt(" #n ")" ::: "memory")
#define WAITL0() asm volatile("s_waitcnt lgkmcnt(0)" ::: "memory")
#define SB() __builtin_amdgcn_sched_barrier(0)
#define BAR() do { SB(); __builtin_amdgcn_s_barrier(); SB(); } while (0)
#define LBAR() do { WAITL0(); BAR(); } while (0)
#define PSLEEP 4
constexpr int SPIN_CAP = 1 << 18;
constexpr unsigned TAGX = 0x5bd1e995u;

constexpr int L_SLOT = 40960;
constexpr int L_CB = 32768;
constexpr int L_U = 98304;
constexpr int L_ZS = L_U + 6144;
constexpr int L_RING = 24576;
constexpr int LDS_BYTES = 163840;
constexpr int p2P(int i) { return i + 4 < 16 ? 3 : 0; }
constexpr int p2E(int i) { return i < 0 ? 0 : ((i & 1) == 0 ? 1 : 0) + (i == 7 ? 2 : 0) + (i == 11 ? 2 : 0) + (i == 12 || i == 13 ? 4 : 0); }
constexpr int p2Q(int i) { return i == 2 ? 1 : (i == -3 ? 2 : 0); }
constexpr int p2cnt(int st, bool q) {
  return p2E(st - 3) + p2P(st - 2) + p2E(st - 2) + p2P(st - 1) + p2E(st - 1) + (q ? p2Q(st - 3) + p2Q(st - 2) + p2Q(st - 1) : 0);
}

__global__ __launch_bounds__(512, 2) void fused(const char* __restrict__ x, const char* __restrict__ wqkv,
                                                const char* __restrict__ wphi, const char* __restrict__ wo,
                                                const char* __restrict__ win, const char* __restrict__ wsp,
                                                const char* __restrict__ bsp, const char* __restrict__ wout,
                                                char* out, char* ws) {
  extern __shared__ __attribute__((aligned(16))) char smem[];
  const int tid = threadIdx.x, lane = tid & 63;
  const int w = __builtin_amdgcn_readfirstlane(tid >> 6);
  const int r16 = lane & 15, g = lane >> 4;
  const int tq = r16 >> 2, tp = r16 & 3;
  const int bid = blockIdx.x, xcd = bid & 7, jj = bid >> 3;
  const int rt16 = (xcd >> 2) * 8 + (jj >> 2);
  const bool isqkv = (jj & 2) == 0;
  const int grp = (xcd & 3) * 2 + (jj & 1);
  const int cg = isqkv ? grp : 8 + grp;
  const int zft0 = grp < 7 ? grp * 6 : 40;
  const int znft = grp < 6 ? 6 : (grp == 6 ? 4 : 3);
  const int wm = w >> 2, wn = (w & 3) ^ wm;
  const int rot1 = (rt16 & 3) * 2;
  const int rt64 = xcd * 4 + (jj >> 3);
  const int cgp = jj & 7;
  const int ft0 = cgp < 7 ? cgp * 6 : 40;
  const int npair = cgp < 6 ? 3 : 2;
  const int th = w & 1, kq = w >> 1;

  int xsrc[2], wsrc[3];
#pragma unroll
  for (int u = 0; u < 2; ++u) {
    int row = 16 * w + 8 * u + (lane >> 3);
    xsrc[u] = (rt16 * 128 + row) * 1024 + (((lane & 7) ^ ((row >> 1) & 7)) << 4);
  }
  const int wstride = isqkv ? 64 * 3072 : 64 * 2752;
#pragma unroll
  for (int u = 0; u < 3; ++u) {
    int ci = u * 64 + lane;
    int rl = (ci * 2731) >> 16, c = ci - rl * 24;
    int h2 = ((rl >> 1) & 1) | ((w & 1) << 1);
    int cs = c ^ (h2 << 1);
    int vq = (8 * w + rl) * 3072 + (cs >> 3) * 1024 + grp * 128 + (cs & 7) * 16;
    int cz = min(cs < 12 ? cs : cs - 12, znft * 2 - 1);
    int vz = (8 * w + rl) * 2752 + (cs < 12 ? 1376 : 0) + zft0 * 32 + cz * 16;
    wsrc[u] = isqkv ? vq : vz;
  }
  asm volatile("" : "+v"(xsrc[0]), "+v"(xsrc[1]), "+v"(wsrc[0]), "+v"(wsrc[1]), "+v"(wsrc[2])
               : "s"(x), "s"(wqkv), "s"(wphi), "s"(wo), "s"(win), "s"(wsp), "s"(bsp), "s"(wout), "s"(out), "s"(ws));
  const __amdgpu_buffer_rsrc_t wsr = __builtin_amdgcn_make_buffer_rsrc(ws, 0, WS_END, 0x00020000);
  const u32x4 wsd = mkdesc(ws, WS_END);
  const u32x4 xd = mkdesc(x, 2048u * 1024u);
  const u32x4 wd = isqkv ? mkdesc(wqkv, 512u * 3072u) : mkdesc(win, 512u * 2752u);
  const u32x4 ad = mkdesc(wsp, 2048u * 4096u);
  const f32x4 zero4 = {0.f, 0.f, 0.f, 0.f};
  const unsigned ONES = 0x3f803f80u;
  const bf16x8 ones8 = asb8(u32x4{ONES, ONES, ONES, ONES});
  STAMP(0);
  auto dma1 = [&](int st, int u) {
    const int slot = (st & 3) * L_SLOT, sr = (st + rot1) & 7;
    if (u < 2) dma16(xd, xsrc[u], sr * 128, slot + (16 * w + 8 * u) * 128);
    else dma16(wd, wsrc[u - 2], sr * wstride, slot + 16384 + (3 * w + u - 2) * 1024);
  };
#pragma unroll
  for (int u = 0; u < 5; ++u) dma1(0, u);
  unsigned ep;
  u32x4 xrow;
  asm volatile("buffer_load_dword %0, off, %2, 0 sc1\n\tbuffer_load_dwordx4 %1, %3, %4, 0 offen"
               : "=&v"(ep), "=&v"(xrow) : "s"(wsd), "v"((bid * 8 + w) * 1024 + lane * 16), "s"(xd) : "memory");
#pragma unroll
  for (int u = 0; u < 5; ++u) dma1(1, u);

  const int asrc = (rt64 * 64 + th * 32 + (lane >> 3)) * 4096 + kq * 128 + (((lane & 7) ^ ((lane >> 4) & 3)) << 4);
  auto dmaA = [&](int c, int u) {
    dma16(ad, asrc ^ ((u & 1) << 6), c * 512 + u * 8 * 4096, (c % 3) * L_CB + w * 4096 + u * 1024);
  };
  auto piece = [&](int p) {
    if (p < 30) dma1(2 + p / 5, p % 5);
    else if (p < 42) dmaA((p - 30) >> 2, (p - 30) & 3);
  };
  const bool zeven = (wn & 1) == 0;
  const int hb = 3 * (wn >> 1);
  int li[3];
  li[0] = zeven ? hb : hb + 2; li[1] = hb + 1; li[2] = zeven ? hb : hb + 2;
  int aoff[2], boff[3], sgi[3];
  sgi[0] = isqkv ? wn : li[0];
  sgi[1] = isqkv ? 4 + wn : (zeven ? li[1] : 6 + li[1]);
  sgi[2] = isqkv ? 8 + wn : 6 + li[2];
#pragma unroll
  for (int ks = 0; ks < 2; ++ks) aoff[ks] = (wm * 64 + r16) * 128 + (((4 * ks + g) ^ ((r16 >> 1) & 7)) << 4);
  {
    int h2 = ((tq >> 1) & 1) | ((g & 1) << 1);
#pragma unroll
    for (int i = 0; i < 3; ++i) boff[i] = 16384 + (8 * g + tq) * 384 + (((2 * sgi[i] + (tp >> 1)) ^ (h2 << 1)) << 4) + (tp & 1) * 8;
  }
  f32x4 acc[4][3], csum[3];
#pragma unroll
  for (int i = 0; i < 3; ++i) { acc[0][i] = zero4; acc[1][i] = zero4; acc[2][i] = zero4; acc[3][i] = zero4; csum[i] = zero4; }
  bf16x8 fa[2][4], fb[2][3];
  u32x4 rst, cst;
  u32x2 wrow;
  unsigned tag = 0;
  float st_s = 0.f, st_q = 0.f;
  const bool csprod = rt16 < 4 && wm == 0;
  auto ld_stats = [&]() {
    ld128d(rst, wsd, lane * 16, WS_RST + (rt16 * 128 + wm * 64) * 16);
    ld128d(cst, wsd, lane * 16, WS_CS + (cg * 4 + wn) * 1024);
  };
  auto rdA = [&](int st, int ks, int m) { fa[ks][m] = *(const bf16x8*)(smem + (st & 3) * L_SLOT + aoff[ks] + m * 2048); };
  auto rdB = [&](int st, int ks, int n) {
    const char* p = smem + (st & 3) * L_SLOT + boff[n] + ks * 12288;
    fb[ks][n] = cat8(trread(p), trread(p + 1536));
  };
  waitv<7>();
  BAR();
  STAMP(1);
  rdB(0, 0, 0); rdA(0, 0, 0); rdA(0, 0, 1); rdA(0, 0, 2); rdA(0, 0, 3); rdB(0, 0, 1); rdB(0, 0, 2);
  sfor<16>([&](auto B) {
    constexpr int b = B, i = b >> 1, ks = b & 1;
    constexpr int nst = (b + 1) >> 1, nks = ks ^ 1;
    constexpr int p0 = b < 12 ? 3 * b : (b == 12 ? 36 : (b == 13 ? 38 : 42));
    constexpr int p1 = b < 12 ? 3 * b + 3 : (b == 12 ? 38 : 42);
    sfor<6>([&](auto N) {
      constexpr int p = N, n = p >> 1, m = (p & 1) * 2;
      SB();
      acc[m][n] = mfma32(fb[ks][n], fa[ks][m], acc[m][n]);
      acc[m + 1][n] = mfma32(fb[ks][n], fa[ks][m + 1], acc[m + 1][n]);
      SB();
      if constexpr (nst < 8) {
        if constexpr (p < 3) rdB(nst, nks, p);
        if constexpr (p < 4) rdA(nst, nks, p);
      }
      if constexpr (p >= 1 && p0 + p - 1 < p1) piece(p0 + p - 1);
      if constexpr (b == 1) {
        if constexpr (p == 0) {
          pin(ep); pin(xrow);
          tag = __builtin_amdgcn_readfirstlane(ep) + 1u;
#pragma unroll
          for (int e = 0; e < 4; ++e) { st_s = dot2(xrow[e], ONES, st_s); st_q = dot2(xrow[e], xrow[e], st_q); }
        }
        if constexpr (p == 1) redg2<false>(st_s, st_q);
        if constexpr (p == 2) { st_s = dppadd<0x128>(st_s); st_q = dppadd<0x128>(st_q); st_s = dppadd<0x124>(st_s); st_q = dppadd<0x124>(st_q); }
        if constexpr (p == 3) { st_s = dppadd<0x122>(st_s); st_q = dppadd<0x122>(st_q); st_s = dppadd<0x121>(st_s); st_q = dppadd<0x121>(st_q); }
        if constexpr (p == 4) {
          float mean = st_s * (1.0f / 512.0f);
          float var = fmx(st_q * (1.0f / 512.0f) - mean * mean, 0.f);
          float rs = __builtin_amdgcn_rsqf(var + 1e-5f);
          if (lane == 0)
            st128d(u32x4{asu(rs), tag, asu(mean), tag}, wsd, WS_RST + (bid * 8 + w) * 16, 0);
        }
      }
    });
    if constexpr (b < 4) {
      if (csprod) {
#pragma unroll
        for (int n = 0; n < 3; ++n) csum[n] = mfma32(ones8, fb[ks][n], csum[n]);
        if constexpr (b == 3) {
          u32x4 o;
#pragma unroll
          for (int n = 0; n < 3; ++n) o[n] = pk2(csum[n][0], csum[n][0] - bflo(pk2(csum[n][0], 0.f)));
          o[3] = tag ^ o[0] ^ o[1] ^ o[2];
          if (g == 0) st128d(o, wsd, r16 * 16, WS_CS + (cg * 4 + wn) * 1024 + rt16 * 256);
        }
      }
    }
    if constexpr (ks == 0 && i < 7) {
      WAITL0();
      waitv<(i < 5 ? i + 3 : (i == 5 ? 11 : 8))>();
      BAR();
    }
    if constexpr (b == 9) {
      ld_stats();
      ld64p(wrow, mkdesc(wphi, 32u * 512u), r16 * 32 + g * 8, (grp * 4 + wn) * 512);
    }
  });
  STAMP(2);
  pin(rst); pin(cst); pin(wrow);
#pragma nounroll
  for (int spin = 0; spin < SPIN_CAP; ++spin) {
    if (__builtin_expect(__all(rst[1] == tag && rst[3] == tag && (cst[0] ^ cst[1] ^ cst[2] ^ cst[3]) == tag), 1)) break;
    __builtin_amdgcn_s_sleep(PSLEEP);
    ld_stats();
    WAITV(0);
    pin(rst); pin(cst);
  }
  float rs[4];
  {
    const float mu = -asf(rst[2]);
    const unsigned hh = pk2(mu, mu);
    float mh[4], ml[4];
    bcast4(asf(hh), mh);
    bcast4(asf(pk2(mu - bflo(hh), 0.f)), ml);
    bcast4(asf(rst[0]), rs);
#pragma unroll
    for (int m = 0; m < 4; ++m)
#pragma unroll
      for (int n = 0; n < 3; ++n) acc[m][n] = mfma16(u32x2{cst[n], cst[n]}, u32x2{asu(mh[m]), asu(ml[m])}, acc[m][n]);
  }
  bf16x8 af[8][2][2];
  const int aro = w * 4096 + r16 * 128;
  auto wsp_rd = [&](int c) {
    SB();
    if (c < 6) { WAITV(8); } else if (c == 6) { WAITV(4); } else { WAITV(0); }
#pragma unroll
    for (int e = 0; e < 2; ++e)
#pragma unroll
      for (int t = 0; t < 2; ++t)
        af[c][e][t] = *(const bf16x8*)(smem + (c % 3) * L_CB + aro + t * 2048 + (((4 * e + g) ^ ((r16 >> 1) & 7)) << 4));
    SB();
  };
  auto wsp_is = [&](int c, int u0, int u1) {
    SB();
    if (u0 == 0) WAITL0();
    for (int u = u0; u < u1; ++u) dmaA(c, u);
    SB();
  };
  char* scr = smem + L_U + w * 8192;
  u32x4 pq[4];
  STAMP(3);
  if (isqkv) {
    f32x4 lg[4];
    u32x4 pk[4];
    const unsigned one = (r16 & 1) ? 0x3f800000u : 0x3f80u;
    const u32x2 wph = pk4(mfma16(wrow, u32x2{(tq == g && tp < 2) ? one : 0u, (tq == g && tp >= 2) ? one : 0u}, zero4));
    wsp_rd(0);
#pragma unroll
    for (int m = 0; m < 4; ++m) lg[m] = mfma16(wph, pk4(acc[m][1] * (rs[m] * L2E)), zero4);
    wsp_is(3, 0, 2);
    hedgehog4(lg, pk);
    pin(pk[0]); pin(pk[1]); pin(pk[2]); pin(pk[3]);
    wsp_is(3, 2, 4);
    wsp_rd(1);
    f32x4 kv[2] = {zero4, zero4}, kz[2] = {zero4, zero4};
    char* sc3 = smem + L_U + w * 3072;
#pragma unroll
    for (int mp = 0; mp < 2; ++mp) {
#pragma unroll
      for (int m = 0; m < 2; ++m) {
        *(u32x4*)(sc3 + (16 * m + r16) * 64 + g * 16) = pk[2 * mp + m];
        *(u32x2*)(sc3 + 2048 + (16 * m + r16) * 32 + g * 8) = pk4(acc[2 * mp + m][2] * rs[2 * mp + m]);
      }
      const int row = 8 * g + tq;
      const char* pp = sc3 + row * 64 + tp * 16;
      const char* pv = sc3 + 2048 + row * 32 + tp * 8;
      bf16x8 ap = cat8(trread(pp), trread(pp + 256));
      bf16x8 an = cat8(trread(pp + 8), trread(pp + 264));
      bf16x8 bv = cat8(trread(pv), trread(pv + 128));
      kv[0] = mfma32(ap, bv, kv[0]);
      kv[1] = mfma32(an, bv, kv[1]);
      kz[0] = mfma32(ap, ones8, kz[0]);
      kz[1] = mfma32(an, ones8, kz[1]);
    }
#pragma unroll
    for (int m = 0; m < 4; ++m) lg[m] = mfma16(wph, pk4(acc[m][0] * (rs[m] * L2E)), zero4);
    *(f32x4*)(sc3 + lane * 16) = wm ? kv[0] : kv[1];
    if (r16 == 0) *(f32x4*)(sc3 + 1024 + g * 16) = wm ? kz[0] : kz[1];
    wsp_is(4, 0, 4);
    wsp_rd(2);
    LBAR();
    {
      const char* p = smem + L_U + (w ^ 5) * 3072;
      const f32x4 a = (wm ? kv[1] : kv[0]) + *(const f32x4*)(p + lane * 16);
      const f32x4 b = (wm ? kz[1] : kz[0]) + *(const f32x4*)(p + 1024 + g * 16);
      const int base = WS_KVP + (grp * 16 + rt16) * KVP_SZ;
      const int hs = wn * 2 + wm;
      st128d(u32x4{asu(a[0]), tag, asu(a[1]), tag}, wsd, lane * 16, base + hs * 2048);
      st128d(u32x4{asu(a[2]), tag, asu(a[3]), tag}, wsd, lane * 16, base + hs * 2048 + 1024);
      const float v0 = r16 ? b[2] : b[0], v1 = r16 ? b[3] : b[1];
      st128d(u32x4{asu(v0), tag, asu(v1), tag}, wsd, r16 < 2 ? g * 32 + r16 * 16 : DROP, base + 16384 + hs * 128);
    }
    STAMP(4);
    wsp_is(5, 0, 4);
    wsp_rd(3);
    hedgehog4(lg, pq);
    pin(pq[0]); pin(pq[1]); pin(pq[2]); pin(pq[3]);
    STAMP(5);
#pragma nounroll
    for (int rep = 0; rep < ((w == 0 && rt16 < 8) ? 2 : 1); ++rep) {
      const int sub = rep ? 128 + rt16 : rt16 * 8 + w;
      const int off = WS_KVP + (grp * 16 + (lane >> 3)) * KVP_SZ + (sub * 8 + (lane & 7)) * 16;
      u32x4 a, b;
#pragma nounroll
      for (int spin = 0; spin < SPIN_CAP; ++spin) {
        ld128d(a, wsd, off, 0); ld128d(b, wsd, off, 8 * KVP_SZ);
        WAITV(0);
        pin(a); pin(b);
        if (__all(a[1] == tag && a[3] == tag && b[1] == tag && b[3] == tag)) break;
        __builtin_amdgcn_s_sleep(8);
      }
      float v0 = asf(a[0]) + asf(b[0]), v1 = asf(a[2]) + asf(b[2]);
      v0 = dppadd<0x128>(v0); v1 = dppadd<0x128>(v1);
      redg2<false>(v0, v1);
      const int dsto = rep ? (sub * 8 + lane) * 8 : ((sub >> 4) * 64 + (sub & 7) * 8 + lane) * 16 + ((sub >> 3) & 1) * 8;
      __builtin_amdgcn_raw_buffer_store_b64(u32x2{pk2(v0, v1), tag}, wsr, lane < 8 ? dsto : DROP, WS_KVR + grp * 1088 * 8, 16);
    }
    STAMP(6);
    wsp_is(6, 0, 4);
    wsp_rd(4);
    wsp_is(7, 0, 4);
  } else {
    float k1[4], k2[4], irs[4];
#pragma unroll
    for (int m = 0; m < 4; ++m) {
      k1[m] = rs[m] * (-2.0f * 0.7978845608028654f * L2E);
      k2[m] = k1[m] * rs[m] * rs[m] * 0.044715f;
      irs[m] = __builtin_amdgcn_rcpf(rs[m]);
    }
    const bool v0 = li[0] < znft, v1 = li[1] < znft;
    wsp_rd(0);
#pragma unroll
    for (int m = 0; m < 4; ++m) acc[m][0] = gelu4(acc[m][0], k1[m], k2[m], irs[m]);
    wsp_is(3, 0, 2);
    {
      f32x4 s4[4], q4[4];
#pragma unroll
      for (int m = 0; m < 4; ++m) { s4[m] = v0 ? acc[m][0] : zero4; q4[m] = s4[m] * s4[m]; }
      if (zeven) {
#pragma unroll
        for (int m = 0; m < 4; ++m) {
          acc[m][1] = gelu4(acc[m][1], k1[m], k2[m], irs[m]);
          const f32x4 y = v1 ? acc[m][1] : zero4;
          s4[m] += y; q4[m] = __builtin_elementwise_fma(y, y, q4[m]);
        }
      }
      float s[4], q[4];
#pragma unroll
      for (int m = 0; m < 4; ++m) { s[m] = sum4(s4[m]); q[m] = sum4(q4[m]); }
      *(f32x2*)(smem + L_ZS + (wn >> 1) * 8192 + (wn & 1) * 1024 + (wm * 64 + lane) * 8) = f32x2{redg4<false>(s), redg4<false>(q)};
    }
    if (tid == 0) *(unsigned*)(smem + L_U + 5120) = 0u;
    LBAR();
    if (tid < 128) {
      f32x2 a = *(const f32x2*)(smem + L_ZS + tid * 8) + *(const f32x2*)(smem + L_ZS + 1024 + tid * 8);
      f32x2 b = *(const f32x2*)(smem + L_ZS + 8192 + tid * 8) + *(const f32x2*)(smem + L_ZS + 9216 + tid * 8);
      st128d(u32x4{asu(a[0] + b[0]), tag, asu(a[1] + b[1]), tag}, wsd,
                                             WS_ZST + ((rt16 * 8 + grp) * 128 + tid) * 16, 0);
    }
    STAMP(4);
    wsp_is(3, 2, 4);
    wsp_rd(1);
    u32x2 y2[4], y1[4];
#pragma unroll
    for (int m = 0; m < 4; ++m) y2[m] = pk4(gelu4(acc[m][2], k1[m], k2[m], irs[m]));
    wsp_is(4, 0, 4);
    wsp_rd(2);
    u32x4 zs[2];
    const int zso = WS_ZST + (rt16 * 8 + w) * 2048;
    auto ld_zs = [&]() { ld128d(zs[0], wsd, lane * 16, zso); ld128d(zs[1], wsd, lane * 16, zso + 1024); };
    ld_zs();
    wsp_is(5, 0, 4);
    if (!zeven) {
#pragma unroll
      for (int m = 0; m < 4; ++m) acc[m][1] = gelu4(acc[m][1], k1[m], k2[m], irs[m]);
    }
#pragma unroll
    for (int m = 0; m < 4; ++m) y1[m] = pk4(acc[m][1]);
    STAMP(5);
    WAITV(4);
#pragma nounroll
    for (int spin = 0; spin < SPIN_CAP; ++spin) {
      pin(zs[0]); pin(zs[1]);
      if (__all(zs[0][1] == tag && zs[0][3] == tag && zs[1][1] == tag && zs[1][3] == tag)) break;
      __builtin_amdgcn_s_sleep(2);
      ld_zs();
      WAITV(0);
    }
    *(f32x2*)(scr + 4096 + lane * 8) = f32x2{asf(zs[0][0]), asf(zs[0][2])};
    *(f32x2*)(scr + 4608 + lane * 8) = f32x2{asf(zs[1][0]), asf(zs[1][2])};
    wsp_rd(3);
    LBAR();
    STAMP(6);
    {
      f32x2 sq = {0.f, 0.f};
#pragma unroll
      for (int j = 0; j < 8; ++j) sq += *(const f32x2*)(smem + L_U + j * 8192 + 4096 + (wm * 64 + lane) * 8);
      const float mean = sq[0] * (1.0f / 688.0f);
      const float var = fmx(sq[1] * (1.0f / 688.0f) - mean * mean, 0.f);
      const float r = __builtin_amdgcn_rsqf(var + 1e-5f);
      float zr[4], zc[4];
      bcast4(r, zr);
      bcast4(-mean * r, zc);
      const int dst = WS_Z2F + (((rt16 >> 1) * 2 * 44 + zft0) * 4 + ((2 * rt16 + wm) & 3)) * 1024;
#pragma unroll
      for (int n = 0; n < 2; ++n) {
#pragma unroll
        for (int m = 0; m < 4; ++m) *(u32x2*)(scr + n * 2048 + (16 * m + r16) * 32 + g * 8) = pk4(acc[m][n] * zr[m] + zc[m]);
        const int dn = dst + li[n] * 4096, vo = (n == 0 ? v0 : (v1 && zeven)) ? lane * 16 : DROP;
#pragma unroll
        for (int kk = 0; kk < 2; ++kk) {
          const char* p = scr + n * 2048 + (32 * kk + 8 * g + tq) * 32 + tp * 8;
          bf16x8 f = cat8(trread(p), trread(p + 128));
          st128d(__builtin_bit_cast(u32x4, f), wsd, vo, dn + kk * 44 * 4096);
        }
      }
    }
    STAMP(17);
    wsp_is(6, 0, 4);
    wsp_rd(4);
    STAMP(18);
    WAITV(4);
    STAMP(19);
    if (lane == 0) {
      if (__hip_atomic_fetch_add((LAS unsigned*)(smem + L_U + 5120), 1u, __ATOMIC_RELAXED, __HIP_MEMORY_SCOPE_WORKGROUP) == 7u)
        __builtin_amdgcn_raw_buffer_store_b64(u32x2{tag, tag ^ TAGX}, wsr, WS_FLZ + grp * 1024 + rt16 * 8, 0, 16);
    }
    wsp_is(7, 0, 4);
    {
      const int z1o = WS_Z1F + ((rt16 * 4 + wm * 2) * 44 + zft0) * 1024;
      const int o2 = z1o + li[2] * 1024, o1 = z1o + li[1] * 1024;
      const int vo2 = v0 ? lane * 16 : DROP, vo1 = (v1 && !zeven) ? lane * 16 : DROP;
#pragma unroll
      for (int mp = 0; mp < 2; ++mp) {
        st128d(u32x4{y2[2 * mp][0], y2[2 * mp][1], y2[2 * mp + 1][0], y2[2 * mp + 1][1]}, wsd, vo2, o2 + mp * 44 * 1024);
        st128d(u32x4{y1[2 * mp][0], y1[2 * mp][1], y1[2 * mp + 1][0], y1[2 * mp + 1][1]}, wsd, vo1, o1 + mp * 44 * 1024);
      }
    }
#pragma unroll
    for (int m = 0; m < 4; ++m) pq[m] = u32x4{0u, 0u, 0u, 0u};
  }
  STAMP(7);
  unsigned biasv[2];
#pragma unroll
  for (int t = 0; t < 2; ++t) ldu16(biasv[t], mkdesc(bsp, 4096u), r16 * 2, (rt64 * 64 + th * 32 + t * 16) * 2);
  wsp_rd(5); wsp_rd(6); wsp_rd(7);
  pin(biasv[0]); pin(biasv[1]);
  STAMP(8);
  int lz = lane;
  asm volatile("" : "+v"(lz));
  const int l16 = lz * 16;
  auto poll_z = [&]() -> unsigned {
    u32x2 f = poll64(wsd, (l16 >> 1) & 127, WS_FLZ + cgp * 1024);
    return (unsigned)__ballot(f[0] == tag && f[1] == (tag ^ TAGX)) & 0xffffu;
  };
  unsigned rdy = 0;
  if (w == 0) {
#pragma nounroll
    for (int spin = 0; spin < SPIN_CAP; ++spin) {
      rdy = poll_z();
      if ((rdy & 15u) == 15u) break;
      __builtin_amdgcn_s_sleep(PSLEEP);
    }
    if (lane == 0) *(unsigned*)(smem + L_U + 5128) = rdy;
  }
  LBAR();
  rdy = __builtin_amdgcn_readfirstlane(*(const unsigned*)(smem + L_U + 5128));
  if (!isqkv && w == 1 && l16 == 0)
    __builtin_amdgcn_raw_buffer_store_b64(u32x2{tag, tag ^ TAGX}, wsr, 0, WS_FLZ + grp * 1024 + 512 + rt16 * 8, 16);
  STAMP(10);
  const int bsrc = WS_Z2F + ft0 * 4096 + w * 1024;
  const int u2off = npair == 3 ? 16384 : 0;
  auto dma2 = [&](int st, int u) {
    const int n = 2 * u + (w >> 2), q = w & 3;
    dma16d(wsd, l16, bsrc + st * (44 * 4096) + (u < 2 ? u * 8192 : u2off), (st & 3) * L_RING + (n ^ (u < 2 ? q : (q & 1))) * 4096 + q * 1024);
  };
  u32x4 kr[4];
  const int kroff = WS_KVR + (grp * 1088 + wn * 256) * 8;
  const int kzoff = WS_KVR + (grp * 1088 + 1024 + wn * 16) * 8;
  auto ld_kr = [&]() {
    int b = (l16 >> 4) & 48;
    asm volatile("" : "+v"(b));
#pragma unroll
    for (int j = 0; j < 2; ++j) { ld128d(kr[j], wsd, l16, kroff + j * 1024); ld128d(kr[2 + j], wsd, b, kzoff + j * 64); }
  };
  if (isqkv) ld_kr();
#pragma unroll
  for (int st = 0; st < 2; ++st)
#pragma unroll
    for (int u = 0; u < 3; ++u) dma2(st, u);
  if (isqkv) {
    WAITV(6);
#pragma nounroll
    for (int spin = 0; spin < SPIN_CAP; ++spin) {
      bool ok = true;
#pragma unroll
      for (int j = 0; j < 4; ++j) { pin(kr[j]); ok = ok && kr[j][1] == tag && kr[j][3] == tag; }
      if (__builtin_expect(__all(ok), 1)) break;
      __builtin_amdgcn_s_sleep(PSLEEP);
      ld_kr();
      WAITV(0);
    }
    const bf16x8 kvo = asb8(u32x4{kr[0][0], kr[0][2], kr[1][0], kr[1][2]});
    const bf16x8 zo = asb8(u32x4{kr[2][0], kr[2][2], kr[3][0], kr[3][2]});
    u32x2 ov[4];
#pragma unroll
    for (int m = 0; m < 4; ++m) {
      const f32x4 num = mfma32(kvo, asb8(pq[m]), zero4), den = mfma32(zo, asb8(pq[m]), zero4);
      ov[m] = pk4(num * __builtin_amdgcn_rcpf(den[0] + 1e-6f));
    }
#pragma unroll
    for (int mp = 0; mp < 2; ++mp)
      st128d(u32x4{ov[2 * mp][0], ov[2 * mp][1], ov[2 * mp + 1][0], ov[2 * mp + 1][1]}, wsd,
          l16, WS_A3F + ((rt16 * 4 + wm * 2 + mp) * 76 + grp * 4 + wn) * 1024);
    waitv<5>();
  } else {
    waitv<3>();
  }
  const int w3off = (lz >> 3) * 1024 + cgp * 128 + (((lz & 7) ^ ((lz >> 4) << 1)) << 4);
  const u32x4 od = mkdesc(wo, 512u * 1024u);
  f32x4 ac2[6][2];
#pragma unroll
  for (int n = 0; n < 6; ++n)
#pragma unroll
    for (int t = 0; t < 2; ++t) { const float b = kq == 0 ? bflo(biasv[t]) : 0.f; ac2[n][t] = f32x4{b, b, b, b}; }
  bf16x8 fz[2][6];
  auto rdZ = [&](int st, int n) { fz[st & 1][n] = *(const bf16x8*)(smem + (st & 3) * L_RING + n * 4096 + kq * 1024 + l16); };
  u32x4 ao[4][2];
  u32x4 z1v[2];
  u32x2 ofl, z1fl;
  const int a3 = WS_A3F + ((rt64 * 2 + th) * 76 + 2 * kq) * 1024;
  const int z1flo = WS_FLZ + cgp * 1024 + 512 + (rt64 >> 1) * 8;
  const int oflo = WS_FLO + rt64 * 256;
  BAR();
#pragma unroll
  for (int n = 0; n < 6; ++n) rdZ(0, n);
#pragma unroll
  for (int st = 2; st < 4; ++st)
#pragma unroll
    for (int u = 0; u < 3; ++u) dma2(st, u);
  sfor<16>([&](auto ST) {
    constexpr int st = ST;
    if constexpr (st < 15) {
      WAITL0();
      if constexpr (p2cnt(st, true) != p2cnt(st, false)) { if (isqkv) waitv<p2cnt(st, true)>(); else waitv<p2cnt(st, false)>(); }
      else waitv<p2cnt(st, false)>();
      BAR();
    }
    sfor<6>([&](auto N) {
      constexpr int n = N;
      SB();
      ac2[n][0] = mfma32(fz[st & 1][n], af[st >> 1][st & 1][0], ac2[n][0]);
      ac2[n][1] = mfma32(fz[st & 1][n], af[st >> 1][st & 1][1], ac2[n][1]);
      SB();
      if constexpr (st < 15 && n >= 1 && n <= 3) { rdZ(st + 1, 2 * n - 2); rdZ(st + 1, 2 * n - 1); }
      if constexpr (st + 4 < 16 && n == 0 && (st & 1) == 0) {
        if (__builtin_expect(((rdy >> (st + 4)) & 3u) != 3u, 0)) {
#pragma nounroll
          for (int spin = 0; spin < SPIN_CAP; ++spin) {
            rdy = poll_z();
            if (((rdy >> (st + 4)) & 3u) == 3u) break;
            __builtin_amdgcn_s_sleep(PSLEEP);
          }
        }
      }
      if constexpr (st + 4 < 16 && n < 3) dma2(st + 4, n);
      if constexpr (n == 3) {
        if constexpr ((st & 1) == 0) dma16(od, w3off, ((st >> 1) * 8 + w) * 8192, L_U + ((st >> 1) * 8 + w) * 1024);
        if constexpr (st == 2) {
          if (isqkv && l16 == 0)
            __builtin_amdgcn_raw_buffer_store_b64(u32x2{tag, tag ^ TAGX}, wsr, 0, WS_FLO + ((rt16 * 2 + wm) * 32 + grp * 4 + wn) * 8, 16);
        }
        if constexpr (st == 7) { ld64ds(z1fl, wsd, 0, z1flo); ld64ds(ofl, wsd, (l16 >> 1) & 255, oflo); }
        if constexpr (st == 11) {
          pin(z1fl); pin(ofl);
#pragma nounroll
          for (int spin = 0; spin < SPIN_CAP; ++spin) {
            if (__builtin_expect(__all(z1fl[0] == tag && z1fl[1] == (tag ^ TAGX) && ofl[0] == tag && ofl[1] == (tag ^ TAGX)), 1)) break;
            __builtin_amdgcn_s_sleep(PSLEEP);
            z1fl = poll64(wsd, 0, z1flo);
            ofl = poll64(wsd, (l16 >> 1) & 255, oflo);
          }
#pragma unroll
          for (int e = 0; e < 2; ++e)
            ld128d(z1v[e], wsd, l16, WS_Z1F + ((rt64 * 2 + th) * 44 + ft0 + min(kq + 4 * e, 2 * npair - 1)) * 1024);
        }
        if constexpr (st == 12 || st == 13) {
#pragma unroll
          for (int i = 0; i < 2; ++i)
#pragma unroll
            for (int h = 0; h < 2; ++h) ld128d(ao[2 * (st - 12) + i][h], wsd, l16, a3 + (8 * (2 * (st - 12) + i) + h) * 1024);
        }
      }
    });
  });
  STAMP(11);
  {
    char* rb = smem + th * 36864 + l16;
#pragma unroll
    for (int i = 1; i < 4; ++i)
#pragma unroll
      for (int t = 0; t < 2; ++t) *(f32x4*)(rb + ((((i ^ kq) * 3 + i - 1) * 2 + t) << 10)) = ac2[i][t];
#pragma unroll
    for (int t = 0; t < 2; ++t) *(f32x4*)(rb + ((((5 ^ (kq & 1)) * 3 + (kq & 2)) * 2 + t) << 10)) = ac2[5][t];
    if (kq >= 2) {
#pragma unroll
      for (int t = 0; t < 2; ++t) *(f32x4*)(rb + ((((4 ^ (kq & 1)) * 3 + 1) * 2 + t) << 10)) = ac2[4][t];
    }
    WAITV(0);
    pin(z1v[0]); pin(z1v[1]);
    LBAR();
#pragma unroll
    for (int e = 0; e < 2; ++e) {
      const int n = kq + 4 * e;
      if (e == 0 || n < 2 * npair) {
        u32x4 o;
#pragma unroll
        for (int t = 0; t < 2; ++t) {
          f32x4 s = ac2[4 * e][t];
#pragma unroll
          for (int j = 0; j < 3; ++j) s += *(const f32x4*)(rb + (((n * 3 + j) * 2 + t) << 10));
          o[2 * t] = pk2(bflo(z1v[e][2 * t]) * s[0], bfhi(z1v[e][2 * t]) * s[1]);
          o[2 * t + 1] = pk2(bflo(z1v[e][2 * t + 1]) * s[2], bfhi(z1v[e][2 * t + 1]) * s[3]);
        }
        if (ft0 + n >= 43) o = u32x4{0u, 0u, 0u, 0u};
        st128d(o, wsd, l16, WS_A3F + ((rt64 * 2 + th) * 76 + 32 + ft0 + n) * 1024);
      }
    }
  }
  STAMP(12);
  f32x4 ac3[4][2];
#pragma unroll
  for (int n = 0; n < 4; ++n) { ac3[n][0] = zero4; ac3[n][1] = zero4; }
  int ly = l16;
  asm volatile("" : "+v"(ly));
  const int lane3 = ly >> 4, r3 = lane3 & 15, g3 = lane3 >> 4, tid3 = w * 64 + lane3;
  int b3[4];
  {
    const int k = 4 * g3 + (r3 >> 2), tp = r3 & 3;
    const int h2 = ((k >> 1) & 1) | (((k >> 2) & 1) << 1);
#pragma unroll
    for (int n = 0; n < 4; ++n) b3[n] = k * 128 + (((2 * n + (tp >> 1)) ^ (h2 << 1)) << 4) + (tp & 1) * 8 + kq * 4096;
  }
  const u32x4 ud = mkdesc(wout, 688u * 1024u);
  const int go = (rt64 * 64 + (tid3 >> 3)) * 1024 + cgp * 128 + (tid3 & 7) * 16;
  u32x4 xres;
  sfor<4>([&](auto I) {
    constexpr int i = I;
    bf16x8 b[4];
#pragma unroll
    for (int n = 0; n < 4; ++n) b[n] = cat8(trread(smem + L_U + b3[n] + i * 16384), trread(smem + L_U + b3[n] + i * 16384 + 2048));
    pin(ao[i][0]); pin(ao[i][1]);
#pragma unroll
    for (int n = 0; n < 4; ++n)
#pragma unroll
      for (int t = 0; t < 2; ++t)
        ac3[n][t] = mfma32(b[n], asb8(u32x4{ao[i][0][2 * t], ao[i][0][2 * t + 1], ao[i][1][2 * t], ao[i][1][2 * t + 1]}), ac3[n][t]);
  });
  WAITV(0);
  LBAR();
  if (tid3 == 0) __builtin_amdgcn_raw_buffer_store_b64(u32x2{tag, tag ^ TAGX}, wsr, WS_FLU + rt64 * 512 + cgp * 8, 0, 16);
  STAMP(13);
  if (w > 0) {
#pragma unroll
    for (int i = 0; i < 13; ++i)
      if (i < 12 || w < 5) dma16(ud, w3off, min(i * 7 + w - 1, 85) * 8192, (i * 7 + w - 1) * 1024);
  }
  asm volatile("buffer_load_dwordx4 %0, %1, %2, 0 offen" : "=v"(xres) : "v"(go), "s"(xd) : "memory");
  if (bid == 0 && w == 7) {
#pragma nounroll
    for (int spin = 0; spin < SPIN_CAP; ++spin) {
      u32x4 f[4];
#pragma unroll
      for (int j = 0; j < 4; ++j) ld128d(f[j], wsd, lane3 * 128, WS_RST + j * 8192);
      WAITV(0);
#pragma unroll
      for (int j = 0; j < 4; ++j) pin(f[j]);
      if (__all(f[0][1] == tag && f[1][1] == tag && f[2][1] == tag && f[3][1] == tag)) break;
    }
    if (lane3 == 0) __builtin_amdgcn_raw_buffer_store_b32(tag, wsr, WS_EP, 0, 16);
  }
  if (w == 0) {
#pragma nounroll
    for (int spin = 0; spin < SPIN_CAP; ++spin) {
      u32x2 f = poll64(wsd, (l16 >> 1) & 63, WS_FLU + rt64 * 512);
      if (__all(f[0] == tag && f[1] == (tag ^ TAGX))) break;
      __builtin_amdgcn_s_sleep(PSLEEP);
    }
  }
  WAITV(0);
  BAR();
  u32x4 au[6][2];
#pragma unroll
  for (int i = 0; i < 6; ++i)
#pragma unroll
    for (int h = 0; h < 2; ++h) ld128d(au[i][h], wsd, l16, a3 + (32 + h + ((i < 5 || kq < 2) ? 8 * i : 32)) * 1024);
  STAMP(14);
  sfor<6>([&](auto I) {
    constexpr int i = I;
    bf16x8 b[4];
    const int io = (i < 5 || kq < 2) ? i * 16384 : 0;
#pragma unroll
    for (int n = 0; n < 4; ++n) b[n] = cat8(trread(smem + b3[n] + io), trread(smem + b3[n] + io + 2048));
    waitv<10 - 2 * i>();
    pin(au[i][0]); pin(au[i][1]);
    if (i < 5 || kq < 2) {
#pragma unroll
      for (int n = 0; n < 4; ++n)
#pragma unroll
        for (int t = 0; t < 2; ++t)
          ac3[n][t] = mfma32(b[n], asb8(u32x4{au[i][0][2 * t], au[i][0][2 * t + 1], au[i][1][2 * t], au[i][1][2 * t + 1]}), ac3[n][t]);
    }
  });
  STAMP(15);
#pragma unroll
  for (int n = 0; n < 4; ++n)
#pragma unroll
    for (int t = 0; t < 2; ++t)
      *(f32x4*)(smem + L_U + kq * 16384 + (th * 32 + t * 16 + r3) * 256 + (((n * 4 + g3) ^ r3) << 4)) = ac3[n][t];
  LBAR();
  {
    pin(xres);
    const int row = tid3 >> 3, c8 = tid3 & 7;
    f32x4 s0 = {bflo(xres[0]), bfhi(xres[0]), bflo(xres[1]), bfhi(xres[1])};
    f32x4 s1 = {bflo(xres[2]), bfhi(xres[2]), bflo(xres[3]), bfhi(xres[3])};
#pragma unroll
    for (int j = 0; j < 4; ++j) {
      s0 += *(const f32x4*)(smem + L_U + j * 16384 + row * 256 + (((2 * c8) ^ (row & 15)) << 4));
      s1 += *(const f32x4*)(smem + L_U + j * 16384 + row * 256 + (((2 * c8 + 1) ^ (row & 15)) << 4));
    }
    const u32x2 lo = pk4(s0), hi = pk4(s1);
    st128d(u32x4{lo[0], lo[1], hi[0], hi[1]}, mkdesc(out, 2048u * 1024u), go, 0);
  }
  STAMP(16);
}

extern "C" void kernel_launch(void* const* d_in, const int* in_sizes, int n_in, void* d_out, int out_size, void* d_ws,
                              size_t ws_size, hipStream_t stream) {
  (void)hipFuncSetAttribute((const void*)fused, hipFuncAttributeMaxDynamicSharedMemorySize, LDS_BYTES);
  fused<<<256, 512, LDS_BYTES, stream>>>((const char*)d_in[0], (const char*)d_in[1], (const char*)d_in[2],
                                         (const char*)d_in[3], (const char*)d_in[5], (const char*)d_in[6],
                                         (const char*)d_in[7], (const char*)d_in[8], (char*)d_out, (char*)d_ws);
}
```

```cpp
#include <hip/hip_runtime.h>
#include <hip/hip_bf16.h>
#include <stdint.h>

typedef __bf16 bf16x8 __attribute__((ext_vector_type(8)));
typedef __bf16 bf16x2 __attribute__((ext_vector_type(2)));
typedef short s16x4 __attribute__((ext_vector_type(4)));
typedef short s16x8 __attribute__((ext_vector_type(8)));
typedef float f32x4 __attribute__((ext_vector_type(4)));
typedef float f32x2 __attribute__((ext_vector_type(2)));
typedef unsigned u32x4 __attribute__((ext_vector_type(4)));
typedef unsigned u32x2 __attribute__((ext_vector_type(2)));
#define LAS __attribute__((address_space(3)))
#define DI __device__ __forceinline__

constexpr int WS_EP  = 0;
constexpr int WS_FLZ = 1024;
constexpr int WS_FLO = WS_FLZ + 4096;
constexpr int WS_FLU = WS_FLO + 8192;
constexpr int WS_CS  = WS_FLU + 16384;
constexpr int WS_RST = WS_CS + 16 * 4 * 1024;
constexpr int WS_ZST = WS_RST + 2048 * 16;
constexpr int KVP_SZ = 1088 * 16;
constexpr int WS_KVP = WS_ZST + 32 * 16 * 1024;
constexpr int WS_KVR = WS_KVP + 8 * 16 * KVP_SZ;
constexpr int WS_Z2F = WS_KVR + 8 * 1088 * 8;
constexpr int WS_Z1F = WS_Z2F + 16 * 44 * 4096;
constexpr int WS_A3F = WS_Z1F + 64 * 44 * 1024;
constexpr int WS_END = WS_A3F + 64 * 76 * 1024;
static_assert(WS_Z2F % 128 == 0 && WS_KVP % 128 == 0 && WS_KVR % 128 == 0, "alignment");

template <int V> struct IC { static constexpr int value = V; constexpr operator int() const { return V; } };
template <int I, int N, class F> DI void sfor_(F&& f) { if constexpr (I < N) { f(IC<I>{}); sfor_<I + 1, N>(f); } }
template <int N, class F> DI void sfor(F&& f) { sfor_<0, N>(f); }
template <int N> DI void waitv() { asm volatile("s_waitcnt vmcnt(%0)" :: "n"(N) : "memory"); }

DI float asf(unsigned u) { return __builtin_bit_cast(float, u); }
DI unsigned asu(float f) { return __builtin_bit_cast(unsigned, f); }
DI u32x4 mkdesc(const void* p, unsigned bytes) {
  unsigned long long a = (unsigned long long)p;
  u32x4 d = {(unsigned)a, (unsigned)(a >> 32) & 0xffffu, bytes, 0x00020000u};
  return d;
}
DI void dma16(u32x4 desc, int voff, int soff, int lds) {
  asm volatile("s_mov_b32 m0, %3\n\ts_nop 0\n\tbuffer_load_dwordx4 %0, %1, %2 offen lds"
               :: "v"(voff), "s"(desc), "s"(soff), "s"(lds) : "memory");
}
DI void dma16d(u32x4 desc, int voff, int soff, int lds) {
  asm volatile("s_mov_b32 m0, %3\n\ts_nop 0\n\tbuffer_load_dwordx4 %0, %1, %2 offen sc1 lds"
               :: "v"(voff), "s"(desc), "s"(soff), "s"(lds) : "memory");
}
DI u32x2 poll64(u32x4 desc, int voff, int soff) {
  u32x2 r;
  asm volatile("buffer_load_dwordx2 %0, %1, %2, %3 offen sc1\n\ts_waitcnt vmcnt(0)" : "=&v"(r) : "v"(voff), "s"(desc), "s"(soff) : "memory");
  return r;
}
DI void ld128d(u32x4& r, u32x4 desc, int voff, int soff) {
  asm volatile("buffer_load_dwordx4 %0, %1, %2, %3 offen sc1" : "=v"(r) : "v"(voff), "s"(desc), "s"(soff) : "memory");
}
DI void ld64d(u32x2& r, u32x4 desc, int voff) {
  asm volatile("buffer_load_dwordx2 %0, %1, %2, 0 offen sc1" : "=v"(r) : "v"(voff), "s"(desc) : "memory");
}
DI void ld64ds(u32x2& r, u32x4 desc, int voff, int soff) {
  asm volatile("buffer_load_dwordx2 %0, %1, %2, %3 offen sc1" : "=v"(r) : "v"(voff), "s"(desc), "s"(soff) : "memory");
}
DI void ld64p(u32x2& r, u32x4 desc, int voff, int soff) {
  asm volatile("buffer_load_dwordx2 %0, %1, %2, %3 offen" : "=v"(r) : "v"(voff), "s"(desc), "s"(soff) : "memory");
}
DI void ldu16(unsigned& r, u32x4 desc, int voff, int soff) {
  asm volatile("buffer_load_ushort %0, %1, %2, %3 offen" : "=v"(r) : "v"(voff), "s"(desc), "s"(soff) : "memory");
}
DI void st128d(u32x4 v, u32x4 desc, int voff, int soff) {
  asm volatile("buffer_store_dwordx4 %0, %1, %2, %3 offen sc1\n\ts_nop 1" :: "v"(v), "v"(voff), "s"(desc), "s"(soff) : "memory");
}
DI void gld32(unsigned& r, const char* p) { asm volatile("global_load_dword %0, %1, off" : "=&v"(r) : "v"(p) : "memory"); }
DI void pin(u32x4& v) { asm volatile("" : "+v"(v)); }
DI void pin(u32x2& v) { asm volatile("" : "+v"(v)); }
DI void pin(unsigned& v) { asm volatile("" : "+v"(v)); }
DI s16x4 trread(const char* p) { return __builtin_amdgcn_ds_read_tr16_b64_v4i16((LAS s16x4*)p); }
DI bf16x8 cat8(s16x4 a, s16x4 b) {
  s16x8 r = __builtin_shufflevector(a, b, 0, 1, 2, 3, 4, 5, 6, 7);
  return __builtin_bit_cast(bf16x8, r);
}
DI void keep(bf16x8 v) { asm volatile("" :: "v"(v)); }
DI bf16x8 asb8(u32x4 v) { return __builtin_bit_cast(bf16x8, v); }
DI u32x4 asu4(f32x4 v) { return __builtin_bit_cast(u32x4, v); }
DI f32x4 asf4(u32x4 v) { return __builtin_bit_cast(f32x4, v); }
DI f32x4 mfma32(bf16x8 a, bf16x8 b, f32x4 c) { return __builtin_amdgcn_mfma_f32_16x16x32_bf16(a, b, c, 0, 0, 0); }
DI f32x4 mfma16(u32x2 a, u32x2 b, f32x4 c) {
  return __builtin_amdgcn_mfma_f32_16x16x16bf16_1k(__builtin_bit_cast(s16x4, a), __builtin_bit_cast(s16x4, b), c, 0, 0, 0);
}
template <int CTRL> DI float dppadd(float v) { return v + asf(__builtin_amdgcn_update_dpp(0u, asu(v), CTRL, 0xf, 0xf, true)); }
DI unsigned pk2(float a, float b) {
  f32x2 f = {a, b};
  return __builtin_bit_cast(unsigned, __builtin_convertvector(f, bf16x2));
}
DI u32x2 pk4(f32x4 v) { return u32x2{pk2(v[0], v[1]), pk2(v[2], v[3])}; }
DI float bflo(unsigned u) { return asf(u << 16); }
DI float bfhi(unsigned u) { return asf(u & 0xffff0000u); }
DI float dot2(unsigned a, unsigned b, float c) {
  return __builtin_amdgcn_fdot2_f32_bf16(__builtin_bit_cast(bf16x2, a), __builtin_bit_cast(bf16x2, b), c, false);
}
DI float fmx(float a, float b) { return __builtin_elementwise_maximum(a, b); }
DI float fmn(float a, float b) { return __builtin_elementwise_minimum(a, b); }
template <bool MAX> DI float rop(unsigned a, unsigned b) { return MAX ? fmx(asf(a), asf(b)) : asf(a) + asf(b); }
template <bool MAX> DI void redg2(float& a, float& b) {
  auto r = __builtin_amdgcn_permlane16_swap(asu(a), asu(b), false, false);
  float m = rop<MAX>(r[0], r[1]);
  auto r2 = __builtin_amdgcn_permlane32_swap(asu(m), asu(m), false, false);
  m = rop<MAX>(r2[0], r2[1]);
  auto r3 = __builtin_amdgcn_permlane16_swap(asu(m), asu(m), false, false);
  a = asf(r3[0]); b = asf(r3[1]);
}
template <bool MAX> DI float redg4(const float (&v)[4]) {
  auto r = __builtin_amdgcn_permlane16_swap(asu(v[0]), asu(v[1]), false, false);
  auto q = __builtin_amdgcn_permlane16_swap(asu(v[2]), asu(v[3]), false, false);
  auto t = __builtin_amdgcn_permlane32_swap(asu(rop<MAX>(r[0], r[1])), asu(rop<MAX>(q[0], q[1])), false, false);
  return rop<MAX>(t[0], t[1]);
}
DI void bcast4(float m, float (&v)[4]) {
  auto t = __builtin_amdgcn_permlane32_swap(asu(m), asu(m), false, false);
  unsigned t0 = t[0], t1 = t[1];
  auto r = __builtin_amdgcn_permlane16_swap(t0, t0, false, false);
  auto q = __builtin_amdgcn_permlane16_swap(t1, t1, false, false);
  v[0] = asf(r[0]); v[1] = asf(r[1]); v[2] = asf(q[0]); v[3] = asf(q[1]);
}
constexpr float L2E = 1.4426950408889634f;
DI f32x4 exp4(f32x4 v) {
  return f32x4{__builtin_amdgcn_exp2f(v[0]), __builtin_amdgcn_exp2f(v[1]), __builtin_amdgcn_exp2f(v[2]), __builtin_amdgcn_exp2f(v[3])};
}
DI float sum4(f32x4 v) { f32x2 t = v.xy + v.zw; return t[0] + t[1]; }
template <class F> DI void hedgehog4(const f32x4 (&p)[4], u32x4 (&o)[4], F&& bg) {
  float mx[4], mn[4];
#pragma unroll
  for (int i = 0; i < 4; ++i) {
    mx[i] = fmx(fmx(p[i][0], p[i][1]), fmx(p[i][2], p[i][3]));
    mn[i] = -fmn(fmn(p[i][0], p[i][1]), fmn(p[i][2], p[i][3]));
  }
  bg(IC<0>{});
  bcast4(redg4<true>(mx), mx);
  bcast4(redg4<true>(mn), mn);
  asm volatile("" : "+v"(mx[0]), "+v"(mx[1]), "+v"(mx[2]), "+v"(mx[3]), "+v"(mn[0]), "+v"(mn[1]), "+v"(mn[2]), "+v"(mn[3]));
  bg(IC<1>{});
  f32x4 ep[4], en[4];
  float sp[4], sn[4];
  sfor<4>([&](auto I) {
    constexpr int i = I;
    ep[i] = exp4(p[i] - mx[i]);
    en[i] = exp4(-p[i] - mn[i]);
    sp[i] = sum4(ep[i]);
    sn[i] = sum4(en[i]);
    asm volatile("" : "+v"(sp[i]), "+v"(sn[i]));
    bg(IC<2 + i>{});
  });
  bcast4(__builtin_amdgcn_rcpf(redg4<false>(sp)), sp);
  bcast4(__builtin_amdgcn_rcpf(redg4<false>(sn)), sn);
  asm volatile("" : "+v"(sp[0]), "+v"(sp[1]), "+v"(sp[2]), "+v"(sp[3]), "+v"(sn[0]), "+v"(sn[1]), "+v"(sn[2]), "+v"(sn[3]));
  bg(IC<6>{});
#pragma unroll
  for (int i = 0; i < 4; ++i) {
    const u32x2 a = pk4(ep[i] * sp[i]), b = pk4(en[i] * sn[i]);
    o[i] = u32x4{a[0], a[1], b[0], b[1]};
    pin(o[i]);
  }
  bg(IC<7>{});
}
template <class F> DI void gelu24(f32x4 (&acc)[2][3], const float (&k1)[2], const float (&k2)[2], const float (&irs)[2], F&& bg) {
  sfor<2>([&](auto M) {
    constexpr int m = M;
    const f32x4 K1 = {k1[m], k1[m], k1[m], k1[m]}, K2 = {k2[m], k2[m], k2[m], k2[m]}, I = {irs[m], irs[m], irs[m], irs[m]};
    f32x4 t[3];
#pragma unroll
    for (int n = 0; n < 3; ++n) t[n] = acc[m][n] * __builtin_elementwise_fma(acc[m][n] * acc[m][n], K2, K1);
#pragma unroll
    for (int n = 0; n < 3; ++n) t[n] = exp4(t[n]);
    asm volatile("" : "+v"(t[0]), "+v"(t[1]), "+v"(t[2]));
    bg(IC<2 * m>{});
#pragma unroll
    for (int n = 0; n < 3; ++n) t[n] = __builtin_elementwise_fma(t[n], I, I);
#pragma unroll
    for (int n = 0; n < 3; ++n)
      acc[m][n] *= f32x4{__builtin_amdgcn_rcpf(t[n][0]), __builtin_amdgcn_rcpf(t[n][1]), __builtin_amdgcn_rcpf(t[n][2]), __builtin_amdgcn_rcpf(t[n][3])};
    asm volatile("" : "+v"(acc[m][0]), "+v"(acc[m][1]), "+v"(acc[m][2]));
    bg(IC<2 * m + 1>{});
  });
}
DI void bcast2(float v, float (&o)[2]) {
  auto r = __builtin_amdgcn_permlane16_swap(asu(v), asu(v), false, false);
  o[0] = asf(r[0]); o[1] = asf(r[1]);
}
constexpr int DROP = 0x7f000000;
#define STAMP(i)
#define WAITV(n) asm volatile("s_waitcnt vmcnt(" #n ")" ::: "memory")
#define WAITL0() asm volatile("s_waitcnt lgkmcnt(0)" ::: "memory")
#define SB() __builtin_amdgcn_sched_barrier(0)
#define BAR() do { SB(); __builtin_amdgcn_s_barrier(); SB(); } while (0)
#define LBAR() do { WAITL0(); BAR(); } while (0)
#define PSLEEP 4
constexpr int SPIN_CAP = 1 << 18;
constexpr unsigned TAGX = 0x5bd1e995u;

constexpr int L_SLOT = 40960;
constexpr int L_ZW = 24576;
constexpr int L_CB = 32768;
constexpr int L_U = 98304;
constexpr int L_WO = 49152;
constexpr int ring2(int st) { return (st & 2) ? (st & 1) * 24576 : 114688 + (st & 1) * 24576; }
constexpr int wout_at(int i) { return i < 3 ? i * 16384 : 114688 + (i - 3) * 16384; }
constexpr int LDS_BYTES = 163840;
constexpr int CODE_LINES = 360;
constexpr int p1X(int b) { return b == 9 ? 3 : 0; }
constexpr int p1cnt(int i) {
  int n = i + 3;
  for (int b = i < 1 ? 0 : (5 * i - 1) / 3; b < 2 * i; ++b) n += p1X(b);
  return n;
}
constexpr int zX(int b) { return (b == 1 ? 1 : 0) + (b == 7 ? 2 : 0) + (b == 15 ? 1 : 0) + (b == 19 ? 2 : 0) + (b == 22 ? 1 : 0) + (b == 25 ? 3 : 0) + (b == 28 ? 1 : 0); }
struct ZSched {
  int blk[52], first[33];
  static constexpr int stage(int p) { return p < 24 ? 2 + p / 4 : (p < 48 ? 8 + (p - 24) / 3 : 16); }
  static constexpr int last(int j) { return j < 8 ? 4 * (j - 2) + 3 : 24 + 3 * (j - 8) + 2; }
  constexpr ZSched() : blk(), first() {
    int cur = 0, cnt = 0;
    for (int p = 0; p < 52; ++p) {
      const int j = stage(p);
      const int mn = j == 16 ? 28 : ((p < 24 && p % 4 == 0) || j < 4 ? 0 : 2 * (j - 4) + 1);
      if (cnt == 3) { ++cur; cnt = 0; }
      if (cur < mn) { cur = mn; cnt = 0; }
      blk[p] = cur; ++cnt;
    }
    for (int b = 0; b <= 32; ++b) { int f = 0; while (f < 52 && blk[f] < b) ++f; first[b] = f; }
  }
  constexpr int cnt(int j) const {
    if (j == 0) return first[1];
    int n = first[2 * j + 1] - last(j + 1) - 1;
    for (int b = blk[last(j + 1)]; b < 2 * j; ++b) n += zX(b);
    return n;
  }
  constexpr int since(int b0, int b1) const {
    int n = first[b1 + 1] - first[b0 + 1];
    for (int b = b0 + 1; b < b1; ++b) n += zX(b);
    return n;
  }
};
constexpr ZSched ZS;
constexpr int cmin(int a, int b) { return a < b ? a : b; }
constexpr int p2P(int i) { return i + 4 < 16 ? 3 : 0; }
constexpr int p2E(int i) { return i < 0 ? 0 : ((i & 4) == 0 ? 1 : 0) + (i == 2 ? 1 : 0) + (i == 11 ? 2 : 0) + (i == 13 ? 2 : 0); }
constexpr int p2Q(int i) { return i == 2 ? 4 : (i == 7 ? 2 : 0); }
constexpr int p2cnt(int st, bool q) {
  if (st == 13) return 0;
  return p2E(st - 3) + p2P(st - 2) + p2E(st - 2) + p2P(st - 1) + p2E(st - 1) + (q ? p2Q(st - 3) + p2Q(st - 2) + p2Q(st - 1) : 0);
}

__global__ __launch_bounds__(512, 2) void fused(const char* __restrict__ x, const char* __restrict__ wqkv,
                                                const char* __restrict__ wphi, const char* __restrict__ wo,
                                                const char* __restrict__ win, const char* __restrict__ wsp,
                                                const char* __restrict__ bsp, const char* __restrict__ wout,
                                                char* out, char* ws) {
  extern __shared__ __attribute__((aligned(16))) char smem[];
  const char* code0 = (const char*)(__builtin_amdgcn_s_getpc() & ~127ull);
  const int tid = threadIdx.x, lane = tid & 63;
  const int w = __builtin_amdgcn_readfirstlane(tid >> 6);
  const int r16 = lane & 15, g = lane >> 4;
  const int tq = r16 >> 2, tp = r16 & 3;
  const int bid = blockIdx.x, xcd = bid & 7, jj = bid >> 3;
  const bool isqkv = (jj & 2) == 0;
  const int rt16 = (xcd >> 2) * 8 + (jj >> 2);
  const int grp = (xcd & 3) * 2 + (jj & 1);
  const int rt32 = rt16 * 2 + (jj & 1), zg = xcd & 3;
  const int znft = zg < 3 ? 12 : 7;
  const int wm = w >> 2, wn = w & 3;
  const int rq = (isqkv ? rt16 : rt32) & 3;
  const int rt64 = xcd * 4 + (jj >> 3);
  const int cgp = jj & 7;
  const int ft0 = cgp < 7 ? cgp * 6 : 40;
  const int npair = cgp < 6 ? 3 : 2;
  const int th = w & 1, kq = w >> 1;
  const int rot2 = cgp;

  int xsrc[2], wsrc[3];
#pragma unroll
  for (int u = 0; u < 2; ++u) {
    const int row = isqkv ? 16 * w + 8 * u + (lane >> 3) : 8 * w + (lane >> 3);
    xsrc[u] = ((isqkv ? rt16 * 128 : rt32 * 64) + row) * 1024 + (((lane & 7) ^ ((row >> 1) & 7)) << 4);
  }
#pragma unroll
  for (int u = 0; u < 3; ++u) {
    int ci = u * 64 + lane;
    int rl = (ci * 2731) >> 16, c = ci - rl * 24;
    int h2 = ((rl >> 1) & 1) | ((w & 1) << 1);
    int cs = c ^ (h2 << 1);
    int vq = (8 * w + rl) * 3072 + (cs >> 3) * 1024 + grp * 128 + (cs & 7) * 16;
    int vz = (8 * w + rl) * 2752 + zg * 384 + min(cs, znft * 2 - 1) * 16;
    wsrc[u] = isqkv ? vq : vz;
  }
  asm volatile("" : "+v"(xsrc[0]), "+v"(xsrc[1]), "+v"(wsrc[0]), "+v"(wsrc[1]), "+v"(wsrc[2]));
  asm volatile("" : "+s"(x), "+s"(wqkv), "+s"(wphi), "+s"(wo), "+s"(win), "+s"(wsp), "+s"(bsp), "+s"(wout), "+s"(out), "+s"(ws));
  const __amdgpu_buffer_rsrc_t wsr = __builtin_amdgcn_make_buffer_rsrc(ws, 0, WS_END, 0x00020000);
  const u32x4 wsd = mkdesc(ws, WS_END);
  const u32x4 xd = mkdesc(x, 2048u * 1024u);
  const u32x4 ad = mkdesc(wsp, 2048u * 4096u);
  const f32x4 zero4 = {0.f, 0.f, 0.f, 0.f};
  const unsigned ONES = 0x3f803f80u;
  const bf16x8 ones8 = asb8(u32x4{ONES, ONES, ONES, ONES});
  STAMP(0);

  const int asrc = (rt64 * 64 + th * 32 + (lane >> 3)) * 4096 + kq * 128 + (((lane & 7) ^ ((lane >> 4) & 3)) << 4);
  auto dmaA = [&](int c, int u) {
    dma16(ad, asrc ^ ((u & 1) << 6), ((c + rot2) & 7) * 512 + u * 8 * 4096, (c % 3) * L_CB + w * 4096 + u * 1024);
  };
  bf16x8 af[8][2][2];
  const int aro = w * 4096 + r16 * 128;
  auto wsp_ld = [&](int c) {
#pragma unroll
    for (int e = 0; e < 2; ++e)
#pragma unroll
      for (int t = 0; t < 2; ++t)
        af[c][e][t] = *(const bf16x8*)(smem + (c % 3) * L_CB + aro + t * 2048 + (((4 * e + g) ^ ((r16 >> 1) & 7)) << 4));
    SB();
  };
  auto wsp_rd = [&](int c) { SB(); WAITV(8); wsp_ld(c); };
  auto wsp_is = [&](int c) {
    SB();
    WAITL0();
#pragma unroll
    for (int u = 0; u < 4; ++u) dmaA(c, u);
    SB();
  };
  auto wsp_h = [&](int c, int h) {
    SB();
    if (h == 0) WAITL0();
    dmaA(c, 2 * h); dmaA(c, 2 * h + 1);
    SB();
  };
  unsigned ep, tag = 0, pf;
  u32x4 xrow, rst, cst;
  float st_s = 0.f, st_q = 0.f;
  auto ld_first = [&]() {
    asm volatile("buffer_load_dword %0, off, %2, 0 sc1\n\tbuffer_load_dwordx4 %1, %3, %4, 0 offen"
                 : "=&v"(ep), "=&v"(xrow) : "s"(wsd), "v"((bid * 8 + w) * 1024 + lane * 16), "s"(xd) : "memory");
  };
  auto row_stats = [&](int step) {
    if (step == 0) {
      pin(ep); pin(xrow);
      tag = __builtin_amdgcn_readfirstlane(ep) + 1u;
#pragma unroll
      for (int e = 0; e < 4; ++e) { st_s = dot2(xrow[e], ONES, st_s); st_q = dot2(xrow[e], xrow[e], st_q); }
      redg2<false>(st_s, st_q);
    }
    if (step == 1) {
      st_s = dppadd<0x128>(st_s); st_q = dppadd<0x128>(st_q); st_s = dppadd<0x124>(st_s); st_q = dppadd<0x124>(st_q);
      st_s = dppadd<0x122>(st_s); st_q = dppadd<0x122>(st_q); st_s = dppadd<0x121>(st_s); st_q = dppadd<0x121>(st_q);
    }
    if (step == 2) {
      const float mean = st_s * (1.0f / 512.0f);
      const float var = fmx(st_q * (1.0f / 512.0f) - mean * mean, 0.f);
      st128d(u32x4{asu(__builtin_amdgcn_rsqf(var + 1e-5f)), tag, asu(mean), tag}, wsd, lane == 0 ? 0 : DROP, WS_RST + (bid * 8 + w) * 16);
    }
  };
  auto cs_publish = [&](const f32x4 (&csum)[3], int cg) {
    u32x4 o;
#pragma unroll
    for (int n = 0; n < 3; ++n) o[n] = pk2(csum[n][0], csum[n][0] - bflo(pk2(csum[n][0], 0.f)));
    o[3] = tag ^ o[0] ^ o[1] ^ o[2];
    st128d(o, wsd, g == 0 ? r16 * 16 : DROP, WS_CS + (cg * 4 + wn) * 1024 + rq * 256);
  };
  auto prefetch = [&]() {
    const int l8 = lane & 7, li = bid * 40 + w * 8 + l8;
    gld32(pf, w < 5 ? (li < 4096 ? wo + li * 128 : wout + min(li - 4096, 5503) * 128) : code0 + min(jj * 24 + (w - 5) * 8 + l8, CODE_LINES - 1) * 128);
  };
  const int h2r = ((tq >> 1) & 1) | ((g & 1) << 1);
  u32x4 pq[4];
  u32x4 la, lb;
  u32x2 zfl;
  const int l1sub = rt16 * 8 + w;
  const int l1off = (lane >> 3) * KVP_SZ + (lane & 7) * 16;
  const int flzv = ((lane + 4 * rot2) & 31) * 8, flzs = WS_FLZ + (cgp >> 1) * 1024;

  if (isqkv) {
    const u32x4 wd = mkdesc(wqkv, 512u * 3072u);
    auto dma1 = [&](int st, int u) {
      const int slot = (st & 3) * L_SLOT, sr = (st + 2 * rq) & 7;
      if (u < 2) dma16(xd, xsrc[u], sr * 128, slot + (16 * w + 8 * u) * 128);
      else dma16(wd, wsrc[u - 2], sr * (64 * 3072), slot + 16384 + (3 * w + u - 2) * 1024);
    };
#pragma unroll
    for (int u = 0; u < 5; ++u) dma1(0, u);
    ld_first();
#pragma unroll
    for (int u = 0; u < 5; ++u) dma1(1, u);
    int aoff[2], boff[3];
#pragma unroll
    for (int ks = 0; ks < 2; ++ks) aoff[ks] = (wm * 64 + r16) * 128 + (((4 * ks + g) ^ ((r16 >> 1) & 7)) << 4);
#pragma unroll
    for (int i = 0; i < 3; ++i) boff[i] = 16384 + (8 * g + tq) * 384 + (((2 * (4 * i + wn) + (tp >> 1)) ^ (h2r << 1)) << 4) + (tp & 1) * 8;
    f32x4 acc[4][3], csum[3];
#pragma unroll
    for (int i = 0; i < 3; ++i) { acc[0][i] = zero4; acc[1][i] = zero4; acc[2][i] = zero4; acc[3][i] = zero4; csum[i] = zero4; }
    bf16x8 fa[2][4], fb[2][3];
    u32x2 wrow;
    float rs[4], ka[4], mh[4], ml[4];
    const bool csprod = rt16 < 4 && wm == 0;
    auto ld_stats = [&]() {
      ld128d(rst, wsd, lane * 16, WS_RST + (rt16 * 128 + wm * 64) * 16);
      ld128d(cst, wsd, lane * 16, WS_CS + (grp * 4 + wn) * 1024);
    };
    auto rdA = [&](int st, int ks, int m) { fa[ks][m] = *(const bf16x8*)(smem + (st & 3) * L_SLOT + aoff[ks] + m * 2048); };
    auto rdB = [&](int st, int ks, int n) {
      const char* p = smem + (st & 3) * L_SLOT + boff[n] + ks * 12288;
      fb[ks][n] = cat8(trread(p), trread(p + 1536));
    };
    waitv<7>();
    BAR();
    STAMP(1);
    rdB(0, 0, 0); rdA(0, 0, 0); rdA(0, 0, 1); rdA(0, 0, 2); rdA(0, 0, 3); rdB(0, 0, 1); rdB(0, 0, 2);
    sfor<16>([&](auto B) {
      constexpr int b = B, i = b >> 1, ks = b & 1;
      constexpr int nst = (b + 1) >> 1, nks = ks ^ 1;
      sfor<6>([&](auto N) {
        constexpr int p = N, n = p >> 1, m = (p & 1) * 2;
        SB();
        acc[m][n] = mfma32(fb[ks][n], fa[ks][m], acc[m][n]);
        acc[m + 1][n] = mfma32(fb[ks][n], fa[ks][m + 1], acc[m + 1][n]);
        SB();
        if constexpr (nst < 8) {
          if constexpr (p < 3) rdB(nst, nks, p);
          if constexpr (p < 4) rdA(nst, nks, p);
        }
        if constexpr (p >= 1 && p <= 3) {
          constexpr int q = 3 * b + p - 1;
          if constexpr (q < 30) dma1(2 + q / 5, q % 5);
          else if constexpr (q < 34) dmaA(0, q - 30);
        }
        if constexpr (b == 1 && p >= 3) row_stats(p - 3);
        if constexpr (b == 13) {
          if constexpr (p == 0) {
            pin(rst); pin(cst); pin(wrow);
#pragma nounroll
            for (int spin = 0; spin < SPIN_CAP; ++spin) {
              if (__builtin_expect(__all(rst[1] == tag && rst[3] == tag && (cst[0] ^ cst[1] ^ cst[2] ^ cst[3]) == tag), 1)) break;
              __builtin_amdgcn_s_sleep(PSLEEP);
              ld_stats();
              WAITV(0);
              pin(rst); pin(cst);
            }
          }
          if constexpr (p == 1) bcast4(asf(pk2(-asf(rst[2]), -asf(rst[2]))), mh);
          if constexpr (p == 2) bcast4(asf(pk2(-asf(rst[2]) - bflo(pk2(-asf(rst[2]), 0.f)), 0.f)), ml);
          if constexpr (p == 3) bcast4(asf(rst[0]), rs);
          if constexpr (p == 4) bcast4(asf(rst[0]) * L2E, ka);
        }
        if constexpr (b == 14) {
          constexpr int q = (p + 3) % 6, fn = q >> 1, fm = (q & 1) * 2;
          acc[fm][fn] = mfma16(u32x2{cst[fn], cst[fn]}, u32x2{asu(mh[fm]), asu(ml[fm])}, acc[fm][fn]);
          acc[fm + 1][fn] = mfma16(u32x2{cst[fn], cst[fn]}, u32x2{asu(mh[fm + 1]), asu(ml[fm + 1])}, acc[fm + 1][fn]);
        }
      });
      if constexpr (b < 4) {
        if (csprod) {
#pragma unroll
          for (int n = 0; n < 3; ++n) csum[n] = mfma32(ones8, fb[ks][n], csum[n]);
          if constexpr (b == 3) cs_publish(csum, grp);
        }
      }
      if constexpr (ks == 0 && i < 7) {
        WAITL0();
        waitv<(i < 6 ? p1cnt(i) : 5)>();
        BAR();
      }
      if constexpr (b == 9) {
        ld_stats();
        ld64p(wrow, mkdesc(wphi, 32u * 512u), r16 * 32 + g * 8, (grp * 4 + wn) * 512);
      }
      if constexpr (b == 11) prefetch();
    });
    STAMP(2);
    f32x4 lg[4];
    u32x4 pk[4];
    const unsigned one = (r16 & 1) ? 0x3f800000u : 0x3f80u;
    const u32x2 wph = pk4(mfma16(wrow, u32x2{(tq == g && tp < 2) ? one : 0u, (tq == g && tp >= 2) ? one : 0u}, zero4));
#pragma unroll
    for (int m = 0; m < 4; ++m) lg[m] = mfma16(wph, pk4(acc[m][1] * ka[m]), zero4);
    hedgehog4(lg, pk, [&](auto I) {
      constexpr int i = I;
      if constexpr (i < 4) wsp_h(1 + (i >> 1), i & 1);
      if constexpr (i == 4) { wsp_rd(0); pin(pf); }
      if constexpr (i == 5 || i == 6) wsp_h(3, i - 5);
      if constexpr (i == 7) wsp_rd(1);
    });
    f32x4 kv[2] = {zero4, zero4}, kz[2] = {zero4, zero4};
    char* sc3 = smem + L_U + w * 3072;
#pragma unroll
    for (int mp = 0; mp < 2; ++mp) {
#pragma unroll
      for (int m = 0; m < 2; ++m) {
        *(u32x4*)(sc3 + (16 * m + r16) * 64 + g * 16) = pk[2 * mp + m];
        *(u32x2*)(sc3 + 2048 + (16 * m + r16) * 32 + g * 8) = pk4(acc[2 * mp + m][2] * rs[2 * mp + m]);
      }
      const int row = 8 * g + tq;
      const char* pp = sc3 + row * 64 + tp * 16;
      const char* pv = sc3 + 2048 + row * 32 + tp * 8;
      bf16x8 ap = cat8(trread(pp), trread(pp + 256));
      bf16x8 an = cat8(trread(pp + 8), trread(pp + 264));
      bf16x8 bv = cat8(trread(pv), trread(pv + 128));
      kv[0] = mfma32(ap, bv, kv[0]);
      kv[1] = mfma32(an, bv, kv[1]);
      kz[0] = mfma32(ap, ones8, kz[0]);
      kz[1] = mfma32(an, ones8, kz[1]);
      wsp_h(4, mp);
    }
#pragma unroll
    for (int m = 0; m < 4; ++m) lg[m] = mfma16(wph, pk4(acc[m][0] * ka[m]), zero4);
    *(f32x4*)(sc3 + lane * 16) = wm ? kv[0] : kv[1];
    if (r16 == 0) *(f32x4*)(sc3 + 1024 + g * 16) = wm ? kz[0] : kz[1];
    wsp_rd(2);
    LBAR();
    {
      const char* p = smem + L_U + (w ^ 4) * 3072;
      const f32x4 a = (wm ? kv[1] : kv[0]) + *(const f32x4*)(p + lane * 16);
      const f32x4 b = (wm ? kz[1] : kz[0]) + *(const f32x4*)(p + 1024 + g * 16);
      const int base = WS_KVP + (grp * 16 + rt16) * KVP_SZ;
      const int hs = wn * 2 + wm;
      st128d(u32x4{asu(a[0]), tag, asu(a[1]), tag}, wsd, lane * 16, base + hs * 2048);
      st128d(u32x4{asu(a[2]), tag, asu(a[3]), tag}, wsd, lane * 16, base + hs * 2048 + 1024);
      const float v0 = r16 ? b[2] : b[0], v1 = r16 ? b[3] : b[1];
      st128d(u32x4{asu(v0), tag, asu(v1), tag}, wsd, r16 < 2 ? g * 32 + r16 * 16 : DROP, base + 16384 + hs * 128);
    }
    STAMP(4);
    hedgehog4(lg, pq, [&](auto I) {
      constexpr int i = I;
      if constexpr (i < 2) wsp_h(5, i);
      if constexpr (i == 2) { wsp_rd(3); ld64ds(zfl, wsd, flzv, flzs); }
      if constexpr (i == 3 || i == 4) wsp_h(6, i - 3);
      if constexpr (i == 5) wsp_rd(4);
      if constexpr (i >= 6) wsp_h(7, i - 6);
    });
    ld128d(la, wsd, l1off, WS_KVP + grp * 16 * KVP_SZ + l1sub * 128);
    ld128d(lb, wsd, l1off, WS_KVP + (grp * 16 + 8) * KVP_SZ + l1sub * 128);
    STAMP(5);
  } else {
    const u32x4 wd = mkdesc(win, 512u * 2752u);
    auto dmaX = [&](int j) { dma16(xd, xsrc[0], ((j + 2 * rq) & 7) * 128, L_U + j * 8192 + w * 1024); };
    auto dmaW = [&](int j, int u) {
      dma16(wd, wsrc[u], ((j + 2 * rq) & 7) * (64 * 2752) + (j < 8 ? 1376 : 0), (j & 3) * L_ZW + (3 * w + u) * 1024);
    };
    dmaX(0); dmaW(0, 0); dmaW(0, 1); dmaW(0, 2);
    ld_first();
    dmaX(1); dmaW(1, 0); dmaW(1, 1); dmaW(1, 2);
    int aoff[2], boff[3];
#pragma unroll
    for (int ks = 0; ks < 2; ++ks) aoff[ks] = L_U + (wm * 32 + r16) * 128 + (((4 * ks + g) ^ ((r16 >> 1) & 7)) << 4);
#pragma unroll
    for (int i = 0; i < 3; ++i) boff[i] = (8 * g + tq) * 384 + (((2 * (3 * wn + i) + (tp >> 1)) ^ (h2r << 1)) << 4) + (tp & 1) * 8;
    f32x4 acc[2][2][3], csum[3];
#pragma unroll
    for (int i = 0; i < 3; ++i) { acc[0][0][i] = zero4; acc[0][1][i] = zero4; acc[1][0][i] = zero4; acc[1][1][i] = zero4; }
    bf16x8 fa[2][2], fb[2][3];
    float irs[2], ka[2], kb[2], mh[2], ml[2];
    u32x4 zs[2];
    const bool csprod = rt32 < 4 && wm == 0;
    const int nv = znft - 3 * wn;
    auto ld_rst = [&]() { ld128d(rst, wsd, (lane & 31) * 16, WS_RST + (rt32 * 64 + wm * 32) * 16); };
    auto ld_cst = [&](int pass) { ld128d(cst, wsd, lane * 16, WS_CS + ((8 + pass * 4 + zg) * 4 + wn) * 1024); };
    auto ld_zs = [&]() { ld128d(zs[0], wsd, lane * 16, WS_ZST + (rt32 * 16 + 2 * w) * 1024); ld128d(zs[1], wsd, lane * 16, WS_ZST + (rt32 * 16 + 2 * w + 1) * 1024); };
    auto rdA = [&](int j, int ks, int m) { fa[ks][m] = *(const bf16x8*)(smem + (j & 7) * 8192 + aoff[ks] + m * 2048); };
    auto rdB = [&](int j, int ks, int n) {
      const char* p = smem + (j & 3) * L_ZW + boff[n] + ks * 12288;
      fb[ks][n] = cat8(trread(p), trread(p + 1536));
    };
    waitv<6>();
    BAR();
    STAMP(1);
    rdB(0, 0, 0); rdA(0, 0, 0); rdA(0, 0, 1); rdB(0, 0, 1); rdB(0, 0, 2);
    sfor<32>([&](auto B) {
      constexpr int b = B, j = b >> 1, ks = b & 1, ps = b >> 4, bl = b & 15;
      constexpr int nj = (b + 1) >> 1, nks = ks ^ 1;
      sfor<3>([&](auto N) {
        constexpr int n = N;
        SB();
        acc[ps][0][n] = mfma32(fb[ks][n], fa[ks][0], acc[ps][0][n]);
        acc[ps][1][n] = mfma32(fb[ks][n], fa[ks][1], acc[ps][1][n]);
        SB();
        if constexpr (nj < 16) {
          if constexpr (n == 0) { rdB(nj, nks, 0); rdA(nj, nks, 0); rdA(nj, nks, 1); }
          if constexpr (n == 1) { rdB(nj, nks, 1); rdB(nj, nks, 2); }
        }
        if constexpr (ZS.first[b] + n < ZS.first[b + 1]) {
          constexpr int q = ZS.first[b] + n;
          if constexpr (q < 24) { if constexpr (q % 4 == 0) dmaX(2 + q / 4); else dmaW(2 + q / 4, q % 4 - 1); }
          else if constexpr (q < 48) dmaW(8 + (q - 24) / 3, (q - 24) % 3);
          else dmaA(0, q - 48);
        }
        if constexpr (b == 1 && n < 2) row_stats(n);
      });
      if constexpr (bl < 4) {
        if (csprod) {
#pragma unroll
          for (int n = 0; n < 3; ++n) csum[n] = mfma32(ones8, fb[ks][n], bl == 0 ? zero4 : csum[n]);
          if constexpr (bl == 3) cs_publish(csum, 8 + ps * 4 + zg);
        }
      }
      if constexpr (bl == 13) {
        pin(rst); pin(cst);
#pragma nounroll
        for (int spin = 0; spin < SPIN_CAP; ++spin) {
          if (__builtin_expect(__all(rst[1] == tag && rst[3] == tag && (cst[0] ^ cst[1] ^ cst[2] ^ cst[3]) == tag), 1)) break;
          __builtin_amdgcn_s_sleep(PSLEEP);
          ld_rst(); ld_cst(ps);
          WAITV(0);
          pin(rst); pin(cst);
        }
        if constexpr (ps == 0) {
          const float mu = -asf(rst[2]), r = asf(rst[0]);
          bcast2(asf(pk2(mu, mu)), mh);
          bcast2(asf(pk2(mu - bflo(pk2(mu, 0.f)), 0.f)), ml);
          bcast2(__builtin_amdgcn_rcpf(r), irs);
          bcast2(r * (-2.0f * 0.7978845608028654f * L2E), ka);
          bcast2(r * r * r * (-2.0f * 0.7978845608028654f * L2E * 0.044715f), kb);
        }
#pragma unroll
        for (int n = 0; n < 3; ++n)
#pragma unroll
          for (int m = 0; m < 2; ++m)
            acc[ps][m][n] = mfma32(asb8(u32x4{cst[n], cst[n], 0u, 0u}), asb8(u32x4{asu(mh[m]), asu(ml[m]), 0u, 0u}), acc[ps][m][n]);
      }
      if constexpr (b == 24) {
        waitv<ZS.since(19, 24)>();
#pragma nounroll
        for (int spin = 0; spin < SPIN_CAP; ++spin) {
          pin(zs[0]); pin(zs[1]);
          if (__builtin_expect(__all(zs[0][1] == tag && zs[0][3] == tag && zs[1][1] == tag && zs[1][3] == tag), 1)) break;
          __builtin_amdgcn_s_sleep(2);
          ld_zs();
          WAITV(0);
        }
        *(f32x2*)(smem + L_U + 24576 + w * 512 + lane * 8) = f32x2{asf(zs[0][0]) + asf(zs[1][0]), asf(zs[0][2]) + asf(zs[1][2])};
        if (tid == 0) *(unsigned*)(smem + L_U + 28672) = 0u;
      }
      if constexpr (ks == 0 && j < 15) {
        WAITL0();
        waitv<(j == 6 ? cmin(ZS.cnt(6), ZS.since(7, 12)) : (j == 14 ? cmin(ZS.cnt(14), ZS.since(22, 28)) : ZS.cnt(j)))>();
        BAR();
      }
      if constexpr (b == 1) row_stats(2);
      if constexpr (b == 7) { ld_rst(); ld_cst(0); }
      if constexpr (b == 15) {
        STAMP(2);
        gelu24(acc[0], ka, kb, irs, [](auto) {});
        float sq[4];
#pragma unroll
        for (int m = 0; m < 2; ++m) {
          f32x4 s4 = zero4, q4 = zero4;
#pragma unroll
          for (int n = 0; n < 3; ++n) { const f32x4 y = n < nv ? acc[0][m][n] : zero4; s4 += y; q4 = __builtin_elementwise_fma(y, y, q4); }
          sq[m] = sum4(s4); sq[2 + m] = sum4(q4);
        }
        const float r = redg4<false>(sq);
        auto t = __builtin_amdgcn_permlane32_swap(asu(r), asu(r), false, false);
        st128d(u32x4{t[0], tag, t[1], tag}, wsd, lane < 32 ? lane * 16 : DROP, WS_ZST + (rt32 * 16 + zg * 4 + wn) * 1024 + wm * 512);
        STAMP(4);
      }
      if constexpr (b == 19) ld_zs();
      if constexpr (b == 22) ld_cst(1);
      if constexpr (b == 25) {
        STAMP(6);
        f32x2 sq = {0.f, 0.f};
#pragma unroll
        for (int i = 0; i < 8; ++i) sq += *(const f32x2*)(smem + L_U + 24576 + i * 512 + (wm * 32 + (lane & 31)) * 8);
        const float mean = sq[0] * (1.0f / 688.0f);
        const float var = fmx(sq[1] * (1.0f / 688.0f) - mean * mean, 0.f);
        const float r = __builtin_amdgcn_rsqf(var + 1e-5f);
        float zr[2], zc[2];
        bcast2(r, zr);
        bcast2(-mean * r, zc);
        char* scr = smem + L_U + w * 3072;
#pragma unroll
        for (int n = 0; n < 3; ++n)
#pragma unroll
          for (int m = 0; m < 2; ++m) *(u32x2*)(scr + n * 1024 + (16 * m + r16) * 32 + g * 8) = pk4(acc[0][m][n] * zr[m] + zc[m]);
        const int dst = WS_Z2F + ((((rt32 >> 2) * 2 + wm) * 44 + zg * 12 + 3 * wn) * 4 + (rt32 & 3)) * 1024;
#pragma unroll
        for (int n = 0; n < 3; ++n) {
          const char* p = scr + n * 1024 + (8 * g + tq) * 32 + tp * 8;
          st128d(__builtin_bit_cast(u32x4, cat8(trread(p), trread(p + 128))), wsd, n < nv ? lane * 16 : DROP, dst + n * 4096);
        }
        STAMP(17);
      }
      if constexpr (b == 27) {
        WAITV(0);
        if (lane == 0) {
          if (__hip_atomic_fetch_add((LAS unsigned*)(smem + L_U + 28672), 1u, __ATOMIC_RELAXED, __HIP_MEMORY_SCOPE_WORKGROUP) == 7u)
            __builtin_amdgcn_raw_buffer_store_b64(u32x2{tag, tag ^ TAGX}, wsr, WS_FLZ + zg * 1024 + rt32 * 8, 0, 16);
        }
        STAMP(18);
      }
      if constexpr (b == 28) prefetch();
    });
    STAMP(19);
    wsp_is(1);
    LBAR();
    gelu24(acc[1], ka, kb, irs, [&](auto I) {
      constexpr int i = I;
      if constexpr (i == 0) wsp_is(2);
      if constexpr (i == 1) { wsp_rd(0); pin(pf); }
      if constexpr (i == 2) wsp_is(3);
      if constexpr (i == 3) wsp_rd(1);
    });
#pragma unroll
    for (int n = 0; n < 3; ++n) {
      const u32x2 a = pk4(acc[1][0][n]), b = pk4(acc[1][1][n]);
      st128d(u32x4{a[0], a[1], b[0], b[1]}, wsd, n < nv ? lane * 16 : DROP, WS_Z1F + ((rt32 * 2 + wm) * 44 + zg * 12 + 3 * wn + n) * 1024);
    }
    STAMP(20);
    wsp_is(4); wsp_rd(2);
    wsp_is(5); wsp_rd(3);
    ld64ds(zfl, wsd, flzv, flzs);
    wsp_is(6); wsp_rd(4);
    wsp_is(7);
    ld128d(la, wsd, lane * 16, WS_KVP);
    ld128d(lb, wsd, lane * 16, WS_KVP);
#pragma unroll
    for (int m = 0; m < 4; ++m) pq[m] = u32x4{0u, 0u, 0u, 0u};
    STAMP(5);
  }
  int lz = lane;
  asm volatile("" : "+v"(lz));
  const int l16 = lz * 16;
  unsigned biasv[2];
#pragma unroll
  for (int t = 0; t < 2; ++t) ldu16(biasv[t], mkdesc(bsp, 4096u), r16 * 2, (rt64 * 64 + th * 32 + t * 16) * 2);
  auto zmask = [&](u32x2 f) -> unsigned { return (unsigned)__ballot(f[0] == tag && f[1] == (tag ^ TAGX)); };
  unsigned rdy;
  auto need_z = [&](int c) {
    if (__builtin_expect(((rdy >> (4 * c)) & 15u) != 15u, 0)) {
#pragma nounroll
      for (int spin = 0; spin < SPIN_CAP; ++spin) {
        rdy = zmask(poll64(wsd, (((l16 >> 4) + 4 * rot2) & 31) * 8, flzs));
        if (((rdy >> (4 * c)) & 15u) == 15u) break;
        __builtin_amdgcn_s_sleep(24);
      }
    }
  };
  const int bsrc = WS_Z2F + ft0 * 4096 + w * 1024;
  const int u2off = npair == 3 ? 16384 : 0;
  auto dma2 = [&](int st, int u) {
    const int n = 2 * u + (w >> 2), q = w & 3;
    dma16d(wsd, l16, bsrc + ((st + 2 * rot2) & 15) * (44 * 4096) + (u < 2 ? u * 8192 : u2off), ring2(st) + (n ^ (u < 2 ? q : (q & 1))) * 4096 + q * 1024);
  };
  waitv<12>();
  pin(zfl);
  rdy = zmask(zfl);
  wsp_ld(5);
  need_z(0);
  BAR();
#pragma unroll
  for (int st = 0; st < 2; ++st)
#pragma unroll
    for (int u = 0; u < 3; ++u) dma2(st, u);
  STAMP(7);
  SB(); waitv<14>(); wsp_ld(6);
  SB(); waitv<6>(); wsp_ld(7);
  pin(biasv[0]); pin(biasv[1]); pin(la); pin(lb);
  if (isqkv) {
#pragma nounroll
    for (int rep = 0; rep < ((w == 0 && rt16 < 8) ? 2 : 1); ++rep) {
      const int sub = rep ? 128 + rt16 : l1sub;
#pragma nounroll
      for (int spin = rep ? -1 : 0; spin < SPIN_CAP; ++spin) {
        if (spin >= 0) {
          if (__builtin_expect(__all(la[1] == tag && la[3] == tag && lb[1] == tag && lb[3] == tag), 1)) break;
          __builtin_amdgcn_s_sleep(PSLEEP);
        }
        ld128d(la, wsd, l1off, WS_KVP + grp * 16 * KVP_SZ + sub * 128);
        ld128d(lb, wsd, l1off, WS_KVP + (grp * 16 + 8) * KVP_SZ + sub * 128);
        WAITV(0);
        pin(la); pin(lb);
      }
      float v0 = asf(la[0]) + asf(lb[0]), v1 = asf(la[2]) + asf(lb[2]);
      v0 = dppadd<0x128>(v0); v1 = dppadd<0x128>(v1);
      redg2<false>(v0, v1);
      const int dsto = rep ? (sub * 8 + lane) * 8 : ((sub >> 4) * 64 + (sub & 7) * 8 + lane) * 16 + ((sub >> 3) & 1) * 8;
      __builtin_amdgcn_raw_buffer_store_b64(u32x2{pk2(v0, v1), tag}, wsr, lane < 8 ? dsto : DROP, WS_KVR + grp * 1088 * 8, 16);
    }
    waitv<4>();
  } else {
    waitv<3>();
  }
  STAMP(8);
  u32x4 kr[4];
  const int kroff = WS_KVR + (grp * 1088 + wn * 256) * 8;
  const int kzoff = WS_KVR + (grp * 1088 + 1024 + wn * 16) * 8;
  auto ld_kr = [&]() {
    int b = (l16 >> 4) & 48;
    asm volatile("" : "+v"(b));
#pragma unroll
    for (int j = 0; j < 2; ++j) { ld128d(kr[j], wsd, l16, kroff + j * 1024); ld128d(kr[2 + j], wsd, b, kzoff + j * 64); }
  };
  const int w3off = (lz >> 3) * 1024 + cgp * 128 + (((lz & 7) ^ ((lz >> 4) << 1)) << 4);
  const u32x4 od = mkdesc(wo, 512u * 1024u);
  f32x4 ac2[6][2];
#pragma unroll
  for (int n = 0; n < 6; ++n)
#pragma unroll
    for (int t = 0; t < 2; ++t) { const float b = kq == 0 ? bflo(biasv[t]) : 0.f; ac2[n][t] = f32x4{b, b, b, b}; }
  bf16x8 fz[2][6];
  auto rdZ = [&](int st, int n) { fz[st & 1][n] = *(const bf16x8*)(smem + ring2(st) + n * 4096 + kq * 1024 + l16); };
  u32x4 ao[4][2];
  u32x4 z1v[2];
  u32x2 ofl, z1fl;
  f32x4 onum[4];
  float oden[4];
  const int a3 = WS_A3F + ((rt64 * 2 + th) * 76 + 2 * kq) * 1024;
  const int z1flo = flzs + 512 + rt64 * 8;
  const int oflo = WS_FLO + rt64 * 256;
  LBAR();
  STAMP(10);
#pragma unroll
  for (int m = 0; m < 4; ++m) *(u32x4*)(smem + L_WO + 32768 + w * 4096 + m * 1024 + l16) = pq[m];
#pragma unroll
  for (int n = 0; n < 6; ++n) rdZ(0, n);
  need_z(1);
#pragma unroll
  for (int st = 2; st < 4; ++st)
#pragma unroll
    for (int u = 0; u < 3; ++u) dma2(st, u);
  sfor<16>([&](auto ST) {
    constexpr int st = ST;
    if constexpr (st < 15) {
      WAITL0();
      if constexpr (p2cnt(st, true) != p2cnt(st, false)) { if (isqkv) waitv<p2cnt(st, true)>(); else waitv<p2cnt(st, false)>(); }
      else waitv<p2cnt(st, false)>();
      BAR();
    }
    sfor<6>([&](auto N) {
      constexpr int n = N;
      SB();
      ac2[n][0] = mfma32(fz[st & 1][n], af[st >> 1][st & 1][0], ac2[n][0]);
      ac2[n][1] = mfma32(fz[st & 1][n], af[st >> 1][st & 1][1], ac2[n][1]);
      SB();
      if constexpr (st < 15 && n >= 1 && n <= 3) { rdZ(st + 1, 2 * n - 2); rdZ(st + 1, 2 * n - 1); }
      if constexpr (st + 4 < 16 && n == 0 && (st & 1) == 0) need_z((st + 4) >> 1);
      if constexpr (st + 4 < 16 && n < 3) dma2(st + 4, n);
      if constexpr (n == 3) {
        if constexpr ((st & 4) == 0) dma16(od, w3off, ((st & 3) * 8 + (st >> 3) * 32 + w) * 8192, L_WO + ((st & 3) * 8 + (st >> 3) * 32 + w) * 1024);
        if constexpr (st == 2) { if (isqkv) ld_kr(); }
        if constexpr (st == 2) {
          __builtin_amdgcn_raw_buffer_store_b64(u32x2{tag, tag ^ TAGX}, wsr, (l16 == 0 && !isqkv && w == 1) ? 0 : DROP, WS_FLZ + zg * 1024 + 512 + rt32 * 8, 16);
        }
        if constexpr (st == 6) {
          if (isqkv) {
#pragma nounroll
            for (int spin = 0; spin < SPIN_CAP; ++spin) {
              bool ok = true;
#pragma unroll
              for (int j = 0; j < 4; ++j) { pin(kr[j]); ok = ok && kr[j][1] == tag && kr[j][3] == tag; }
              if (__builtin_expect(__all(ok), 1)) break;
              __builtin_amdgcn_s_sleep(PSLEEP);
              ld_kr();
              WAITV(0);
            }
            const bf16x8 kvo = asb8(u32x4{kr[0][0], kr[0][2], kr[1][0], kr[1][2]});
            const bf16x8 zo = asb8(u32x4{kr[2][0], kr[2][2], kr[3][0], kr[3][2]});
#pragma unroll
            for (int m = 0; m < 4; ++m) {
              const bf16x8 f = *(const bf16x8*)(smem + L_WO + 32768 + w * 4096 + m * 1024 + l16);
              onum[m] = mfma32(kvo, f, zero4); oden[m] = mfma32(zo, f, zero4)[0];
            }
          }
        }
        if constexpr (st == 7) {
          if (isqkv) {
            u32x2 ov[4];
#pragma unroll
            for (int m = 0; m < 4; ++m) ov[m] = pk4(onum[m] * __builtin_amdgcn_rcpf(oden[m] + 1e-6f));
#pragma unroll
            for (int mp = 0; mp < 2; ++mp)
              st128d(u32x4{ov[2 * mp][0], ov[2 * mp][1], ov[2 * mp + 1][0], ov[2 * mp + 1][1]}, wsd,
                     l16, WS_A3F + ((rt16 * 4 + wm * 2 + mp) * 76 + grp * 4 + wn) * 1024);
          }
        }
        if constexpr (st == 11) {
          __builtin_amdgcn_raw_buffer_store_b64(u32x2{tag, tag ^ TAGX}, wsr, (l16 == 0 && isqkv) ? 0 : DROP, WS_FLO + ((rt16 * 2 + wm) * 32 + grp * 4 + wn) * 8, 16);
          ld64ds(z1fl, wsd, 0, z1flo);
        }
        if constexpr (st == 13) {
          pin(z1fl);
#pragma nounroll
          for (int spin = 0; spin < SPIN_CAP; ++spin) {
            if (__builtin_expect(__all(z1fl[0] == tag && z1fl[1] == (tag ^ TAGX)), 1)) break;
            __builtin_amdgcn_s_sleep(PSLEEP);
            z1fl = poll64(wsd, 0, z1flo);
          }
#pragma unroll
          for (int e = 0; e < 2; ++e)
            ld128d(z1v[e], wsd, l16, WS_Z1F + ((rt64 * 2 + th) * 44 + ft0 + min(kq + 4 * e, 2 * npair - 1)) * 1024);
        }
        if constexpr (st == 15) ld64ds(ofl, wsd, (l16 >> 1) & 255, oflo);
      }
    });
  });
  STAMP(11);
  {
    auto rblk = [&](int n) { const int i = th * 6 + n; return (i < 8 ? 114688 + i * 6144 : (i - 8) * 6144) + l16; };
#pragma unroll
    for (int i = 1; i < 4; ++i)
#pragma unroll
      for (int t = 0; t < 2; ++t) *(f32x4*)(smem + rblk(i ^ kq) + (((i - 1) * 2 + t) << 10)) = ac2[i][t];
#pragma unroll
    for (int t = 0; t < 2; ++t) *(f32x4*)(smem + rblk(5 ^ (kq & 1)) + (((kq & 2) * 2 + t) << 10)) = ac2[5][t];
    if (kq >= 2) {
#pragma unroll
      for (int t = 0; t < 2; ++t) *(f32x4*)(smem + rblk(4 ^ (kq & 1)) + ((2 + t) << 10)) = ac2[4][t];
    }
    waitv<0>();
    pin(z1v[0]); pin(z1v[1]); pin(ofl);
    LBAR();
#pragma unroll
    for (int e = 0; e < 2; ++e) {
      const int n = kq + 4 * e;
      if (e == 0 || n < 2 * npair) {
        u32x4 o;
#pragma unroll
        for (int t = 0; t < 2; ++t) {
          f32x4 s = ac2[4 * e][t];
#pragma unroll
          for (int j = 0; j < 3; ++j) s += *(const f32x4*)(smem + rblk(n) + ((j * 2 + t) << 10));
          o[2 * t] = pk2(bflo(z1v[e][2 * t]) * s[0], bfhi(z1v[e][2 * t]) * s[1]);
          o[2 * t + 1] = pk2(bflo(z1v[e][2 * t + 1]) * s[2], bfhi(z1v[e][2 * t + 1]) * s[3]);
        }
        if (ft0 + n >= 43) o = u32x4{0u, 0u, 0u, 0u};
        st128d(o, wsd, l16, WS_A3F + ((rt64 * 2 + th) * 76 + 32 + ft0 + n) * 1024);
      }
    }
  }
  STAMP(12);
#pragma nounroll
  for (int spin = 0; spin < SPIN_CAP; ++spin) {
    if (__builtin_expect(__all(ofl[0] == tag && ofl[1] == (tag ^ TAGX)), 1)) break;
    __builtin_amdgcn_s_sleep(PSLEEP);
    ofl = poll64(wsd, (l16 >> 1) & 255, oflo);
  }
#pragma unroll
  for (int i = 0; i < 4; ++i)
#pragma unroll
    for (int h = 0; h < 2; ++h) ld128d(ao[i][h], wsd, l16, a3 + (8 * i + h) * 1024);
  f32x4 ac3[4][2];
#pragma unroll
  for (int n = 0; n < 4; ++n) { ac3[n][0] = zero4; ac3[n][1] = zero4; }
  int ly = l16;
  asm volatile("" : "+v"(ly));
  const int lane3 = ly >> 4, r3 = lane3 & 15, g3 = lane3 >> 4, tid3 = w * 64 + lane3;
  int b3[4];
  {
    const int k = 4 * g3 + (r3 >> 2), tp3 = r3 & 3;
    const int h2 = ((k >> 1) & 1) | (((k >> 2) & 1) << 1);
#pragma unroll
    for (int n = 0; n < 4; ++n) b3[n] = k * 128 + (((2 * n + (tp3 >> 1)) ^ (h2 << 1)) << 4) + (tp3 & 1) * 8 + kq * 4096;
  }
  const u32x4 ud = mkdesc(wout, 688u * 1024u);
  const int go = (rt64 * 64 + (tid3 >> 3)) * 1024 + cgp * 128 + (tid3 & 7) * 16;
  u32x4 xres;
  waitv<8>();
  LBAR();
  if (tid3 == 0) __builtin_amdgcn_raw_buffer_store_b64(u32x2{tag, tag ^ TAGX}, wsr, WS_FLU + rt64 * 512 + cgp * 8, 0, 16);
  STAMP(13);
  sfor<4>([&](auto I) {
    constexpr int i = I;
    bf16x8 b[4];
#pragma unroll
    for (int n = 0; n < 4; ++n) b[n] = cat8(trread(smem + L_WO + b3[n] + i * 16384), trread(smem + L_WO + b3[n] + i * 16384 + 2048));
    waitv<6 - 2 * i + 3 * i>();
    pin(ao[i][0]); pin(ao[i][1]);
#pragma unroll
    for (int j = 3 * i; j < 3 * i + 3; ++j)
      if (j < 11) dma16(ud, w3off, min(j * 8 + w, 85) * 8192, wout_at(j >> 1) + ((j & 1) * 8 + w) * 1024);
#pragma unroll
    for (int n = 0; n < 4; ++n)
#pragma unroll
      for (int t = 0; t < 2; ++t)
        ac3[n][t] = mfma32(b[n], asb8(u32x4{ao[i][0][2 * t], ao[i][0][2 * t + 1], ao[i][1][2 * t], ao[i][1][2 * t + 1]}), ac3[n][t]);
  });
  asm volatile("buffer_load_dwordx4 %0, %1, %2, 0 offen" : "=v"(xres) : "v"(go), "s"(xd) : "memory");
  if (bid == 0 && w == 7) {
#pragma nounroll
    for (int spin = 0; spin < SPIN_CAP; ++spin) {
      u32x4 f[4];
#pragma unroll
      for (int j = 0; j < 4; ++j) ld128d(f[j], wsd, lane3 * 128, WS_RST + j * 8192);
      WAITV(0);
#pragma unroll
      for (int j = 0; j < 4; ++j) pin(f[j]);
      if (__all(f[0][1] == tag && f[1][1] == tag && f[2][1] == tag && f[3][1] == tag)) break;
    }
    if (lane3 == 0) __builtin_amdgcn_raw_buffer_store_b32(tag, wsr, WS_EP, 0, 16);
  }
  if (w == 0) {
#pragma nounroll
    for (int spin = 0; spin < SPIN_CAP; ++spin) {
      u32x2 f = poll64(wsd, (l16 >> 1) & 63, WS_FLU + rt64 * 512);
      if (__all(f[0] == tag && f[1] == (tag ^ TAGX))) break;
      __builtin_amdgcn_s_sleep(PSLEEP);
    }
  }
  WAITV(0);
  LBAR();
  u32x4 au[6][2];
#pragma unroll
  for (int i = 0; i < 6; ++i)
#pragma unroll
    for (int h = 0; h < 2; ++h) ld128d(au[i][h], wsd, l16, a3 + (32 + h + ((i < 5 || kq < 2) ? 8 * i : 32)) * 1024);
  STAMP(14);
  sfor<6>([&](auto I) {
    constexpr int i = I;
    bf16x8 b[4];
    const int io = (i < 5 || kq < 2) ? wout_at(i) : 0;
#pragma unroll
    for (int n = 0; n < 4; ++n) b[n] = cat8(trread(smem + b3[n] + io), trread(smem + b3[n] + io + 2048));
    waitv<10 - 2 * i>();
    pin(au[i][0]); pin(au[i][1]);
    if (i < 5 || kq < 2) {
#pragma unroll
      for (int n = 0; n < 4; ++n)
#pragma unroll
        for (int t = 0; t < 2; ++t)
          ac3[n][t] = mfma32(b[n], asb8(u32x4{au[i][0][2 * t], au[i][0][2 * t + 1], au[i][1][2 * t], au[i][1][2 * t + 1]}), ac3[n][t]);
    }
  });
  STAMP(15);
#pragma unroll
  for (int n = 0; n < 4; ++n)
#pragma unroll
    for (int t = 0; t < 2; ++t)
      *(f32x4*)(smem + L_WO + kq * 16384 + (th * 32 + t * 16 + r3) * 256 + (((n * 4 + g3) ^ r3) << 4)) = ac3[n][t];
  LBAR();
  {
    pin(xres);
    const int row = tid3 >> 3, c8 = tid3 & 7;
    f32x4 s0 = {bflo(xres[0]), bfhi(xres[0]), bflo(xres[1]), bfhi(xres[1])};
    f32x4 s1 = {bflo(xres[2]), bfhi(xres[2]), bflo(xres[3]), bfhi(xres[3])};
#pragma unroll
    for (int j = 0; j < 4; ++j) {
      s0 += *(const f32x4*)(smem + L_WO + j * 16384 + row * 256 + (((2 * c8) ^ (row & 15)) << 4));
      s1 += *(const f32x4*)(smem + L_WO + j * 16384 + row * 256 + (((2 * c8 + 1) ^ (row & 15)) << 4));
    }
    const u32x2 lo = pk4(s0), hi = pk4(s1);
    st128d(u32x4{lo[0], lo[1], hi[0], hi[1]}, mkdesc(out, 2048u * 1024u), go, 0);
  }
  STAMP(16);
}

extern "C" void kernel_launch(void* const* d_in, const int* in_sizes, int n_in, void* d_out, int out_size, void* d_ws,
                              size_t ws_size, hipStream_t stream) {
  (void)hipFuncSetAttribute((const void*)fused, hipFuncAttributeMaxDynamicSharedMemorySize, LDS_BYTES);
  fused<<<256, 512, LDS_BYTES, stream>>>((const char*)d_in[0], (const char*)d_in[1], (const char*)d_in[2],
                                         (const char*)d_in[3], (const char*)d_in[5], (const char*)d_in[6],
                                         (const char*)d_in[7], (const char*)d_in[8], (char*)d_out, (char*)d_ws);
}
```

```cpp
#include <hip/hip_runtime.h>
#include <hip/hip_bf16.h>
#include <stdint.h>

typedef __bf16 bf16x8 __attribute__((ext_vector_type(8)));
typedef __bf16 bf16x2 __attribute__((ext_vector_type(2)));
typedef short s16x4 __attribute__((ext_vector_type(4)));
typedef short s16x8 __attribute__((ext_vector_type(8)));
typedef float f32x4 __attribute__((ext_vector_type(4)));
typedef float f32x2 __attribute__((ext_vector_type(2)));
typedef unsigned u32x4 __attribute__((ext_vector_type(4)));
typedef unsigned u32x2 __attribute__((ext_vector_type(2)));
#define LAS __attribute__((address_space(3)))
#define DI __device__ __forceinline__

constexpr int WS_EP  = 0;
constexpr int WS_FLZ = 1024;
constexpr int WS_FLO = WS_FLZ + 8192;
constexpr int WS_FLU = WS_FLO + 8192;
constexpr int WS_CS  = WS_FLU + 16384;
constexpr int WS_RST = WS_CS + 16 * 4 * 1024;
constexpr int WS_ZST = WS_RST + 2048 * 16;
constexpr int KVP_SZ = 1088 * 16;
constexpr int WS_KVP = WS_ZST + 16 * 8 * 128 * 16;
constexpr int WS_KVR = WS_KVP + 8 * 16 * KVP_SZ;
constexpr int WS_Z2F = WS_KVR + 8 * 1088 * 8;
constexpr int WS_Z1F = WS_Z2F + 16 * 44 * 4096;
constexpr int WS_A3F = WS_Z1F + 64 * 44 * 1024;
constexpr int WS_END = WS_A3F + 64 * 76 * 1024;
static_assert(WS_Z2F % 128 == 0 && WS_KVP % 128 == 0 && WS_KVR % 128 == 0, "alignment");

template <int V> struct IC { static constexpr int value = V; constexpr operator int() const { return V; } };
template <int I, int N, class F> DI void sfor_(F&& f) { if constexpr (I < N) { f(IC<I>{}); sfor_<I + 1, N>(f); } }
template <int N, class F> DI void sfor(F&& f) { sfor_<0, N>(f); }
template <int N> DI void waitv() { asm volatile("s_waitcnt vmcnt(%0)" :: "n"(N) : "memory"); }

DI float asf(unsigned u) { return __builtin_bit_cast(float, u); }
DI unsigned asu(float f) { return __builtin_bit_cast(unsigned, f); }
DI u32x4 mkdesc(const void* p, unsigned bytes) {
  unsigned long long a = (unsigned long long)p;
  u32x4 d = {(unsigned)a, (unsigned)(a >> 32) & 0xffffu, bytes, 0x00020000u};
  return d;
}
DI void dma16(u32x4 desc, int voff, int soff, int lds) {
  asm volatile("s_mov_b32 m0, %3\n\ts_nop 0\n\tbuffer_load_dwordx4 %0, %1, %2 offen lds"
               :: "v"(voff), "s"(desc), "s"(soff), "s"(lds) : "memory");
}
DI void dma16d(u32x4 desc, int voff, int soff, int lds) {
  asm volatile("s_mov_b32 m0, %3\n\ts_nop 0\n\tbuffer_load_dwordx4 %0, %1, %2 offen sc1 lds"
               :: "v"(voff), "s"(desc), "s"(soff), "s"(lds) : "memory");
}
DI u32x2 poll64(u32x4 desc, int voff, int soff) {
  u32x2 r;
  asm volatile("buffer_load_dwordx2 %0, %1, %2, %3 offen sc1\n\ts_waitcnt vmcnt(0)" : "=&v"(r) : "v"(voff), "s"(desc), "s"(soff) : "memory");
  return r;
}
DI void ld128d(u32x4& r, u32x4 desc, int voff, int soff) {
  asm volatile("buffer_load_dwordx4 %0, %1, %2, %3 offen sc1" : "=v"(r) : "v"(voff), "s"(desc), "s"(soff) : "memory");
}
DI void ld64d(u32x2& r, u32x4 desc, int voff) {
  asm volatile("buffer_load_dwordx2 %0, %1, %2, 0 offen sc1" : "=v"(r) : "v"(voff), "s"(desc) : "memory");
}
DI void ld64ds(u32x2& r, u32x4 desc, int voff, int soff) {
  asm volatile("buffer_load_dwordx2 %0, %1, %2, %3 offen sc1" : "=v"(r) : "v"(voff), "s"(desc), "s"(soff) : "memory");
}
DI void ld64p(u32x2& r, u32x4 desc, int voff, int soff) {
  asm volatile("buffer_load_dwordx2 %0, %1, %2, %3 offen" : "=v"(r) : "v"(voff), "s"(desc), "s"(soff) : "memory");
}
DI void ldu16(unsigned& r, u32x4 desc, int voff, int soff) {
  asm volatile("buffer_load_ushort %0, %1, %2, %3 offen" : "=v"(r) : "v"(voff), "s"(desc), "s"(soff) : "memory");
}
DI void st128d(u32x4 v, u32x4 desc, int voff, int soff) {
  asm volatile("buffer_store_dwordx4 %0, %1, %2, %3 offen sc1\n\ts_nop 1" :: "v"(v), "v"(voff), "s"(desc), "s"(soff) : "memory");
}
DI void gld32(unsigned& r, const char* p) { asm volatile("global_load_dword %0, %1, off" : "=&v"(r) : "v"(p) : "memory"); }
DI void pin(u32x4& v) { asm volatile("" : "+v"(v)); }
DI void pin(u32x2& v) { asm volatile("" : "+v"(v)); }
DI void pin(unsigned& v) { asm volatile("" : "+v"(v)); }
DI s16x4 trread(const char* p) { return __builtin_amdgcn_ds_read_tr16_b64_v4i16((LAS s16x4*)p); }
DI bf16x8 cat8(s16x4 a, s16x4 b) {
  s16x8 r = __builtin_shufflevector(a, b, 0, 1, 2, 3, 4, 5, 6, 7);
  return __builtin_bit_cast(bf16x8, r);
}
DI void keep(bf16x8 v) { asm volatile("" :: "v"(v)); }
DI bf16x8 asb8(u32x4 v) { return __builtin_bit_cast(bf16x8, v); }
DI u32x4 asu4(f32x4 v) { return __builtin_bit_cast(u32x4, v); }
DI f32x4 asf4(u32x4 v) { return __builtin_bit_cast(f32x4, v); }
DI f32x4 mfma32(bf16x8 a, bf16x8 b, f32x4 c) { return __builtin_amdgcn_mfma_f32_16x16x32_bf16(a, b, c, 0, 0, 0); }
DI f32x4 mfma16(u32x2 a, u32x2 b, f32x4 c) {
  return __builtin_amdgcn_mfma_f32_16x16x16bf16_1k(__builtin_bit_cast(s16x4, a), __builtin_bit_cast(s16x4, b), c, 0, 0, 0);
}
template <int CTRL> DI float dppadd(float v) { return v + asf(__builtin_amdgcn_update_dpp(0u, asu(v), CTRL, 0xf, 0xf, true)); }
DI unsigned pk2(float a, float b) {
  f32x2 f = {a, b};
  return __builtin_bit_cast(unsigned, __builtin_convertvector(f, bf16x2));
}
DI u32x2 pk4(f32x4 v) { return u32x2{pk2(v[0], v[1]), pk2(v[2], v[3])}; }
DI float bflo(unsigned u) { return asf(u << 16); }
DI float bfhi(unsigned u) { return asf(u & 0xffff0000u); }
DI float dot2(unsigned a, unsigned b, float c) {
  return __builtin_amdgcn_fdot2_f32_bf16(__builtin_bit_cast(bf16x2, a), __builtin_bit_cast(bf16x2, b), c, false);
}
DI float fmx(float a, float b) { return __builtin_elementwise_maximum(a, b); }
DI float fmn(float a, float b) { return __builtin_elementwise_minimum(a, b); }
template <bool MAX> DI float rop(unsigned a, unsigned b) { return MAX ? fmx(asf(a), asf(b)) : asf(a) + asf(b); }
template <bool MAX> DI void redg2(float& a, float& b) {
  auto r = __builtin_amdgcn_permlane16_swap(asu(a), asu(b), false, false);
  float m = rop<MAX>(r[0], r[1]);
  auto r2 = __builtin_amdgcn_permlane32_swap(asu(m), asu(m), false, false);
  m = rop<MAX>(r2[0], r2[1]);
  auto r3 = __builtin_amdgcn_permlane16_swap(asu(m), asu(m), false, false);
  a = asf(r3[0]); b = asf(r3[1]);
}
template <bool MAX> DI float redg4(const float (&v)[4]) {
  auto r = __builtin_amdgcn_permlane16_swap(asu(v[0]), asu(v[1]), false, false);
  auto q = __builtin_amdgcn_permlane16_swap(asu(v[2]), asu(v[3]), false, false);
  auto t = __builtin_amdgcn_permlane32_swap(asu(rop<MAX>(r[0], r[1])), asu(rop<MAX>(q[0], q[1])), false, false);
  return rop<MAX>(t[0], t[1]);
}
DI void bcast4(float m, float (&v)[4]) {
  auto t = __builtin_amdgcn_permlane32_swap(asu(m), asu(m), false, false);
  unsigned t0 = t[0], t1 = t[1];
  auto r = __builtin_amdgcn_permlane16_swap(t0, t0, false, false);
  auto q = __builtin_amdgcn_permlane16_swap(t1, t1, false, false);
  v[0] = asf(r[0]); v[1] = asf(r[1]); v[2] = asf(q[0]); v[3] = asf(q[1]);
}
constexpr float L2E = 1.4426950408889634f;
DI f32x4 exp4(f32x4 v) {
  return f32x4{__builtin_amdgcn_exp2f(v[0]), __builtin_amdgcn_exp2f(v[1]), __builtin_amdgcn_exp2f(v[2]), __builtin_amdgcn_exp2f(v[3])};
}
DI float sum4(f32x4 v) { f32x2 t = v.xy + v.zw; return t[0] + t[1]; }
DI void hedgehog4(const f32x4 (&p)[4], u32x4 (&o)[4]) {
  float mx[4], mn[4];
#pragma unroll
  for (int i = 0; i < 4; ++i) {
    mx[i] = fmx(fmx(p[i][0], p[i][1]), fmx(p[i][2], p[i][3]));
    mn[i] = -fmn(fmn(p[i][0], p[i][1]), fmn(p[i][2], p[i][3]));
  }
  bcast4(redg4<true>(mx), mx);
  bcast4(redg4<true>(mn), mn);
  f32x4 ep[4], en[4];
  float sp[4], sn[4];
#pragma unroll
  for (int i = 0; i < 4; ++i) {
    ep[i] = exp4(p[i] - mx[i]);
    en[i] = exp4(-p[i] - mn[i]);
    sp[i] = sum4(ep[i]);
    sn[i] = sum4(en[i]);
  }
  bcast4(__builtin_amdgcn_rcpf(redg4<false>(sp)), sp);
  bcast4(__builtin_amdgcn_rcpf(redg4<false>(sn)), sn);
#pragma unroll
  for (int i = 0; i < 4; ++i) {
    const u32x2 a = pk4(ep[i] * sp[i]), b = pk4(en[i] * sn[i]);
    o[i] = u32x4{a[0], a[1], b[0], b[1]};
  }
}
template <int N> DI void gelu16(f32x4 (&acc)[4][3], const float (&k1)[4], const float (&k2)[4], const float (&irs)[4]) {
  f32x4 t[4];
#pragma unroll
  for (int m = 0; m < 4; ++m) {
    const f32x4 a = acc[m][N], K1 = {k1[m], k1[m], k1[m], k1[m]}, K2 = {k2[m], k2[m], k2[m], k2[m]};
    t[m] = a * __builtin_elementwise_fma(a * a, K2, K1);
  }
#pragma unroll
  for (int m = 0; m < 4; ++m) t[m] = exp4(t[m]);
#pragma unroll
  for (int m = 0; m < 4; ++m) { const f32x4 I = {irs[m], irs[m], irs[m], irs[m]}; t[m] = __builtin_elementwise_fma(t[m], I, I); }
#pragma unroll
  for (int m = 0; m < 4; ++m)
    acc[m][N] *= f32x4{__builtin_amdgcn_rcpf(t[m][0]), __builtin_amdgcn_rcpf(t[m][1]), __builtin_amdgcn_rcpf(t[m][2]), __builtin_amdgcn_rcpf(t[m][3])};
  asm volatile("" : "+v"(acc[0][N]), "+v"(acc[1][N]), "+v"(acc[2][N]), "+v"(acc[3][N]));
}
constexpr int DROP = 0x7f000000;
#define STAMP(i)
#define WAITV(n) asm volatile("s_waitcnt vmcnt(" #n ")" ::: "memory")
#define WAITL0() asm volatile("s_waitcnt lgkmcnt(0)" ::: "memory")
#define SB() __builtin_amdgcn_sched_barrier(0)
#define BAR() do { SB(); __builtin_amdgcn_s_barrier(); SB(); } while (0)
#define LBAR() do { WAITL0(); BAR(); } while (0)
#define PSLEEP 4
constexpr int SPIN_CAP = 1 << 18;
constexpr unsigned TAGX = 0x5bd1e995u;
constexpr unsigned EMPTY = 0xffffffffu;

constexpr int L_SLOT = 40960;
constexpr int L_CB = 32768;
constexpr int L_U = 98304;
constexpr int L_ZS = L_U + 6144;
constexpr int L_RING = 24576;
constexpr int LDS_BYTES = 163840;
constexpr int CODE_LINES = 200;
constexpr int p1X(int b) { return b == 9 ? 3 : 0; }
constexpr int p1cnt(int i) {
  int n = i + 3;
  for (int b = i < 1 ? 0 : (5 * i - 1) / 3; b < 2 * i; ++b) n += p1X(b);
  return n;
}
constexpr int p2P(int i) { return i + 4 < 16 ? 3 : 0; }
constexpr int p2E(int i) { return i < 0 ? 0 : (i == 2 ? 2 : 0) + (i == 7 ? 1 : 0) + (i == 11 ? 2 : 0) + (i == 12 ? 7 : 0) + (i == 13 ? 5 : 0); }
constexpr int p2Z(int i) { return i >= 0 && i < 8 ? 1 : 0; }
constexpr int p2cnt(int st, bool q) {
  return p2E(st - 3) + p2P(st - 2) + p2E(st - 2) + p2P(st - 1) + p2E(st - 1) + (q ? 0 : p2Z(st - 3) + p2Z(st - 2) + p2Z(st - 1));
}

__global__ __launch_bounds__(512, 2) void fused(const char* __restrict__ x, const char* __restrict__ wqkv,
                                                const char* __restrict__ wphi, const char* __restrict__ wo,
                                                const char* __restrict__ win, const char* __restrict__ wsp,
                                                const char* __restrict__ bsp, const char* __restrict__ wout,
                                                char* out, char* ws) {
  extern __shared__ __attribute__((aligned(16))) char smem[];
  const int tid = threadIdx.x, lane = tid & 63;
  const int w = __builtin_amdgcn_readfirstlane(tid >> 6);
  const int r16 = lane & 15, g = lane >> 4;
  const int tq = r16 >> 2, tp = r16 & 3;
  const int bid = blockIdx.x, xcd = bid & 7, jj = bid >> 3;
  const int rt16 = (xcd >> 2) * 8 + (jj >> 2);
  const bool isqkv = (jj & 2) == 0;
  const int grp = (xcd & 3) * 2 + (jj & 1);
  const int cg = isqkv ? grp : 8 + grp;
  const int zft0 = grp < 7 ? grp * 6 : 40;
  const int znft = grp < 6 ? 6 : (grp == 6 ? 4 : 3);
  const int wm = w >> 2, wn = (w & 3) ^ wm;
  const int rot1 = (rt16 & 3) * 2;
  const int rt64 = xcd * 4 + (jj >> 3);
  const int cgp = jj & 7;
  const int ft0 = cgp < 7 ? cgp * 6 : 40;
  const int npair = cgp < 6 ? 3 : 2;
  const int th = w & 1, kq = w >> 1;
#define ROT2 cgp
  const int rot2 = ROT2;

  int xsrc[2], wsrc[3];
#pragma unroll
  for (int u = 0; u < 2; ++u) {
    int row = 16 * w + 8 * u + (lane >> 3);
    xsrc[u] = (rt16 * 128 + row) * 1024 + (((lane & 7) ^ ((row >> 1) & 7)) << 4);
  }
  const int wstride = isqkv ? 64 * 3072 : 64 * 2752;
#pragma unroll
  for (int u = 0; u < 3; ++u) {
    int ci = u * 64 + lane;
    int rl = (ci * 2731) >> 16, c = ci - rl * 24;
    int h2 = ((rl >> 1) & 1) | ((w & 1) << 1);
    int cs = c ^ (h2 << 1);
    int vq = (8 * w + rl) * 3072 + (cs >> 3) * 1024 + grp * 128 + (cs & 7) * 16;
    int cz = min(cs < 12 ? cs : cs - 12, znft * 2 - 1);
    int vz = (8 * w + rl) * 2752 + (cs < 12 ? 1376 : 0) + zft0 * 32 + cz * 16;
    wsrc[u] = isqkv ? vq : vz;
  }
  asm volatile("" : "+v"(xsrc[0]), "+v"(xsrc[1]), "+v"(wsrc[0]), "+v"(wsrc[1]), "+v"(wsrc[2]));
  asm volatile("" : "+s"(x), "+s"(wqkv), "+s"(wphi), "+s"(wo), "+s"(win), "+s"(wsp), "+s"(bsp), "+s"(wout), "+s"(out), "+s"(ws));
  const __amdgpu_buffer_rsrc_t wsr = __builtin_amdgcn_make_buffer_rsrc(ws, 0, WS_END, 0x00020000);
  const u32x4 wsd = mkdesc(ws, WS_END);
  const u32x4 xd = mkdesc(x, 2048u * 1024u);
  const u32x4 wd = isqkv ? mkdesc(wqkv, 512u * 3072u) : mkdesc(win, 512u * 2752u);
  const u32x4 ad = mkdesc(wsp, 2048u * 4096u);
  const f32x4 zero4 = {0.f, 0.f, 0.f, 0.f};
  const unsigned ONES = 0x3f803f80u;
  const bf16x8 ones8 = asb8(u32x4{ONES, ONES, ONES, ONES});
  STAMP(0);
  auto dma1 = [&](int st, int u) {
    const int slot = (st & 3) * L_SLOT, sr = (st + rot1) & 7;
    if (u < 2) dma16(xd, xsrc[u], sr * 128, slot + (16 * w + 8 * u) * 128);
    else dma16(wd, wsrc[u - 2], sr * wstride, slot + 16384 + (3 * w + u - 2) * 1024);
  };
#pragma unroll
  for (int u = 0; u < 5; ++u) dma1(0, u);
  unsigned ep;
  u32x4 xrow;
  asm volatile("buffer_load_dword %0, off, %2, 0 sc1\n\tbuffer_load_dwordx4 %1, %3, %4, 0 offen"
               : "=&v"(ep), "=&v"(xrow) : "s"(wsd), "v"((bid * 8 + w) * 1024 + lane * 16), "s"(xd) : "memory");
#pragma unroll
  for (int u = 0; u < 5; ++u) dma1(1, u);

  const int asrc = (rt64 * 64 + th * 32 + (lane >> 3)) * 4096 + kq * 128 + (((lane & 7) ^ ((lane >> 4) & 3)) << 4);
  auto dmaA = [&](int c, int u) {
    dma16(ad, asrc ^ ((u & 1) << 6), ((c + rot2) & 7) * 512 + u * 8 * 4096, (c % 3) * L_CB + w * 4096 + u * 1024);
  };
  auto piece = [&](int p) {
    if (p < 30) dma1(2 + p / 5, p % 5);
    else if (p < 42) dmaA((p - 30) >> 2, (p - 30) & 3);
  };
  const bool zeven = (wn & 1) == 0;
  const int hb = 3 * (wn >> 1);
  int li[3];
  li[0] = zeven ? hb : hb + 2; li[1] = hb + 1; li[2] = zeven ? hb : hb + 2;
  int aoff[2], boff[3], sgi[3];
  sgi[0] = isqkv ? wn : li[0];
  sgi[1] = isqkv ? 4 + wn : (zeven ? li[1] : 6 + li[1]);
  sgi[2] = isqkv ? 8 + wn : 6 + li[2];
#pragma unroll
  for (int ks = 0; ks < 2; ++ks) aoff[ks] = (wm * 64 + r16) * 128 + (((4 * ks + g) ^ ((r16 >> 1) & 7)) << 4);
  {
    int h2 = ((tq >> 1) & 1) | ((g & 1) << 1);
#pragma unroll
    for (int i = 0; i < 3; ++i) boff[i] = 16384 + (8 * g + tq) * 384 + (((2 * sgi[i] + (tp >> 1)) ^ (h2 << 1)) << 4) + (tp & 1) * 8;
  }
  f32x4 acc[4][3], csum[3];
#pragma unroll
  for (int i = 0; i < 3; ++i) { acc[0][i] = zero4; acc[1][i] = zero4; acc[2][i] = zero4; acc[3][i] = zero4; csum[i] = zero4; }
  bf16x8 fa[2][4], fb[2][3];
  u32x4 rst, cst;
  u32x2 wrow;
  unsigned tag = 0, pf;
  float st_s = 0.f, st_q = 0.f;
  float rs[4], ka[4], kb[4], mh[4], ml[4];
  const bool csprod = rt16 < 4 && wm == 0;
  auto ld_stats = [&]() {
    ld128d(rst, wsd, lane * 16, WS_RST + (rt16 * 128 + wm * 64) * 16);
    ld128d(cst, wsd, lane * 16, WS_CS + (cg * 4 + wn) * 1024);
  };
  auto rdA = [&](int st, int ks, int m) { fa[ks][m] = *(const bf16x8*)(smem + (st & 3) * L_SLOT + aoff[ks] + m * 2048); };
  auto rdB = [&](int st, int ks, int n) {
    const char* p = smem + (st & 3) * L_SLOT + boff[n] + ks * 12288;
    fb[ks][n] = cat8(trread(p), trread(p + 1536));
  };
  waitv<7>();
  BAR();
  STAMP(1);
  rdB(0, 0, 0); rdA(0, 0, 0); rdA(0, 0, 1); rdA(0, 0, 2); rdA(0, 0, 3); rdB(0, 0, 1); rdB(0, 0, 2);
  sfor<16>([&](auto B) {
    constexpr int b = B, i = b >> 1, ks = b & 1;
    constexpr int nst = (b + 1) >> 1, nks = ks ^ 1;
    constexpr int p0 = 3 * b, p1 = 3 * b + 3 < 34 ? 3 * b + 3 : 34;
    sfor<6>([&](auto N) {
      constexpr int p = N, n = p >> 1, m = (p & 1) * 2;
      SB();
      acc[m][n] = mfma32(fb[ks][n], fa[ks][m], acc[m][n]);
      acc[m + 1][n] = mfma32(fb[ks][n], fa[ks][m + 1], acc[m + 1][n]);
      SB();
      if constexpr (nst < 8) {
        if constexpr (p < 3) rdB(nst, nks, p);
        if constexpr (p < 4) rdA(nst, nks, p);
      }
      if constexpr (p >= 1 && p0 + p - 1 < p1) piece(p0 + p - 1);
      if constexpr (b == 13) {
        if constexpr (p == 0) {
          pin(rst); pin(cst); pin(wrow);
#pragma nounroll
          for (int spin = 0; spin < SPIN_CAP; ++spin) {
            if (__builtin_expect(__all(rst[1] == tag && rst[3] == tag && (cst[0] ^ cst[1] ^ cst[2] ^ cst[3]) == tag), 1)) break;
            __builtin_amdgcn_s_sleep(PSLEEP);
            ld_stats();
            WAITV(0);
            pin(rst); pin(cst);
          }
        }
        if constexpr (p == 1) bcast4(asf(pk2(-asf(rst[2]), -asf(rst[2]))), mh);
        if constexpr (p == 2) bcast4(asf(pk2(-asf(rst[2]) - bflo(pk2(-asf(rst[2]), 0.f)), 0.f)), ml);
        if constexpr (p == 3) bcast4(isqkv ? asf(rst[0]) : __builtin_amdgcn_rcpf(asf(rst[0])), rs);
        if constexpr (p == 4) bcast4(asf(rst[0]) * (isqkv ? L2E : -2.0f * 0.7978845608028654f * L2E), ka);
        if constexpr (p == 5) bcast4(asf(rst[0]) * asf(rst[0]) * asf(rst[0]) * (-2.0f * 0.7978845608028654f * L2E * 0.044715f), kb);
      }
      if constexpr (b == 14) {
        constexpr int q = (p + 3) % 6, fn = q >> 1, fm = (q & 1) * 2;
        acc[fm][fn] = mfma16(u32x2{cst[fn], cst[fn]}, u32x2{asu(mh[fm]), asu(ml[fm])}, acc[fm][fn]);
        acc[fm + 1][fn] = mfma16(u32x2{cst[fn], cst[fn]}, u32x2{asu(mh[fm + 1]), asu(ml[fm + 1])}, acc[fm + 1][fn]);
      }
      if constexpr (b == 1) {
        if constexpr (p == 0) {
          pin(ep); pin(xrow);
          tag = __builtin_amdgcn_readfirstlane(ep) + 1u;
#pragma unroll
          for (int e = 0; e < 4; ++e) { st_s = dot2(xrow[e], ONES, st_s); st_q = dot2(xrow[e], xrow[e], st_q); }
        }
        if constexpr (p == 1) redg2<false>(st_s, st_q);
        if constexpr (p == 2) { st_s = dppadd<0x128>(st_s); st_q = dppadd<0x128>(st_q); st_s = dppadd<0x124>(st_s); st_q = dppadd<0x124>(st_q); }
        if constexpr (p == 3) { st_s = dppadd<0x122>(st_s); st_q = dppadd<0x122>(st_q); st_s = dppadd<0x121>(st_s); st_q = dppadd<0x121>(st_q); }
        if constexpr (p == 4) {
          float mean = st_s * (1.0f / 512.0f);
          float var = fmx(st_q * (1.0f / 512.0f) - mean * mean, 0.f);
          float rs = __builtin_amdgcn_rsqf(var + 1e-5f);
          if (lane == 0)
            st128d(u32x4{asu(rs), tag, asu(mean), tag}, wsd, WS_RST + (bid * 8 + w) * 16, 0);
        }
      }
    });
    if constexpr (b < 4) {
      if (csprod) {
#pragma unroll
        for (int n = 0; n < 3; ++n) csum[n] = mfma32(ones8, fb[ks][n], csum[n]);
        if constexpr (b == 3) {
          u32x4 o;
#pragma unroll
          for (int n = 0; n < 3; ++n) o[n] = pk2(csum[n][0], csum[n][0] - bflo(pk2(csum[n][0], 0.f)));
          o[3] = tag ^ o[0] ^ o[1] ^ o[2];
          if (g == 0) st128d(o, wsd, r16 * 16, WS_CS + (cg * 4 + wn) * 1024 + rt16 * 256);
        }
      }
    }
    if constexpr (ks == 0 && i < 7) {
      WAITL0();
      waitv<(i < 6 ? p1cnt(i) : 5)>();
      BAR();
    }
    if constexpr (b == 11) {
      const int l8 = lane & 7, li = bid * 40 + w * 8 + l8;
      const char* p = w < 5 ? (li < 4096 ? wo + li * 128 : wout + min(li - 4096, 5503) * 128)
                            : (const char*)(__builtin_amdgcn_s_getpc() & ~127ull) + min(jj * 8 + l8, CODE_LINES - 1) * 128;
      gld32(pf, p);
    }
    if constexpr (b == 12) {
#pragma unroll
      for (int e = 0; e < 2; ++e)
        st128d(u32x4{EMPTY, EMPTY, EMPTY, EMPTY}, wsd, (e == 0 || kq + 4 < 2 * npair) ? lane * 16 : DROP, WS_A3F + ((rt64 * 2 + th) * 76 + 32 + ft0 + kq + 4 * e) * 1024);
    }
    if constexpr (b == 9) {
      ld_stats();
      ld64p(wrow, mkdesc(wphi, 32u * 512u), r16 * 32 + g * 8, (grp * 4 + wn) * 512);
    }
  });
  STAMP(2);
  bf16x8 af[8][2][2];
  const int aro = w * 4096 + r16 * 128;
  auto wsp_ld = [&](int c) {
#pragma unroll
    for (int e = 0; e < 2; ++e)
#pragma unroll
      for (int t = 0; t < 2; ++t)
        af[c][e][t] = *(const bf16x8*)(smem + (c % 3) * L_CB + aro + t * 2048 + (((4 * e + g) ^ ((r16 >> 1) & 7)) << 4));
    SB();
  };
  auto wsp_rd = [&](int c) {
    SB();
    if (c == 1) pin(pf);
    if (c < 6) { WAITV(8); } else if (c == 6) { WAITV(4); } else { WAITV(0); }
    wsp_ld(c);
  };
  auto wsp_is = [&](int c, int u0, int u1) {
    SB();
    if (u0 == 0) WAITL0();
    for (int u = u0; u < u1; ++u) dmaA(c, u);
    SB();
  };
  char* scr = smem + L_U + w * 8192;
  u32x4 pq[4];
  STAMP(3);
  if (isqkv) {
    f32x4 lg[4];
    u32x4 pk[4];
    const unsigned one = (r16 & 1) ? 0x3f800000u : 0x3f80u;
    const u32x2 wph = pk4(mfma16(wrow, u32x2{(tq == g && tp < 2) ? one : 0u, (tq == g && tp >= 2) ? one : 0u}, zero4));
#pragma unroll
    for (int m = 0; m < 4; ++m) lg[m] = mfma16(wph, pk4(acc[m][1] * ka[m]), zero4);
    wsp_is(1, 0, 4);
    wsp_is(2, 0, 4);
    hedgehog4(lg, pk);
    pin(pk[0]); pin(pk[1]); pin(pk[2]); pin(pk[3]);
    wsp_rd(0);
    wsp_is(3, 0, 4);
    wsp_rd(1);
    f32x4 kv[2] = {zero4, zero4}, kz[2] = {zero4, zero4};
    char* sc3 = smem + L_U + w * 3072;
#pragma unroll
    for (int mp = 0; mp < 2; ++mp) {
#pragma unroll
      for (int m = 0; m < 2; ++m) {
        *(u32x4*)(sc3 + (16 * m + r16) * 64 + g * 16) = pk[2 * mp + m];
        *(u32x2*)(sc3 + 2048 + (16 * m + r16) * 32 + g * 8) = pk4(acc[2 * mp + m][2] * rs[2 * mp + m]);
      }
      const int row = 8 * g + tq;
      const char* pp = sc3 + row * 64 + tp * 16;
      const char* pv = sc3 + 2048 + row * 32 + tp * 8;
      bf16x8 ap = cat8(trread(pp), trread(pp + 256));
      bf16x8 an = cat8(trread(pp + 8), trread(pp + 264));
      bf16x8 bv = cat8(trread(pv), trread(pv + 128));
      kv[0] = mfma32(ap, bv, kv[0]);
      kv[1] = mfma32(an, bv, kv[1]);
      kz[0] = mfma32(ap, ones8, kz[0]);
      kz[1] = mfma32(an, ones8, kz[1]);
    }
#pragma unroll
    for (int m = 0; m < 4; ++m) lg[m] = mfma16(wph, pk4(acc[m][0] * ka[m]), zero4);
    *(f32x4*)(sc3 + lane * 16) = wm ? kv[0] : kv[1];
    if (r16 == 0) *(f32x4*)(sc3 + 1024 + g * 16) = wm ? kz[0] : kz[1];
    wsp_is(4, 0, 4);
    wsp_rd(2);
    LBAR();
    {
      const char* p = smem + L_U + (w ^ 5) * 3072;
      const f32x4 a = (wm ? kv[1] : kv[0]) + *(const f32x4*)(p + lane * 16);
      const f32x4 b = (wm ? kz[1] : kz[0]) + *(const f32x4*)(p + 1024 + g * 16);
      const int base = WS_KVP + (grp * 16 + rt16) * KVP_SZ;
      const int hs = wn * 2 + wm;
      st128d(u32x4{asu(a[0]), tag, asu(a[1]), tag}, wsd, lane * 16, base + hs * 2048);
      st128d(u32x4{asu(a[2]), tag, asu(a[3]), tag}, wsd, lane * 16, base + hs * 2048 + 1024);
      const float v0 = r16 ? b[2] : b[0], v1 = r16 ? b[3] : b[1];
      st128d(u32x4{asu(v0), tag, asu(v1), tag}, wsd, r16 < 2 ? g * 32 + r16 * 16 : DROP, base + 16384 + hs * 128);
    }
    STAMP(4);
    wsp_is(5, 0, 4);
    wsp_rd(3);
    hedgehog4(lg, pq);
    pin(pq[0]); pin(pq[1]); pin(pq[2]); pin(pq[3]);
    wsp_is(6, 0, 4);
    wsp_rd(4);
    wsp_is(7, 0, 4);
    STAMP(5);
#pragma nounroll
    for (int rep = 0; rep < ((w == 0 && rt16 < 8) ? 2 : 1); ++rep) {
      const int sub = rep ? 128 + rt16 : rt16 * 8 + w;
      const int off = WS_KVP + (grp * 16 + (lane >> 3)) * KVP_SZ + (sub * 8 + (lane & 7)) * 16;
      u32x4 a, b;
#pragma nounroll
      for (int spin = 0; spin < SPIN_CAP; ++spin) {
        ld128d(a, wsd, off, 0); ld128d(b, wsd, off, 8 * KVP_SZ);
        WAITV(0);
        pin(a); pin(b);
        if (__all(a[1] == tag && a[3] == tag && b[1] == tag && b[3] == tag)) break;
        __builtin_amdgcn_s_sleep(8);
      }
      float v0 = asf(a[0]) + asf(b[0]), v1 = asf(a[2]) + asf(b[2]);
      v0 = dppadd<0x128>(v0); v1 = dppadd<0x128>(v1);
      redg2<false>(v0, v1);
      const int dsto = rep ? (sub * 8 + lane) * 8 : ((sub >> 4) * 64 + (sub & 7) * 8 + lane) * 16 + ((sub >> 3) & 1) * 8;
      __builtin_amdgcn_raw_buffer_store_b64(u32x2{pk2(v0, v1), tag}, wsr, lane < 8 ? dsto : DROP, WS_KVR + grp * 1088 * 8, 16);
    }
    STAMP(6);
  } else {
    const bool v0 = li[0] < znft, v1 = li[1] < znft;
    gelu16<0>(acc, ka, kb, rs);
    STAMP(22);
    wsp_is(1, 0, 4);
    STAMP(23);
    {
      f32x4 s4[4], q4[4];
#pragma unroll
      for (int m = 0; m < 4; ++m) { s4[m] = v0 ? acc[m][0] : zero4; q4[m] = s4[m] * s4[m]; }
      if (zeven) {
        gelu16<1>(acc, ka, kb, rs);
#pragma unroll
        for (int m = 0; m < 4; ++m) {
          const f32x4 y = v1 ? acc[m][1] : zero4;
          s4[m] += y; q4[m] = __builtin_elementwise_fma(y, y, q4[m]);
        }
      }
      float s[4], q[4];
#pragma unroll
      for (int m = 0; m < 4; ++m) { s[m] = sum4(s4[m]); q[m] = sum4(q4[m]); }
      *(f32x2*)(smem + L_ZS + (wn >> 1) * 8192 + (wn & 1) * 1024 + (wm * 64 + lane) * 8) = f32x2{redg4<false>(s), redg4<false>(q)};
    }
    STAMP(20);
    if (w >= 2) wsp_is(2, 0, 4);
    STAMP(21);
    LBAR();
    if (tid < 128) {
      f32x2 a = *(const f32x2*)(smem + L_ZS + tid * 8) + *(const f32x2*)(smem + L_ZS + 1024 + tid * 8);
      f32x2 b = *(const f32x2*)(smem + L_ZS + 8192 + tid * 8) + *(const f32x2*)(smem + L_ZS + 9216 + tid * 8);
      st128d(u32x4{asu(a[0] + b[0]), tag, asu(a[1] + b[1]), tag}, wsd,
                                             WS_ZST + ((rt16 * 8 + grp) * 128 + tid) * 16, 0);
    }
    STAMP(4);
    if (w < 2) wsp_is(2, 0, 4);
    gelu16<2>(acc, ka, kb, rs);
    u32x4 zs[2];
    const int zso = WS_ZST + (rt16 * 8 + w) * 2048;
    auto ld_zs = [&]() { ld128d(zs[0], wsd, lane * 16, zso); ld128d(zs[1], wsd, lane * 16, zso + 1024); };
    ld_zs();
    SB(); WAITV(10); wsp_ld(0);
    wsp_is(3, 0, 4);
    STAMP(5);
    WAITV(4);
    pin(pf);
    wsp_ld(1); wsp_ld(2);
#pragma nounroll
    for (int spin = 0; spin < SPIN_CAP; ++spin) {
      pin(zs[0]); pin(zs[1]);
      if (__all(zs[0][1] == tag && zs[0][3] == tag && zs[1][1] == tag && zs[1][3] == tag)) break;
      __builtin_amdgcn_s_sleep(2);
      ld_zs();
      WAITV(0);
    }
    *(f32x2*)(scr + 4096 + lane * 8) = f32x2{asf(zs[0][0]), asf(zs[0][2])};
    *(f32x2*)(scr + 4608 + lane * 8) = f32x2{asf(zs[1][0]), asf(zs[1][2])};
    LBAR();
    STAMP(6);
    {
      f32x2 sq = {0.f, 0.f};
#pragma unroll
      for (int j = 0; j < 8; ++j) sq += *(const f32x2*)(smem + L_U + j * 8192 + 4096 + (wm * 64 + lane) * 8);
      const float mean = sq[0] * (1.0f / 688.0f);
      const float var = fmx(sq[1] * (1.0f / 688.0f) - mean * mean, 0.f);
      const float r = __builtin_amdgcn_rsqf(var + 1e-5f);
      float zr[4], zc[4];
      bcast4(r, zr);
      bcast4(-mean * r, zc);
      const int dst = WS_Z2F + (((rt16 >> 1) * 2 * 44 + zft0) * 4 + ((2 * rt16 + wm) & 3)) * 1024;
#pragma unroll
      for (int n = 0; n < 2; ++n) {
#pragma unroll
        for (int m = 0; m < 4; ++m) *(u32x2*)(scr + n * 2048 + (16 * m + r16) * 32 + g * 8) = pk4(acc[m][n] * zr[m] + zc[m]);
        const int dn = dst + li[n] * 4096, vo = (n == 0 ? v0 : (v1 && zeven)) ? lane * 16 : DROP;
#pragma unroll
        for (int kk = 0; kk < 2; ++kk) {
          const char* p = scr + n * 2048 + (32 * kk + 8 * g + tq) * 32 + tp * 8;
          bf16x8 f = cat8(trread(p), trread(p + 128));
          st128d(__builtin_bit_cast(u32x4, f), wsd, vo, dn + kk * 44 * 4096);
        }
      }
    }
    STAMP(17);
    if (!zeven) gelu16<1>(acc, ka, kb, rs);
    STAMP(18);
    WAITV(0);
    BAR();
    if (tid == 0) __builtin_amdgcn_raw_buffer_store_b64(u32x2{tag, tag ^ TAGX}, wsr, WS_FLZ + grp * 1024 + rt16 * 8, 0, 16);
    STAMP(19);
    wsp_is(4, 0, 4);
    wsp_is(5, 0, 4);
    wsp_ld(3);
    wsp_is(6, 0, 4);
    wsp_rd(4);
    wsp_is(7, 0, 4);
#pragma unroll
    for (int mp = 0; mp < 2; ++mp) {
      const u32x2 a = pk4(acc[2 * mp][2]), b = pk4(acc[2 * mp + 1][2]), c = pk4(acc[2 * mp][1]), d = pk4(acc[2 * mp + 1][1]);
      pq[mp] = u32x4{a[0], a[1], b[0], b[1]};
      pq[2 + mp] = u32x4{c[0], c[1], d[0], d[1]};
    }
  }
  STAMP(7);
  unsigned biasv[2];
#pragma unroll
  for (int t = 0; t < 2; ++t) ldu16(biasv[t], mkdesc(bsp, 4096u), r16 * 2, (rt64 * 64 + th * 32 + t * 16) * 2);
  wsp_rd(5); wsp_rd(6); wsp_rd(7);
  pin(biasv[0]); pin(biasv[1]);
  if (!isqkv) {
    const int z1o = WS_Z1F + ((rt16 * 4 + wm * 2) * 44 + zft0) * 1024;
    const int vo2 = li[2] < znft ? lane * 16 : DROP, vo1 = (li[1] < znft && !zeven) ? lane * 16 : DROP;
#pragma unroll
    for (int mp = 0; mp < 2; ++mp) {
      st128d(pq[mp], wsd, vo2, z1o + (mp * 44 + li[2]) * 1024);
      st128d(pq[2 + mp], wsd, vo1, z1o + (mp * 44 + li[1]) * 1024);
    }
  }
  STAMP(8);
  int lz = lane;
  asm volatile("" : "+v"(lz));
  const int l16 = lz * 16;
  auto poll_z = [&]() -> unsigned {
    u32x2 f = poll64(wsd, ((l16 >> 1) + 16 * rot2) & 127, WS_FLZ + cgp * 1024);
    return (unsigned)__ballot(f[0] == tag && f[1] == (tag ^ TAGX)) & 0xffffu;
  };
  const int w3off = (lz >> 3) * 1024 + cgp * 128 + (((lz & 7) ^ ((lz >> 4) << 1)) << 4);
  const u32x4 od = mkdesc(wo, 512u * 1024u);
  if (isqkv) {
    u32x4 kr[4];
    auto ld_kr = [&]() {
#pragma unroll
      for (int j = 0; j < 2; ++j) {
        ld128d(kr[j], wsd, l16, WS_KVR + (grp * 1088 + wn * 256) * 8 + j * 1024);
        ld128d(kr[2 + j], wsd, (l16 >> 4) & 48, WS_KVR + (grp * 1088 + 1024 + wn * 16) * 8 + j * 64);
      }
    };
    BAR();
#pragma unroll
    for (int i = 0; i < 8; ++i) dma16(od, w3off, (i * 8 + w) * 8192, L_U + (i * 8 + w) * 1024);
#pragma nounroll
    for (int spin = 0; spin < SPIN_CAP; ++spin) {
      ld_kr();
      WAITV(0);
      bool ok = true;
#pragma unroll
      for (int j = 0; j < 4; ++j) { pin(kr[j]); ok = ok && kr[j][1] == tag && kr[j][3] == tag; }
      if (__builtin_expect(__all(ok), 1)) break;
      __builtin_amdgcn_s_sleep(8);
    }
    const bf16x8 kvo = asb8(u32x4{kr[0][0], kr[0][2], kr[1][0], kr[1][2]});
    const bf16x8 zo = asb8(u32x4{kr[2][0], kr[2][2], kr[3][0], kr[3][2]});
    u32x2 ov[4];
#pragma unroll
    for (int m = 0; m < 4; ++m) {
      const f32x4 num = mfma32(kvo, asb8(pq[m]), zero4), den = mfma32(zo, asb8(pq[m]), zero4);
      ov[m] = pk4(num * __builtin_amdgcn_rcpf(den[0] + 1e-6f));
    }
#pragma unroll
    for (int mp = 0; mp < 2; ++mp)
      st128d(u32x4{ov[2 * mp][0], ov[2 * mp][1], ov[2 * mp + 1][0], ov[2 * mp + 1][1]}, wsd,
             l16, WS_A3F + ((rt16 * 4 + wm * 2 + mp) * 76 + grp * 4 + wn) * 1024);
  }
  STAMP(9);
  unsigned rdy = 0;
  if (w == 0) {
#pragma nounroll
    for (int spin = 0; spin < SPIN_CAP; ++spin) {
      rdy = poll_z();
      if ((rdy & 15u) == 15u) break;
      __builtin_amdgcn_s_sleep(PSLEEP);
    }
    if (lane == 0) *(unsigned*)(smem + 2 * L_CB) = rdy;
  }
  LBAR();
  rdy = __builtin_amdgcn_readfirstlane(*(const unsigned*)(smem + 2 * L_CB));
  STAMP(10);
  const int bsrc = WS_Z2F + ft0 * 4096 + w * 1024;
  const int u2off = npair == 3 ? 16384 : 0;
  auto dma2 = [&](int st, int u) {
    const int n = 2 * u + (w >> 2), q = w & 3;
    dma16d(wsd, l16, bsrc + ((st + 2 * rot2) & 15) * (44 * 4096) + (u < 2 ? u * 8192 : u2off), (st & 3) * L_RING + (n ^ (u < 2 ? q : (q & 1))) * 4096 + q * 1024);
  };
#pragma unroll
  for (int st = 0; st < 2; ++st)
#pragma unroll
    for (int u = 0; u < 3; ++u) dma2(st, u);
  waitv<3>();
  f32x4 ac2[6][2];
#pragma unroll
  for (int n = 0; n < 6; ++n)
#pragma unroll
    for (int t = 0; t < 2; ++t) { const float b = (n == 0 || (n == 4 && kq < 2)) ? bflo(biasv[t]) : 0.f; ac2[n][t] = f32x4{b, b, b, b}; }
  const u32x4 ud = mkdesc(wout, 688u * 1024u);
  bf16x8 fz[2][6];
  auto rdZ = [&](int st, int n) { fz[st & 1][n] = *(const bf16x8*)(smem + (st & 3) * L_RING + n * 4096 + kq * 1024 + l16); };
  u32x4 ao[4][2];
  u32x4 z1v[2];
  u32x2 ofl;
  const int a3 = WS_A3F + ((rt64 * 2 + th) * 76 + 2 * kq) * 1024;
  auto flo = [&]() {
    return l16 < 512 ? WS_FLO + rt64 * 256 + (l16 >> 1) : (l16 < 640 ? WS_FLU + rt64 * 512 + ((l16 >> 1) & 63) : WS_FLZ + cgp * 1024 + 512 + (rt64 >> 1) * 8);
  };
  BAR();
#pragma unroll
  for (int n = 0; n < 6; ++n) rdZ(0, n);
#pragma unroll
  for (int st = 2; st < 4; ++st)
#pragma unroll
    for (int u = 0; u < 3; ++u) dma2(st, u);
  char* rb = smem + 36864 + th * 18432 + l16;
  auto red_wr = [&](int i) {
    if (i == 4 && kq < 2) return;
    const int blk = i < 4 ? (i ^ kq) * 3 + i - 1 : (i ^ (kq & 1)) * 3 + (i == 5 ? (kq & 2) : 1);
    const u32x2 a = pk4(ac2[i][0]), b = pk4(ac2[i][1]);
    *(u32x4*)(rb + (blk << 10)) = u32x4{a[0], a[1], b[0], b[1]};
  };
  sfor<16>([&](auto ST) {
    constexpr int st = ST;
    if constexpr (st < 15) {
      WAITL0();
      if constexpr (p2cnt(st, true) != p2cnt(st, false)) { if (isqkv) waitv<p2cnt(st, true)>(); else waitv<p2cnt(st, false)>(); }
      else waitv<p2cnt(st, false)>();
      BAR();
    }
    if constexpr ((st & 3) == 0) { STAMP(24 + st / 4); }
    sfor<6>([&](auto N) {
      constexpr int n = N;
      constexpr int ord[6] = {1, 2, 3, 5, 4, 0}, c = st == 15 ? ord[n] : n;
      SB();
      ac2[c][0] = mfma32(fz[st & 1][c], af[st >> 1][st & 1][0], ac2[c][0]);
      ac2[c][1] = mfma32(fz[st & 1][c], af[st >> 1][st & 1][1], ac2[c][1]);
      SB();
      if constexpr (st == 15 && n >= 1) red_wr(ord[n - 1]);
      if constexpr (st < 15 && n >= 1 && n <= 3) { rdZ(st + 1, 2 * n - 2); rdZ(st + 1, 2 * n - 1); }
      if constexpr (st + 4 < 16 && n == 0 && (st & 1) == 0) {
        if (__builtin_expect(((rdy >> (st + 4)) & 3u) != 3u, 0)) {
#pragma nounroll
          for (int spin = 0; spin < SPIN_CAP; ++spin) {
            rdy = poll_z();
            if (((rdy >> (st + 4)) & 3u) == 3u) break;
            __builtin_amdgcn_s_sleep(PSLEEP);
          }
        }
      }
      if constexpr (st + 4 < 16 && n < 3) dma2(st + 4, n);
      if constexpr (n == 3) {
        if constexpr (st < 8) { if (!isqkv) dma16(od, w3off, (st * 8 + w) * 8192, L_U + (st * 8 + w) * 1024); }
        if constexpr (st == 12 || st == 13) {
#pragma unroll
          for (int j = 3 * (st - 12); j < (st == 12 ? 3 : 4); ++j) dma16(ud, w3off, (j * 8 + w) * 8192, (j * 8 + w) * 1024);
        }
        if constexpr (st == 2) {
          const int fo = isqkv ? WS_FLO + ((rt16 * 2 + wm) * 32 + grp * 4 + wn) * 8 : WS_FLZ + grp * 1024 + 512 + rt16 * 8;
          __builtin_amdgcn_raw_buffer_store_b64(u32x2{tag, tag ^ TAGX}, wsr, (l16 == 0 && (isqkv || w == 1)) ? 0 : DROP, fo, 16);
          __builtin_amdgcn_raw_buffer_store_b64(u32x2{tag, tag ^ TAGX}, wsr, (l16 == 0 && w == 2) ? 0 : DROP, WS_FLU + rt64 * 512 + cgp * 8, 16);
        }
        if constexpr (st == 7) ld64ds(ofl, wsd, flo(), 0);
        if constexpr (st == 11) {
          pin(ofl);
#pragma nounroll
          for (int spin = 0; spin < SPIN_CAP; ++spin) {
            if (__builtin_expect(__all(ofl[0] == tag && ofl[1] == (tag ^ TAGX)), 1)) break;
            __builtin_amdgcn_s_sleep(PSLEEP);
            ofl = poll64(wsd, flo(), 0);
          }
#pragma unroll
          for (int e = 0; e < 2; ++e)
            ld128d(z1v[e], wsd, l16, WS_Z1F + ((rt64 * 2 + th) * 44 + ft0 + min(kq + 4 * e, 2 * npair - 1)) * 1024);
        }
        if constexpr (st == 12 || st == 13) {
#pragma unroll
          for (int i = 0; i < 2; ++i)
#pragma unroll
            for (int h = 0; h < 2; ++h) ld128d(ao[2 * (st - 12) + i][h], wsd, l16, a3 + (8 * (2 * (st - 12) + i) + h) * 1024);
        }
      }
    });
  });
  STAMP(11);
  {
    WAITV(0);
    pin(z1v[0]); pin(z1v[1]);
    LBAR();
#pragma unroll
    for (int e = 0; e < 2; ++e) {
      const int n = kq + 4 * e;
      if (e == 0 || n < 2 * npair) {
        u32x4 o, pr[3];
#pragma unroll
        for (int j = 0; j < 3; ++j) pr[j] = *(const u32x4*)(rb + ((n * 3 + j) << 10));
#pragma unroll
        for (int t = 0; t < 2; ++t) {
          f32x4 s = ac2[4 * e][t];
#pragma unroll
          for (int j = 0; j < 3; ++j) s += f32x4{bflo(pr[j][2 * t]), bfhi(pr[j][2 * t]), bflo(pr[j][2 * t + 1]), bfhi(pr[j][2 * t + 1])};
          o[2 * t] = pk2(bflo(z1v[e][2 * t]) * s[0], bfhi(z1v[e][2 * t]) * s[1]);
          o[2 * t + 1] = pk2(bflo(z1v[e][2 * t + 1]) * s[2], bfhi(z1v[e][2 * t + 1]) * s[3]);
        }
        if (ft0 + n >= 43) o = u32x4{0u, 0u, 0u, 0u};
        st128d(o, wsd, l16, WS_A3F + ((rt64 * 2 + th) * 76 + 32 + ft0 + n) * 1024);
      }
    }
  }
  STAMP(12);
  f32x4 ac3[4][2];
#pragma unroll
  for (int n = 0; n < 4; ++n) { ac3[n][0] = zero4; ac3[n][1] = zero4; }
  int ly = l16;
  asm volatile("" : "+v"(ly));
  const int lane3 = ly >> 4, r3 = lane3 & 15, g3 = lane3 >> 4, tid3 = w * 64 + lane3;
  int b3[4];
  {
    const int k = 4 * g3 + (r3 >> 2), tp = r3 & 3;
    const int h2 = ((k >> 1) & 1) | (((k >> 2) & 1) << 1);
#pragma unroll
    for (int n = 0; n < 4; ++n) b3[n] = k * 128 + (((2 * n + (tp >> 1)) ^ (h2 << 1)) << 4) + (tp & 1) * 8 + kq * 4096;
  }
  const int go = (rt64 * 64 + (tid3 >> 3)) * 1024 + cgp * 128 + (tid3 & 7) * 16;
  u32x4 xres;
  LBAR();
  STAMP(13);
  sfor<4>([&](auto I) {
    constexpr int i = I;
    bf16x8 b[4];
#pragma unroll
    for (int n = 0; n < 4; ++n) b[n] = cat8(trread(smem + L_U + b3[n] + i * 16384), trread(smem + L_U + b3[n] + i * 16384 + 2048));
    pin(ao[i][0]); pin(ao[i][1]);
#pragma unroll
    for (int j = 4 + 2 * i; j < 6 + 2 * i; ++j)
      if (j < 11) dma16(ud, w3off, min(j * 8 + w, 85) * 8192, (j * 8 + w) * 1024);
#pragma unroll
    for (int n = 0; n < 4; ++n)
#pragma unroll
      for (int t = 0; t < 2; ++t)
        ac3[n][t] = mfma32(b[n], asb8(u32x4{ao[i][0][2 * t], ao[i][0][2 * t + 1], ao[i][1][2 * t], ao[i][1][2 * t + 1]}), ac3[n][t]);
  });
  asm volatile("buffer_load_dwordx4 %0, %1, %2, 0 offen" : "=v"(xres) : "v"(go), "s"(xd) : "memory");
  if (bid == 0 && w == 7) {
#pragma nounroll
    for (int spin = 0; spin < SPIN_CAP; ++spin) {
      u32x4 f[4];
#pragma unroll
      for (int j = 0; j < 4; ++j) ld128d(f[j], wsd, lane3 * 128, WS_RST + j * 8192);
      WAITV(0);
#pragma unroll
      for (int j = 0; j < 4; ++j) pin(f[j]);
      if (__all(f[0][1] == tag && f[1][1] == tag && f[2][1] == tag && f[3][1] == tag)) break;
    }
    if (lane3 == 0) __builtin_amdgcn_raw_buffer_store_b32(tag, wsr, WS_EP, 0, 16);
  }
  u32x4 au[6][2];
  auto ld_au = [&](int i) {
#pragma unroll
    for (int h = 0; h < 2; ++h) ld128d(au[i][h], wsd, l16, a3 + (32 + h + ((i < 5 || kq < 2) ? 8 * i : 32)) * 1024);
  };
#pragma unroll
  for (int i = 0; i < 6; ++i) ld_au(i);
  waitv<12>();
  LBAR();
  STAMP(14);
  sfor<6>([&](auto I) {
    constexpr int i = I;
    bf16x8 b[4];
    const int io = (i < 5 || kq < 2) ? i * 16384 : 0;
#pragma unroll
    for (int n = 0; n < 4; ++n) b[n] = cat8(trread(smem + b3[n] + io), trread(smem + b3[n] + io + 2048));
    waitv<10 - 2 * i>();
#pragma nounroll
    for (int spin = 0; spin < SPIN_CAP; ++spin) {
      pin(au[i][0]); pin(au[i][1]);
      const u32x4 mx = __builtin_elementwise_max(au[i][0], au[i][1]);
      if (__builtin_expect(__all(max(max(mx[0], mx[1]), max(mx[2], mx[3])) != EMPTY), 1)) break;
      __builtin_amdgcn_s_sleep(PSLEEP);
      ld_au(i);
      WAITV(0);
    }
    if (i < 5 || kq < 2) {
#pragma unroll
      for (int n = 0; n < 4; ++n)
#pragma unroll
        for (int t = 0; t < 2; ++t)
          ac3[n][t] = mfma32(b[n], asb8(u32x4{au[i][0][2 * t], au[i][0][2 * t + 1], au[i][1][2 * t], au[i][1][2 * t + 1]}), ac3[n][t]);
    }
  });
  STAMP(15);
#pragma unroll
  for (int n = 0; n < 4; ++n)
#pragma unroll
    for (int t = 0; t < 2; ++t)
      *(f32x4*)(smem + L_U + kq * 16384 + (th * 32 + t * 16 + r3) * 256 + (((n * 4 + g3) ^ r3) << 4)) = ac3[n][t];
  LBAR();
  {
    pin(xres);
    const int row = tid3 >> 3, c8 = tid3 & 7;
    f32x4 s0 = {bflo(xres[0]), bfhi(xres[0]), bflo(xres[1]), bfhi(xres[1])};
    f32x4 s1 = {bflo(xres[2]), bfhi(xres[2]), bflo(xres[3]), bfhi(xres[3])};
#pragma unroll
    for (int j = 0; j < 4; ++j) {
      s0 += *(const f32x4*)(smem + L_U + j * 16384 + row * 256 + (((2 * c8) ^ (row & 15)) << 4));
      s1 += *(const f32x4*)(smem + L_U + j * 16384 + row * 256 + (((2 * c8 + 1) ^ (row & 15)) << 4));
    }
    const u32x2 lo = pk4(s0), hi = pk4(s1);
    st128d(u32x4{lo[0], lo[1], hi[0], hi[1]}, mkdesc(out, 2048u * 1024u), go, 0);
  }
  STAMP(16);
}

extern "C" void kernel_launch(void* const* d_in, const int* in_sizes, int n_in, void* d_out, int out_size, void* d_ws,
                              size_t ws_size, hipStream_t stream) {
  (void)hipFuncSetAttribute((const void*)fused, hipFuncAttributeMaxDynamicSharedMemorySize, LDS_BYTES);
  fused<<<256, 512, LDS_BYTES, stream>>>((const char*)d_in[0], (const char*)d_in[1], (const char*)d_in[2],
                                         (const char*)d_in[3], (const char*)d_in[5], (const char*)d_in[6],
                                         (const char*)d_in[7], (const char*)d_in[8], (char*)d_out, (char*)d_ws);
}
```

```cpp
#include <hip/hip_runtime.h>
#include <hip/hip_bf16.h>
#include <stdint.h>

typedef __bf16 bf16x8 __attribute__((ext_vector_type(8)));
typedef __bf16 bf16x2 __attribute__((ext_vector_type(2)));
typedef short s16x4 __attribute__((ext_vector_type(4)));
typedef short s16x8 __attribute__((ext_vector_type(8)));
typedef float f32x4 __attribute__((ext_vector_type(4)));
typedef float f32x2 __attribute__((ext_vector_type(2)));
typedef unsigned u32x4 __attribute__((ext_vector_type(4)));
typedef unsigned u32x2 __attribute__((ext_vector_type(2)));
#define LAS __attribute__((address_space(3)))
#define DI __device__ __forceinline__

constexpr int WS_EP  = 0;
constexpr int WS_FLZ = 1024;
constexpr int WS_FLO = WS_FLZ + 8192;
constexpr int WS_FLU = WS_FLO + 8192;
constexpr int WS_CS  = WS_FLU + 16384;
constexpr int WS_RST = WS_CS + 16 * 4 * 1024;
constexpr int WS_ZST = WS_RST + 2048 * 16;
constexpr int KVP_SZ = 1088 * 16;
constexpr int WS_KVP = WS_ZST + 16 * 8 * 128 * 16;
constexpr int WS_KVR = WS_KVP + 8 * 16 * KVP_SZ;
constexpr int WS_Z2F = WS_KVR + 8 * 1088 * 8;
constexpr int WS_Z1F = WS_Z2F + 16 * 44 * 4096;
constexpr int WS_A3F = WS_Z1F + 64 * 44 * 1024;
constexpr int WS_END = WS_A3F + 64 * 76 * 1024;
static_assert(WS_Z2F % 128 == 0 && WS_KVP % 128 == 0 && WS_KVR % 128 == 0, "alignment");

template <int V> struct IC { static constexpr int value = V; constexpr operator int() const { return V; } };
template <int I, int N, class F> DI void sfor_(F&& f) { if constexpr (I < N) { f(IC<I>{}); sfor_<I + 1, N>(f); } }
template <int N, class F> DI void sfor(F&& f) { sfor_<0, N>(f); }
template <int N> DI void waitv() { asm volatile("s_waitcnt vmcnt(%0)" :: "n"(N) : "memory"); }

DI float asf(unsigned u) { return __builtin_bit_cast(float, u); }
DI unsigned asu(float f) { return __builtin_bit_cast(unsigned, f); }
DI u32x4 mkdesc(const void* p, unsigned bytes) {
  unsigned long long a = (unsigned long long)p;
  u32x4 d = {(unsigned)a, (unsigned)(a >> 32) & 0xffffu, bytes, 0x00020000u};
  return d;
}
DI void dma16(u32x4 desc, int voff, int soff, int lds) {
  asm volatile("s_mov_b32 m0, %3\n\ts_nop 0\n\tbuffer_load_dwordx4 %0, %1, %2 offen lds"
               :: "v"(voff), "s"(desc), "s"(soff), "s"(lds) : "memory");
}
DI void dma16d(u32x4 desc, int voff, int soff, int lds) {
  asm volatile("s_mov_b32 m0, %3\n\ts_nop 0\n\tbuffer_load_dwordx4 %0, %1, %2 offen sc1 lds"
               :: "v"(voff), "s"(desc), "s"(soff), "s"(lds) : "memory");
}
DI u32x2 poll64(u32x4 desc, int voff, int soff) {
  u32x2 r;
  asm volatile("buffer_load_dwordx2 %0, %1, %2, %3 offen sc1\n\ts_waitcnt vmcnt(0)" : "=&v"(r) : "v"(voff), "s"(desc), "s"(soff) : "memory");
  return r;
}
DI void ld128d(u32x4& r, u32x4 desc, int voff, int soff) {
  asm volatile("buffer_load_dwordx4 %0, %1, %2, %3 offen sc1" : "=v"(r) : "v"(voff), "s"(desc), "s"(soff) : "memory");
}
DI void ld64d(u32x2& r, u32x4 desc, int voff) {
  asm volatile("buffer_load_dwordx2 %0, %1, %2, 0 offen sc1" : "=v"(r) : "v"(voff), "s"(desc) : "memory");
}
DI void ld64ds(u32x2& r, u32x4 desc, int voff, int soff) {
  asm volatile("buffer_load_dwordx2 %0, %1, %2, %3 offen sc1" : "=v"(r) : "v"(voff), "s"(desc), "s"(soff) : "memory");
}
DI void ld64p(u32x2& r, u32x4 desc, int voff, int soff) {
  asm volatile("buffer_load_dwordx2 %0, %1, %2, %3 offen" : "=v"(r) : "v"(voff), "s"(desc), "s"(soff) : "memory");
}
DI void ldu16(unsigned& r, u32x4 desc, int voff, int soff) {
  asm volatile("buffer_load_ushort %0, %1, %2, %3 offen" : "=v"(r) : "v"(voff), "s"(desc), "s"(soff) : "memory");
}
DI void st128d(u32x4 v, u32x4 desc, int voff, int soff) {
  asm volatile("buffer_store_dwordx4 %0, %1, %2, %3 offen sc1\n\ts_nop 1" :: "v"(v), "v"(voff), "s"(desc), "s"(soff) : "memory");
}
DI void gld32(unsigned& r, const char* p) { asm volatile("global_load_dword %0, %1, off" : "=&v"(r) : "v"(p) : "memory"); }
DI void pin(u32x4& v) { asm volatile("" : "+v"(v)); }
DI void pin(u32x2& v) { asm volatile("" : "+v"(v)); }
DI void pin(unsigned& v) { asm volatile("" : "+v"(v)); }
DI s16x4 trread(const char* p) { return __builtin_amdgcn_ds_read_tr16_b64_v4i16((LAS s16x4*)p); }
DI bf16x8 cat8(s16x4 a, s16x4 b) {
  s16x8 r = __builtin_shufflevector(a, b, 0, 1, 2, 3, 4, 5, 6, 7);
  return __builtin_bit_cast(bf16x8, r);
}
DI void keep(bf16x8 v) { asm volatile("" :: "v"(v)); }
DI bf16x8 asb8(u32x4 v) { return __builtin_bit_cast(bf16x8, v); }
DI u32x4 asu4(f32x4 v) { return __builtin_bit_cast(u32x4, v); }
DI f32x4 asf4(u32x4 v) { return __builtin_bit_cast(f32x4, v); }
DI f32x4 mfma32(bf16x8 a, bf16x8 b, f32x4 c) { return __builtin_amdgcn_mfma_f32_16x16x32_bf16(a, b, c, 0, 0, 0); }
DI f32x4 mfma16(u32x2 a, u32x2 b, f32x4 c) {
  return __builtin_amdgcn_mfma_f32_16x16x16bf16_1k(__builtin_bit_cast(s16x4, a), __builtin_bit_cast(s16x4, b), c, 0, 0, 0);
}
template <int CTRL> DI float dppadd(float v) { return v + asf(__builtin_amdgcn_update_dpp(0u, asu(v), CTRL, 0xf, 0xf, true)); }
DI unsigned pk2(float a, float b) {
  f32x2 f = {a, b};
  return __builtin_bit_cast(unsigned, __builtin_convertvector(f, bf16x2));
}
DI u32x2 pk4(f32x4 v) { return u32x2{pk2(v[0], v[1]), pk2(v[2], v[3])}; }
DI float bflo(unsigned u) { return asf(u << 16); }
DI float bfhi(unsigned u) { return asf(u & 0xffff0000u); }
DI float dot2(unsigned a, unsigned b, float c) {
  return __builtin_amdgcn_fdot2_f32_bf16(__builtin_bit_cast(bf16x2, a), __builtin_bit_cast(bf16x2, b), c, false);
}
DI float fmx(float a, float b) { return __builtin_elementwise_maximum(a, b); }
DI float fmn(float a, float b) { return __builtin_elementwise_minimum(a, b); }
template <bool MAX> DI float rop(unsigned a, unsigned b) { return MAX ? fmx(asf(a), asf(b)) : asf(a) + asf(b); }
template <bool MAX> DI void redg2(float& a, float& b) {
  auto r = __builtin_amdgcn_permlane16_swap(asu(a), asu(b), false, false);
  float m = rop<MAX>(r[0], r[1]);
  auto r2 = __builtin_amdgcn_permlane32_swap(asu(m), asu(m), false, false);
  m = rop<MAX>(r2[0], r2[1]);
  auto r3 = __builtin_amdgcn_permlane16_swap(asu(m), asu(m), false, false);
  a = asf(r3[0]); b = asf(r3[1]);
}
template <bool MAX> DI float redg4(const float (&v)[4]) {
  auto r = __builtin_amdgcn_permlane16_swap(asu(v[0]), asu(v[1]), false, false);
  auto q = __builtin_amdgcn_permlane16_swap(asu(v[2]), asu(v[3]), false, false);
  auto t = __builtin_amdgcn_permlane32_swap(asu(rop<MAX>(r[0], r[1])), asu(rop<MAX>(q[0], q[1])), false, false);
  return rop<MAX>(t[0], t[1]);
}
DI void bcast4(float m, float (&v)[4]) {
  auto t = __builtin_amdgcn_permlane32_swap(asu(m), asu(m), false, false);
  unsigned t0 = t[0], t1 = t[1];
  auto r = __builtin_amdgcn_permlane16_swap(t0, t0, false, false);
  auto q = __builtin_amdgcn_permlane16_swap(t1, t1, false, false);
  v[0] = asf(r[0]); v[1] = asf(r[1]); v[2] = asf(q[0]); v[3] = asf(q[1]);
}
constexpr float L2E = 1.4426950408889634f;
DI f32x4 exp4(f32x4 v) {
  return f32x4{__builtin_amdgcn_exp2f(v[0]), __builtin_amdgcn_exp2f(v[1]), __builtin_amdgcn_exp2f(v[2]), __builtin_amdgcn_exp2f(v[3])};
}
DI float sum4(f32x4 v) { f32x2 t = v.xy + v.zw; return t[0] + t[1]; }
template <class F> DI void hedgehog4(const f32x4 (&p)[4], u32x4 (&o)[4], F&& bg) {
  float mx[4], mn[4];
#pragma unroll
  for (int i = 0; i < 4; ++i) {
    mx[i] = fmx(fmx(p[i][0], p[i][1]), fmx(p[i][2], p[i][3]));
    mn[i] = -fmn(fmn(p[i][0], p[i][1]), fmn(p[i][2], p[i][3]));
  }
  bg(IC<0>{});
  bcast4(redg4<true>(mx), mx);
  bcast4(redg4<true>(mn), mn);
  asm volatile("" : "+v"(mx[0]), "+v"(mx[1]), "+v"(mx[2]), "+v"(mx[3]), "+v"(mn[0]), "+v"(mn[1]), "+v"(mn[2]), "+v"(mn[3]));
  bg(IC<1>{});
  f32x4 ep[4], en[4];
  float sp[4], sn[4];
  sfor<4>([&](auto I) {
    constexpr int i = I;
    ep[i] = exp4(p[i] - mx[i]);
    en[i] = exp4(-p[i] - mn[i]);
    sp[i] = sum4(ep[i]);
    sn[i] = sum4(en[i]);
    asm volatile("" : "+v"(sp[i]), "+v"(sn[i]));
    bg(IC<2 + i>{});
  });
  bcast4(__builtin_amdgcn_rcpf(redg4<false>(sp)), sp);
  bcast4(__builtin_amdgcn_rcpf(redg4<false>(sn)), sn);
  asm volatile("" : "+v"(sp[0]), "+v"(sp[1]), "+v"(sp[2]), "+v"(sp[3]), "+v"(sn[0]), "+v"(sn[1]), "+v"(sn[2]), "+v"(sn[3]));
  bg(IC<6>{});
#pragma unroll
  for (int i = 0; i < 4; ++i) {
    const u32x2 a = pk4(ep[i] * sp[i]), b = pk4(en[i] * sn[i]);
    o[i] = u32x4{a[0], a[1], b[0], b[1]};
    pin(o[i]);
  }
  bg(IC<7>{});
}
template <int N> DI void gelu16(f32x4 (&acc)[4][3], const float (&k1)[4], const float (&k2)[4], const float (&irs)[4]) {
  f32x4 t[4];
#pragma unroll
  for (int m = 0; m < 4; ++m) {
    const f32x4 a = acc[m][N], K1 = {k1[m], k1[m], k1[m], k1[m]}, K2 = {k2[m], k2[m], k2[m], k2[m]};
    t[m] = a * __builtin_elementwise_fma(a * a, K2, K1);
  }
#pragma unroll
  for (int m = 0; m < 4; ++m) t[m] = exp4(t[m]);
#pragma unroll
  for (int m = 0; m < 4; ++m) { const f32x4 I = {irs[m], irs[m], irs[m], irs[m]}; t[m] = __builtin_elementwise_fma(t[m], I, I); }
#pragma unroll
  for (int m = 0; m < 4; ++m)
    acc[m][N] *= f32x4{__builtin_amdgcn_rcpf(t[m][0]), __builtin_amdgcn_rcpf(t[m][1]), __builtin_amdgcn_rcpf(t[m][2]), __builtin_amdgcn_rcpf(t[m][3])};
  asm volatile("" : "+v"(acc[0][N]), "+v"(acc[1][N]), "+v"(acc[2][N]), "+v"(acc[3][N]));
}
constexpr int DROP = 0x7f000000;
#define STAMP(i)
#define WAITV(n) asm volatile("s_waitcnt vmcnt(" #n ")" ::: "memory")
#define WAITL0() asm volatile("s_waitcnt lgkmcnt(0)" ::: "memory")
#define SB() __builtin_amdgcn_sched_barrier(0)
#define BAR() do { SB(); __builtin_amdgcn_s_barrier(); SB(); } while (0)
#define LBAR() do { WAITL0(); BAR(); } while (0)
#define PSLEEP 4
#define ZVAR 0
#define QVAR 0
constexpr int SPIN_CAP = 1 << 18;
constexpr unsigned TAGX = 0x5bd1e995u;
constexpr unsigned EMPTY = 0xffffffffu;

constexpr int L_SLOT = 40960;
constexpr int L_CB = 32768;
constexpr int L_U = 98304;
constexpr int L_ZS = L_U + 6144;
constexpr int L_RING = 24576;
constexpr int LDS_BYTES = 163840;
constexpr int CODE_LINES = 200;
constexpr int p1X(int b) { return b == 9 ? 3 : 0; }
constexpr int p1cnt(int i) {
  int n = i + 3;
  for (int b = i < 1 ? 0 : (5 * i - 1) / 3; b < 2 * i; ++b) n += p1X(b);
  return n;
}
constexpr int p2P(int i) { return i + 4 < 16 ? 3 : 0; }
constexpr int p2E(int i) { return i < 0 ? 0 : (i == 2 ? 2 : 0) + (i == 7 ? 1 : 0) + (i == 11 ? 2 : 0) + (i == 12 ? 7 : 0) + (i == 13 ? 5 : 0); }
constexpr int p2Z(int i) { return i >= 0 && i < 8 ? 1 : 0; }
constexpr int p2cnt(int st, bool q) {
  return p2E(st - 3) + p2P(st - 2) + p2E(st - 2) + p2P(st - 1) + p2E(st - 1) + ((q && QVAR == 0) || QVAR == 2 ? 0 : p2Z(st - 3) + p2Z(st - 2) + p2Z(st - 1));
}

__global__ __launch_bounds__(512, 2) void fused(const char* __restrict__ x, const char* __restrict__ wqkv,
                                                const char* __restrict__ wphi, const char* __restrict__ wo,
                                                const char* __restrict__ win, const char* __restrict__ wsp,
                                                const char* __restrict__ bsp, const char* __restrict__ wout,
                                                char* out, char* ws) {
  extern __shared__ __attribute__((aligned(16))) char smem[];
  const int tid = threadIdx.x, lane = tid & 63;
  const int w = __builtin_amdgcn_readfirstlane(tid >> 6);
  const int r16 = lane & 15, g = lane >> 4;
  const int tq = r16 >> 2, tp = r16 & 3;
  const int bid = blockIdx.x, xcd = bid & 7, jj = bid >> 3;
  const int rt16 = (xcd >> 2) * 8 + (jj >> 2);
  const bool isqkv = (jj & 2) == 0;
  const int grp = (xcd & 3) * 2 + (jj & 1);
  const int cg = isqkv ? grp : 8 + grp;
  const int zft0 = grp < 7 ? grp * 6 : 40;
  const int znft = grp < 6 ? 6 : (grp == 6 ? 4 : 3);
  const int wm = w >> 2, wn = (w & 3) ^ wm;
  const int rot1 = (rt16 & 3) * 2;
  const int rt64 = xcd * 4 + (jj >> 3);
  const int cgp = jj & 7;
  const int ft0 = cgp < 7 ? cgp * 6 : 40;
  const int npair = cgp < 6 ? 3 : 2;
  const int th = w & 1, kq = w >> 1;
#define ROT2 cgp
  const int rot2 = ROT2;

  int xsrc[2], wsrc[3];
#pragma unroll
  for (int u = 0; u < 2; ++u) {
    int row = 16 * w + 8 * u + (lane >> 3);
    xsrc[u] = (rt16 * 128 + row) * 1024 + (((lane & 7) ^ ((row >> 1) & 7)) << 4);
  }
  const int wstride = isqkv ? 64 * 3072 : 64 * 2752;
#pragma unroll
  for (int u = 0; u < 3; ++u) {
    int ci = u * 64 + lane;
    int rl = (ci * 2731) >> 16, c = ci - rl * 24;
    int h2 = ((rl >> 1) & 1) | ((w & 1) << 1);
    int cs = c ^ (h2 << 1);
    int vq = (8 * w + rl) * 3072 + (cs >> 3) * 1024 + grp * 128 + (cs & 7) * 16;
    int cz = min(cs < 12 ? cs : cs - 12, znft * 2 - 1);
    int vz = (8 * w + rl) * 2752 + (cs < 12 ? 1376 : 0) + zft0 * 32 + cz * 16;
    wsrc[u] = isqkv ? vq : vz;
  }
  asm volatile("" : "+v"(xsrc[0]), "+v"(xsrc[1]), "+v"(wsrc[0]), "+v"(wsrc[1]), "+v"(wsrc[2]));
  asm volatile("" : "+s"(x), "+s"(wqkv), "+s"(wphi), "+s"(wo), "+s"(win), "+s"(wsp), "+s"(bsp), "+s"(wout), "+s"(out), "+s"(ws));
  const __amdgpu_buffer_rsrc_t wsr = __builtin_amdgcn_make_buffer_rsrc(ws, 0, WS_END, 0x00020000);
  const u32x4 wsd = mkdesc(ws, WS_END);
  const u32x4 xd = mkdesc(x, 2048u * 1024u);
  const u32x4 wd = isqkv ? mkdesc(wqkv, 512u * 3072u) : mkdesc(win, 512u * 2752u);
  const u32x4 ad = mkdesc(wsp, 2048u * 4096u);
  const f32x4 zero4 = {0.f, 0.f, 0.f, 0.f};
  const unsigned ONES = 0x3f803f80u;
  const bf16x8 ones8 = asb8(u32x4{ONES, ONES, ONES, ONES});
  STAMP(0);
  auto dma1 = [&](int st, int u) {
    const int slot = (st & 3) * L_SLOT, sr = (st + rot1) & 7;
    if (u < 2) dma16(xd, xsrc[u], sr * 128, slot + (16 * w + 8 * u) * 128);
    else dma16(wd, wsrc[u - 2], sr * wstride, slot + 16384 + (3 * w + u - 2) * 1024);
  };
#pragma unroll
  for (int u = 0; u < 5; ++u) dma1(0, u);
  unsigned ep;
  u32x4 xrow;
  asm volatile("buffer_load_dword %0, off, %2, 0 sc1\n\tbuffer_load_dwordx4 %1, %3, %4, 0 offen"
               : "=&v"(ep), "=&v"(xrow) : "s"(wsd), "v"((bid * 8 + w) * 1024 + lane * 16), "s"(xd) : "memory");
#pragma unroll
  for (int u = 0; u < 5; ++u) dma1(1, u);

  const int asrc = (rt64 * 64 + th * 32 + (lane >> 3)) * 4096 + kq * 128 + (((lane & 7) ^ ((lane >> 4) & 3)) << 4);
  auto dmaA = [&](int c, int u) {
    dma16(ad, asrc ^ ((u & 1) << 6), ((c + rot2) & 7) * 512 + u * 8 * 4096, (c % 3) * L_CB + w * 4096 + u * 1024);
  };
  auto piece = [&](int p) {
    if (p < 30) dma1(2 + p / 5, p % 5);
    else if (p < 42) dmaA((p - 30) >> 2, (p - 30) & 3);
  };
  const bool zeven = (wn & 1) == 0;
  const int hb = 3 * (wn >> 1);
  int li[3];
  li[0] = zeven ? hb : hb + 2; li[1] = hb + 1; li[2] = zeven ? hb : hb + 2;
  int aoff[2], boff[3], sgi[3];
  sgi[0] = isqkv ? wn : li[0];
  sgi[1] = isqkv ? 4 + wn : (zeven ? li[1] : 6 + li[1]);
  sgi[2] = isqkv ? 8 + wn : 6 + li[2];
#pragma unroll
  for (int ks = 0; ks < 2; ++ks) aoff[ks] = (wm * 64 + r16) * 128 + (((4 * ks + g) ^ ((r16 >> 1) & 7)) << 4);
  {
    int h2 = ((tq >> 1) & 1) | ((g & 1) << 1);
#pragma unroll
    for (int i = 0; i < 3; ++i) boff[i] = 16384 + (8 * g + tq) * 384 + (((2 * sgi[i] + (tp >> 1)) ^ (h2 << 1)) << 4) + (tp & 1) * 8;
  }
  f32x4 acc[4][3], csum[3];
#pragma unroll
  for (int i = 0; i < 3; ++i) { acc[0][i] = zero4; acc[1][i] = zero4; acc[2][i] = zero4; acc[3][i] = zero4; csum[i] = zero4; }
  bf16x8 fa[2][4], fb[2][3];
  u32x4 rst, cst;
  u32x2 wrow;
  unsigned tag = 0, pf;
  float st_s = 0.f, st_q = 0.f;
  float rs[4], ka[4], kb[4], mh[4], ml[4];
  const bool csprod = rt16 < 4 && wm == 0;
  auto ld_stats = [&]() {
    ld128d(rst, wsd, lane * 16, WS_RST + (rt16 * 128 + wm * 64) * 16);
    ld128d(cst, wsd, lane * 16, WS_CS + (cg * 4 + wn) * 1024);
  };
  auto rdA = [&](int st, int ks, int m) { fa[ks][m] = *(const bf16x8*)(smem + (st & 3) * L_SLOT + aoff[ks] + m * 2048); };
  auto rdB = [&](int st, int ks, int n) {
    const char* p = smem + (st & 3) * L_SLOT + boff[n] + ks * 12288;
    fb[ks][n] = cat8(trread(p), trread(p + 1536));
  };
  waitv<7>();
  BAR();
  STAMP(1);
  rdB(0, 0, 0); rdA(0, 0, 0); rdA(0, 0, 1); rdA(0, 0, 2); rdA(0, 0, 3); rdB(0, 0, 1); rdB(0, 0, 2);
  sfor<16>([&](auto B) {
    constexpr int b = B, i = b >> 1, ks = b & 1;
    constexpr int nst = (b + 1) >> 1, nks = ks ^ 1;
    constexpr int p0 = 3 * b, p1 = 3 * b + 3 < 34 ? 3 * b + 3 : 34;
    sfor<6>([&](auto N) {
      constexpr int p = N, n = p >> 1, m = (p & 1) * 2;
      SB();
      acc[m][n] = mfma32(fb[ks][n], fa[ks][m], acc[m][n]);
      acc[m + 1][n] = mfma32(fb[ks][n], fa[ks][m + 1], acc[m + 1][n]);
      SB();
      if constexpr (nst < 8) {
        if constexpr (p < 3) rdB(nst, nks, p);
        if constexpr (p < 4) rdA(nst, nks, p);
      }
      if constexpr (p >= 1 && p0 + p - 1 < p1) piece(p0 + p - 1);
      if constexpr (b == 13) {
        if constexpr (p == 0) {
          pin(rst); pin(cst); pin(wrow);
#pragma nounroll
          for (int spin = 0; spin < SPIN_CAP; ++spin) {
            if (__builtin_expect(__all(rst[1] == tag && rst[3] == tag && (cst[0] ^ cst[1] ^ cst[2] ^ cst[3]) == tag), 1)) break;
            __builtin_amdgcn_s_sleep(PSLEEP);
            ld_stats();
            WAITV(0);
            pin(rst); pin(cst);
          }
        }
        if constexpr (p == 1) bcast4(asf(pk2(-asf(rst[2]), -asf(rst[2]))), mh);
        if constexpr (p == 2) bcast4(asf(pk2(-asf(rst[2]) - bflo(pk2(-asf(rst[2]), 0.f)), 0.f)), ml);
        if constexpr (p == 3) bcast4(isqkv ? asf(rst[0]) : __builtin_amdgcn_rcpf(asf(rst[0])), rs);
        if constexpr (p == 4) bcast4(asf(rst[0]) * (isqkv ? L2E : -2.0f * 0.7978845608028654f * L2E), ka);
        if constexpr (p == 5) bcast4(asf(rst[0]) * asf(rst[0]) * asf(rst[0]) * (-2.0f * 0.7978845608028654f * L2E * 0.044715f), kb);
      }
      if constexpr (b == 14) {
        constexpr int q = (p + 3) % 6, fn = q >> 1, fm = (q & 1) * 2;
        acc[fm][fn] = mfma16(u32x2{cst[fn], cst[fn]}, u32x2{asu(mh[fm]), asu(ml[fm])}, acc[fm][fn]);
        acc[fm + 1][fn] = mfma16(u32x2{cst[fn], cst[fn]}, u32x2{asu(mh[fm + 1]), asu(ml[fm + 1])}, acc[fm + 1][fn]);
      }
      if constexpr (b == 1) {
        if constexpr (p == 0) {
          pin(ep); pin(xrow);
          tag = __builtin_amdgcn_readfirstlane(ep) + 1u;
#pragma unroll
          for (int e = 0; e < 4; ++e) { st_s = dot2(xrow[e], ONES, st_s); st_q = dot2(xrow[e], xrow[e], st_q); }
        }
        if constexpr (p == 1) redg2<false>(st_s, st_q);
        if constexpr (p == 2) { st_s = dppadd<0x128>(st_s); st_q = dppadd<0x128>(st_q); st_s = dppadd<0x124>(st_s); st_q = dppadd<0x124>(st_q); }
        if constexpr (p == 3) { st_s = dppadd<0x122>(st_s); st_q = dppadd<0x122>(st_q); st_s = dppadd<0x121>(st_s); st_q = dppadd<0x121>(st_q); }
        if constexpr (p == 4) {
          float mean = st_s * (1.0f / 512.0f);
          float var = fmx(st_q * (1.0f / 512.0f) - mean * mean, 0.f);
          float rs = __builtin_amdgcn_rsqf(var + 1e-5f);
          if (lane == 0)
            st128d(u32x4{asu(rs), tag, asu(mean), tag}, wsd, WS_RST + (bid * 8 + w) * 16, 0);
        }
      }
    });
    if constexpr (b < 4) {
      if (csprod) {
#pragma unroll
        for (int n = 0; n < 3; ++n) csum[n] = mfma32(ones8, fb[ks][n], csum[n]);
        if constexpr (b == 3) {
          u32x4 o;
#pragma unroll
          for (int n = 0; n < 3; ++n) o[n] = pk2(csum[n][0], csum[n][0] - bflo(pk2(csum[n][0], 0.f)));
          o[3] = tag ^ o[0] ^ o[1] ^ o[2];
          if (g == 0) st128d(o, wsd, r16 * 16, WS_CS + (cg * 4 + wn) * 1024 + rt16 * 256);
        }
      }
    }
    if constexpr (ks == 0 && i < 7) {
      WAITL0();
      waitv<(i < 6 ? p1cnt(i) : 5)>();
      BAR();
    }
    if constexpr (b == 11) {
      const int l8 = lane & 7, li = bid * 40 + w * 8 + l8;
      const char* p = w < 5 ? (li < 4096 ? wo + li * 128 : wout + min(li - 4096, 5503) * 128)
                            : (const char*)(__builtin_amdgcn_s_getpc() & ~127ull) + min(jj * 8 + l8, CODE_LINES - 1) * 128;
      gld32(pf, p);
    }
    if constexpr (b == 12) {
#pragma unroll
      for (int e = 0; e < 2; ++e)
        st128d(u32x4{EMPTY, EMPTY, EMPTY, EMPTY}, wsd, (e == 0 || kq + 4 < 2 * npair) ? lane * 16 : DROP, WS_A3F + ((rt64 * 2 + th) * 76 + 32 + ft0 + kq + 4 * e) * 1024);
    }
    if constexpr (b == 9) {
      ld_stats();
      ld64p(wrow, mkdesc(wphi, 32u * 512u), r16 * 32 + g * 8, (grp * 4 + wn) * 512);
    }
  });
  STAMP(2);
  bf16x8 af[8][2][2];
  const int aro = w * 4096 + r16 * 128;
  auto wsp_ld = [&](int c) {
#pragma unroll
    for (int e = 0; e < 2; ++e)
#pragma unroll
      for (int t = 0; t < 2; ++t)
        af[c][e][t] = *(const bf16x8*)(smem + (c % 3) * L_CB + aro + t * 2048 + (((4 * e + g) ^ ((r16 >> 1) & 7)) << 4));
    SB();
  };
  auto wsp_rd = [&](int c) {
    SB();
    if (c == 1) pin(pf);
    if (c < 6) { WAITV(8); } else if (c == 6) { WAITV(4); } else { WAITV(0); }
    wsp_ld(c);
  };
  auto wsp_is = [&](int c, int u0, int u1) {
    SB();
    if (u0 == 0) WAITL0();
    for (int u = u0; u < u1; ++u) dmaA(c, u);
    SB();
  };
  char* scr = smem + L_U + w * 8192;
  u32x4 pq[4];
  STAMP(3);
  if (isqkv) {
    f32x4 lg[4];
    u32x4 pk[4];
    const unsigned one = (r16 & 1) ? 0x3f800000u : 0x3f80u;
    const u32x2 wph = pk4(mfma16(wrow, u32x2{(tq == g && tp < 2) ? one : 0u, (tq == g && tp >= 2) ? one : 0u}, zero4));
#pragma unroll
    for (int m = 0; m < 4; ++m) lg[m] = mfma16(wph, pk4(acc[m][1] * ka[m]), zero4);
    hedgehog4(lg, pk, [&](auto I) {
      constexpr int i = I;
      if constexpr (i >= 1 && i <= 4) wsp_is(1 + ((i - 1) >> 1), ((i - 1) & 1) * 2, ((i - 1) & 1) * 2 + 2);
    });
    f32x4 kv[2] = {zero4, zero4}, kz[2] = {zero4, zero4};
    char* sc3 = smem + L_U + w * 3072;
#pragma unroll
    for (int mp = 0; mp < 2; ++mp) {
#pragma unroll
      for (int m = 0; m < 2; ++m) {
        *(u32x4*)(sc3 + (16 * m + r16) * 64 + g * 16) = pk[2 * mp + m];
        *(u32x2*)(sc3 + 2048 + (16 * m + r16) * 32 + g * 8) = pk4(acc[2 * mp + m][2] * rs[2 * mp + m]);
      }
      const int row = 8 * g + tq;
      const char* pp = sc3 + row * 64 + tp * 16;
      const char* pv = sc3 + 2048 + row * 32 + tp * 8;
      bf16x8 ap = cat8(trread(pp), trread(pp + 256));
      bf16x8 an = cat8(trread(pp + 8), trread(pp + 264));
      bf16x8 bv = cat8(trread(pv), trread(pv + 128));
      kv[0] = mfma32(ap, bv, kv[0]);
      kv[1] = mfma32(an, bv, kv[1]);
      kz[0] = mfma32(ap, ones8, kz[0]);
      kz[1] = mfma32(an, ones8, kz[1]);
    }
#pragma unroll
    for (int m = 0; m < 4; ++m) lg[m] = mfma16(wph, pk4(acc[m][0] * ka[m]), zero4);
    *(f32x4*)(sc3 + lane * 16) = wm ? kv[0] : kv[1];
    if (r16 == 0) *(f32x4*)(sc3 + 1024 + g * 16) = wm ? kz[0] : kz[1];
    LBAR();
    {
      const char* p = smem + L_U + (w ^ 5) * 3072;
      const f32x4 a = (wm ? kv[1] : kv[0]) + *(const f32x4*)(p + lane * 16);
      const f32x4 b = (wm ? kz[1] : kz[0]) + *(const f32x4*)(p + 1024 + g * 16);
      const int base = WS_KVP + (grp * 16 + rt16) * KVP_SZ;
      const int hs = wn * 2 + wm;
      st128d(u32x4{asu(a[0]), tag, asu(a[1]), tag}, wsd, lane * 16, base + hs * 2048);
      st128d(u32x4{asu(a[2]), tag, asu(a[3]), tag}, wsd, lane * 16, base + hs * 2048 + 1024);
      const float v0 = r16 ? b[2] : b[0], v1 = r16 ? b[3] : b[1];
      st128d(u32x4{asu(v0), tag, asu(v1), tag}, wsd, r16 < 2 ? g * 32 + r16 * 16 : DROP, base + 16384 + hs * 128);
    }
    STAMP(4);
    u32x4 a, b;
    const int l1off = (lane >> 3) * KVP_SZ + (lane & 7) * 16;
    auto ld_l1 = [&](int sub) {
      ld128d(a, wsd, l1off, WS_KVP + grp * 16 * KVP_SZ + sub * 128);
      ld128d(b, wsd, l1off, WS_KVP + (grp * 16 + 8) * KVP_SZ + sub * 128);
    };
    hedgehog4(lg, pq, [&](auto I) {
      constexpr int i = I;
      if constexpr (i == 0) { SB(); WAITV(3); pin(pf); wsp_ld(0); wsp_ld(1); wsp_ld(2); }
      if constexpr (i >= 1 && i <= 6) wsp_is(3 + ((i - 1) >> 1), ((i - 1) & 1) * 2, ((i - 1) & 1) * 2 + 2);
      if constexpr (i == 7) ld_l1(rt16 * 8 + w);
    });
    SB(); WAITV(10); wsp_ld(3);
    wsp_is(6, 0, 4);
    SB(); WAITV(10); wsp_ld(4);
    wsp_is(7, 0, 4);
    STAMP(5);
    WAITV(8);
#pragma nounroll
    for (int rep = 0; rep < ((w == 0 && rt16 < 8) ? 2 : 1); ++rep) {
      const int sub = rep ? 128 + rt16 : rt16 * 8 + w;
#pragma nounroll
      for (int spin = rep ? -1 : 0; spin < SPIN_CAP; ++spin) {
        if (spin >= 0) {
          pin(a); pin(b);
          if (__builtin_expect(__all(a[1] == tag && a[3] == tag && b[1] == tag && b[3] == tag), 1)) break;
          __builtin_amdgcn_s_sleep(8);
        }
        ld_l1(sub);
        WAITV(0);
      }
      float v0 = asf(a[0]) + asf(b[0]), v1 = asf(a[2]) + asf(b[2]);
      v0 = dppadd<0x128>(v0); v1 = dppadd<0x128>(v1);
      redg2<false>(v0, v1);
      const int dsto = rep ? (sub * 8 + lane) * 8 : ((sub >> 4) * 64 + (sub & 7) * 8 + lane) * 16 + ((sub >> 3) & 1) * 8;
      __builtin_amdgcn_raw_buffer_store_b64(u32x2{pk2(v0, v1), tag}, wsr, lane < 8 ? dsto : DROP, WS_KVR + grp * 1088 * 8, 16);
    }
    STAMP(6);
  } else {
    const bool v0 = li[0] < znft, v1 = li[1] < znft;
    gelu16<0>(acc, ka, kb, rs);
    STAMP(22);
    wsp_is(1, 0, 4);
    STAMP(23);
    {
      f32x4 s4[4], q4[4];
#pragma unroll
      for (int m = 0; m < 4; ++m) { s4[m] = v0 ? acc[m][0] : zero4; q4[m] = s4[m] * s4[m]; }
      if (zeven) {
        gelu16<1>(acc, ka, kb, rs);
#pragma unroll
        for (int m = 0; m < 4; ++m) {
          const f32x4 y = v1 ? acc[m][1] : zero4;
          s4[m] += y; q4[m] = __builtin_elementwise_fma(y, y, q4[m]);
        }
      }
      float s[4], q[4];
#pragma unroll
      for (int m = 0; m < 4; ++m) { s[m] = sum4(s4[m]); q[m] = sum4(q4[m]); }
      *(f32x2*)(smem + L_ZS + (wn >> 1) * 8192 + (wn & 1) * 1024 + (wm * 64 + lane) * 8) = f32x2{redg4<false>(s), redg4<false>(q)};
    }
    STAMP(20);
    if (w >= 2) wsp_is(2, 0, 4);
    STAMP(21);
    LBAR();
    if (tid < 128) {
      f32x2 a = *(const f32x2*)(smem + L_ZS + tid * 8) + *(const f32x2*)(smem + L_ZS + 1024 + tid * 8);
      f32x2 b = *(const f32x2*)(smem + L_ZS + 8192 + tid * 8) + *(const f32x2*)(smem + L_ZS + 9216 + tid * 8);
      st128d(u32x4{asu(a[0] + b[0]), tag, asu(a[1] + b[1]), tag}, wsd,
                                             WS_ZST + ((rt16 * 8 + grp) * 128 + tid) * 16, 0);
    }
    STAMP(4);
    if (w < 2) wsp_is(2, 0, 4);
    gelu16<2>(acc, ka, kb, rs);
    u32x4 zs[2];
    const int zso = WS_ZST + (rt16 * 8 + w) * 2048;
    auto ld_zs = [&]() { ld128d(zs[0], wsd, lane * 16, zso); ld128d(zs[1], wsd, lane * 16, zso + 1024); };
    ld_zs();
    SB(); WAITV(10); wsp_ld(0);
    wsp_is(3, 0, 4);
    STAMP(5);
    WAITV(4);
    pin(pf);
    wsp_ld(1); wsp_ld(2);
#pragma nounroll
    for (int spin = 0; spin < SPIN_CAP; ++spin) {
      pin(zs[0]); pin(zs[1]);
      if (__all(zs[0][1] == tag && zs[0][3] == tag && zs[1][1] == tag && zs[1][3] == tag)) break;
      __builtin_amdgcn_s_sleep(2);
      ld_zs();
      WAITV(0);
    }
    *(f32x2*)(scr + 4096 + lane * 8) = f32x2{asf(zs[0][0]), asf(zs[0][2])};
    *(f32x2*)(scr + 4608 + lane * 8) = f32x2{asf(zs[1][0]), asf(zs[1][2])};
    LBAR();
    STAMP(6);
    {
      f32x2 sq = {0.f, 0.f};
#pragma unroll
      for (int j = 0; j < 8; ++j) sq += *(const f32x2*)(smem + L_U + j * 8192 + 4096 + (wm * 64 + lane) * 8);
      const float mean = sq[0] * (1.0f / 688.0f);
      const float var = fmx(sq[1] * (1.0f / 688.0f) - mean * mean, 0.f);
      const float r = __builtin_amdgcn_rsqf(var + 1e-5f);
      float zr[4], zc[4];
      bcast4(r, zr);
      bcast4(-mean * r, zc);
      const int dst = WS_Z2F + (((rt16 >> 1) * 2 * 44 + zft0) * 4 + ((2 * rt16 + wm) & 3)) * 1024;
#pragma unroll
      for (int n = 0; n < 2; ++n) {
#pragma unroll
        for (int m = 0; m < 4; ++m) *(u32x2*)(scr + n * 2048 + (16 * m + r16) * 32 + g * 8) = pk4(acc[m][n] * zr[m] + zc[m]);
        const int dn = dst + li[n] * 4096, vo = (n == 0 ? v0 : (v1 && zeven)) ? lane * 16 : DROP;
#pragma unroll
        for (int kk = 0; kk < 2; ++kk) {
          const char* p = scr + n * 2048 + (32 * kk + 8 * g + tq) * 32 + tp * 8;
          bf16x8 f = cat8(trread(p), trread(p + 128));
          st128d(__builtin_bit_cast(u32x4, f), wsd, vo, dn + kk * 44 * 4096);
        }
      }
    }
    STAMP(17);
    if (!zeven) gelu16<1>(acc, ka, kb, rs);
    STAMP(18);
    WAITV(0);
    BAR();
    if (tid == 0) __builtin_amdgcn_raw_buffer_store_b64(u32x2{tag, tag ^ TAGX}, wsr, WS_FLZ + grp * 1024 + rt16 * 8, 0, 16);
    STAMP(19);
    wsp_is(4, 0, 4);
    wsp_is(5, 0, 4);
    wsp_ld(3);
    wsp_is(6, 0, 4);
    wsp_rd(4);
    wsp_is(7, 0, 4);
#pragma unroll
    for (int mp = 0; mp < 2; ++mp) {
      const u32x2 a = pk4(acc[2 * mp][2]), b = pk4(acc[2 * mp + 1][2]), c = pk4(acc[2 * mp][1]), d = pk4(acc[2 * mp + 1][1]);
      pq[mp] = u32x4{a[0], a[1], b[0], b[1]};
      pq[2 + mp] = u32x4{c[0], c[1], d[0], d[1]};
    }
  }
  STAMP(7);
  unsigned biasv[2];
#pragma unroll
  for (int t = 0; t < 2; ++t) ldu16(biasv[t], mkdesc(bsp, 4096u), r16 * 2, (rt64 * 64 + th * 32 + t * 16) * 2);
  wsp_rd(5); wsp_rd(6); wsp_rd(7);
  pin(biasv[0]); pin(biasv[1]);
  if (!isqkv) {
    const int z1o = WS_Z1F + ((rt16 * 4 + wm * 2) * 44 + zft0) * 1024;
    const int vo2 = li[2] < znft ? lane * 16 : DROP, vo1 = (li[1] < znft && !zeven) ? lane * 16 : DROP;
#pragma unroll
    for (int mp = 0; mp < 2; ++mp) {
      st128d(pq[mp], wsd, vo2, z1o + (mp * 44 + li[2]) * 1024);
      st128d(pq[2 + mp], wsd, vo1, z1o + (mp * 44 + li[1]) * 1024);
    }
  }
  STAMP(8);
  int lz = lane;
  asm volatile("" : "+v"(lz));
  const int l16 = lz * 16;
  auto poll_z = [&]() -> unsigned {
    u32x2 f = poll64(wsd, ((l16 >> 1) + 16 * rot2) & 127, WS_FLZ + cgp * 1024);
    return (unsigned)__ballot(f[0] == tag && f[1] == (tag ^ TAGX)) & 0xffffu;
  };
  const int w3off = (lz >> 3) * 1024 + cgp * 128 + (((lz & 7) ^ ((lz >> 4) << 1)) << 4);
  const u32x4 od = mkdesc(wo, 512u * 1024u);
  if (isqkv) {
    u32x4 kr[4];
    auto ld_kr = [&]() {
#pragma unroll
      for (int j = 0; j < 2; ++j) {
        ld128d(kr[j], wsd, l16, WS_KVR + (grp * 1088 + wn * 256) * 8 + j * 1024);
        ld128d(kr[2 + j], wsd, (l16 >> 4) & 48, WS_KVR + (grp * 1088 + 1024 + wn * 16) * 8 + j * 64);
      }
    };
    BAR();
#pragma unroll
    for (int i = 0; i < 8; ++i) dma16(od, w3off, (i * 8 + w) * 8192, L_U + (i * 8 + w) * 1024);
#pragma nounroll
    for (int spin = 0; spin < SPIN_CAP; ++spin) {
      ld_kr();
      WAITV(0);
      bool ok = true;
#pragma unroll
      for (int j = 0; j < 4; ++j) { pin(kr[j]); ok = ok && kr[j][1] == tag && kr[j][3] == tag; }
      if (__builtin_expect(__all(ok), 1)) break;
      __builtin_amdgcn_s_sleep(8);
    }
    const bf16x8 kvo = asb8(u32x4{kr[0][0], kr[0][2], kr[1][0], kr[1][2]});
    const bf16x8 zo = asb8(u32x4{kr[2][0], kr[2][2], kr[3][0], kr[3][2]});
    u32x2 ov[4];
#pragma unroll
    for (int m = 0; m < 4; ++m) {
      const f32x4 num = mfma32(kvo, asb8(pq[m]), zero4), den = mfma32(zo, asb8(pq[m]), zero4);
      ov[m] = pk4(num * __builtin_amdgcn_rcpf(den[0] + 1e-6f));
    }
#pragma unroll
    for (int mp = 0; mp < 2; ++mp)
      st128d(u32x4{ov[2 * mp][0], ov[2 * mp][1], ov[2 * mp + 1][0], ov[2 * mp + 1][1]}, wsd,
             l16, WS_A3F + ((rt16 * 4 + wm * 2 + mp) * 76 + grp * 4 + wn) * 1024);
  }
  STAMP(9);
  unsigned rdy = 0;
  if (w == 0) {
#pragma nounroll
    for (int spin = 0; spin < SPIN_CAP; ++spin) {
      rdy = poll_z();
      if ((rdy & 15u) == 15u) break;
      __builtin_amdgcn_s_sleep(PSLEEP);
    }
    if (lane == 0) *(unsigned*)(smem + 2 * L_CB) = rdy;
  }
  LBAR();
  rdy = __builtin_amdgcn_readfirstlane(*(const unsigned*)(smem + 2 * L_CB));
  STAMP(10);
  const int bsrc = WS_Z2F + ft0 * 4096 + w * 1024;
  const int u2off = npair == 3 ? 16384 : 0;
  const int l16n = npair == 3 ? l16 : DROP;
  auto dma2 = [&](int st, int u) {
    const int n = 2 * u + (w >> 2), q = w & 3;
    dma16d(wsd, l16, bsrc + ((st + 2 * rot2) & 15) * (44 * 4096) + (u < 2 ? u * 8192 : u2off), (st & 3) * L_RING + (n ^ (u < 2 ? q : (q & 1))) * 4096 + q * 1024);
  };
#pragma unroll
  for (int st = 0; st < 2; ++st)
#pragma unroll
    for (int u = 0; u < 3; ++u) dma2(st, u);
  waitv<3>();
  f32x4 ac2[6][2];
#pragma unroll
  for (int n = 0; n < 6; ++n)
#pragma unroll
    for (int t = 0; t < 2; ++t) { const float b = (n == 0 || (n == 4 && kq < 2)) ? bflo(biasv[t]) : 0.f; ac2[n][t] = f32x4{b, b, b, b}; }
  const u32x4 ud = mkdesc(wout, 688u * 1024u);
  bf16x8 fz[2][6];
  auto rdZ = [&](int st, int n) { fz[st & 1][n] = *(const bf16x8*)(smem + (st & 3) * L_RING + n * 4096 + kq * 1024 + l16); };
  u32x4 ao[4][2];
  u32x4 z1v[2];
  u32x2 ofl;
  const int a3 = WS_A3F + ((rt64 * 2 + th) * 76 + 2 * kq) * 1024;
  auto flo = [&]() {
    return l16 < 512 ? WS_FLO + rt64 * 256 + (l16 >> 1) : (l16 < 640 ? WS_FLU + rt64 * 512 + ((l16 >> 1) & 63) : WS_FLZ + cgp * 1024 + 512 + (rt64 >> 1) * 8);
  };
  BAR();
#pragma unroll
  for (int n = 0; n < 6; ++n) rdZ(0, n);
#pragma unroll
  for (int st = 2; st < 4; ++st)
#pragma unroll
    for (int u = 0; u < 3; ++u) dma2(st, u);
  char* rb = smem + 36864 + th * 18432 + l16;
  auto red_wr = [&](int i) {
    if (i == 4 && kq < 2) return;
    const int blk = i < 4 ? (i ^ kq) * 3 + i - 1 : (i ^ (kq & 1)) * 3 + (i == 5 ? (kq & 2) : 1);
    const u32x2 a = pk4(ac2[i][0]), b = pk4(ac2[i][1]);
    *(u32x4*)(rb + (blk << 10)) = u32x4{a[0], a[1], b[0], b[1]};
  };
  sfor<16>([&](auto ST) {
    constexpr int st = ST;
    if constexpr (st < 15) {
      WAITL0();
      if constexpr (p2cnt(st, true) != p2cnt(st, false)) { if (isqkv) waitv<p2cnt(st, true)>(); else waitv<p2cnt(st, false)>(); }
      else waitv<p2cnt(st, false)>();
      BAR();
    }
    if constexpr ((st & 3) == 0) { STAMP(24 + st / 4); }
    sfor<6>([&](auto N) {
      constexpr int n = N;
      constexpr int ord[6] = {1, 2, 3, 5, 4, 0}, c = st == 15 ? ord[n] : n;
      SB();
      ac2[c][0] = mfma32(fz[st & 1][c], af[st >> 1][st & 1][0], ac2[c][0]);
      ac2[c][1] = mfma32(fz[st & 1][c], af[st >> 1][st & 1][1], ac2[c][1]);
      SB();
      if constexpr (st == 15 && n >= 1) red_wr(ord[n - 1]);
      if constexpr (st < 15 && n >= 1 && n <= 3) { rdZ(st + 1, 2 * n - 2); rdZ(st + 1, 2 * n - 1); }
      if constexpr (st + 4 < 16 && n == 0 && (st & 1) == 0) {
        if (__builtin_expect(((rdy >> (st + 4)) & 3u) != 3u, 0)) {
#pragma nounroll
          for (int spin = 0; spin < SPIN_CAP; ++spin) {
            rdy = poll_z();
            if (((rdy >> (st + 4)) & 3u) == 3u) break;
            __builtin_amdgcn_s_sleep(PSLEEP);
          }
        }
      }
      if constexpr (st + 4 < 16 && n < 3) dma2(st + 4, n);
      if constexpr (n == 3) {
        if constexpr (st < 8 && QVAR != 2) { if (QVAR == 1 || !isqkv) dma16(od, w3off, (st * 8 + w) * 8192, L_U + (st * 8 + w) * 1024); }
        if constexpr (st == 12 || st == 13) {
#pragma unroll
          for (int j = 3 * (st - 12); j < (st == 12 ? 3 : 4); ++j) dma16(ud, w3off, (j * 8 + w) * 8192, (j * 8 + w) * 1024);
        }
        if constexpr (st == 2) {
          const int fo = isqkv ? WS_FLO + ((rt16 * 2 + wm) * 32 + grp * 4 + wn) * 8 : WS_FLZ + grp * 1024 + 512 + rt16 * 8;
          __builtin_amdgcn_raw_buffer_store_b64(u32x2{tag, tag ^ TAGX}, wsr, (l16 == 0 && (isqkv || w == 1)) ? 0 : DROP, fo, 16);
          __builtin_amdgcn_raw_buffer_store_b64(u32x2{tag, tag ^ TAGX}, wsr, (l16 == 0 && w == 2) ? 0 : DROP, WS_FLU + rt64 * 512 + cgp * 8, 16);
        }
        if constexpr (st == 7) ld64ds(ofl, wsd, flo(), 0);
        if constexpr (st == 11) {
          pin(ofl);
#pragma nounroll
          for (int spin = 0; spin < SPIN_CAP; ++spin) {
            if (__builtin_expect(__all(ofl[0] == tag && ofl[1] == (tag ^ TAGX)), 1)) break;
            __builtin_amdgcn_s_sleep(PSLEEP);
            ofl = poll64(wsd, flo(), 0);
          }
#pragma unroll
          for (int e = 0; e < 2; ++e)
            ld128d(z1v[e], wsd, l16, WS_Z1F + ((rt64 * 2 + th) * 44 + ft0 + min(kq + 4 * e, 2 * npair - 1)) * 1024);
        }
        if constexpr (st == 12 || st == 13) {
#pragma unroll
          for (int i = 0; i < 2; ++i)
#pragma unroll
            for (int h = 0; h < 2; ++h) ld128d(ao[2 * (st - 12) + i][h], wsd, l16, a3 + (8 * (2 * (st - 12) + i) + h) * 1024);
        }
      }
    });
  });
  STAMP(11);
  {
    WAITV(0);
    pin(z1v[0]); pin(z1v[1]);
    LBAR();
#pragma unroll
    for (int e = 0; e < 2; ++e) {
      const int n = kq + 4 * e;
      if (e == 0 || n < 2 * npair) {
        u32x4 o, pr[3];
#pragma unroll
        for (int j = 0; j < 3; ++j) pr[j] = *(const u32x4*)(rb + ((n * 3 + j) << 10));
#pragma unroll
        for (int t = 0; t < 2; ++t) {
          f32x4 s = ac2[4 * e][t];
#pragma unroll
          for (int j = 0; j < 3; ++j) s += f32x4{bflo(pr[j][2 * t]), bfhi(pr[j][2 * t]), bflo(pr[j][2 * t + 1]), bfhi(pr[j][2 * t + 1])};
          o[2 * t] = pk2(bflo(z1v[e][2 * t]) * s[0], bfhi(z1v[e][2 * t]) * s[1]);
          o[2 * t + 1] = pk2(bflo(z1v[e][2 * t + 1]) * s[2], bfhi(z1v[e][2 * t + 1]) * s[3]);
        }
        if (ft0 + n >= 43) o = u32x4{0u, 0u, 0u, 0u};
        st128d(o, wsd, l16, WS_A3F + ((rt64 * 2 + th) * 76 + 32 + ft0 + n) * 1024);
      }
    }
  }
  STAMP(12);
  f32x4 ac3[4][2];
#pragma unroll
  for (int n = 0; n < 4; ++n) { ac3[n][0] = zero4; ac3[n][1] = zero4; }
  int ly = l16;
  asm volatile("" : "+v"(ly));
  const int lane3 = ly >> 4, r3 = lane3 & 15, g3 = lane3 >> 4, tid3 = w * 64 + lane3;
  int b3[4];
  {
    const int k = 4 * g3 + (r3 >> 2), tp = r3 & 3;
    const int h2 = ((k >> 1) & 1) | (((k >> 2) & 1) << 1);
#pragma unroll
    for (int n = 0; n < 4; ++n) b3[n] = k * 128 + (((2 * n + (tp >> 1)) ^ (h2 << 1)) << 4) + (tp & 1) * 8 + kq * 4096;
  }
  const int go = (rt64 * 64 + (tid3 >> 3)) * 1024 + cgp * 128 + (tid3 & 7) * 16;
  u32x4 xres;
  LBAR();
  STAMP(13);
  sfor<4>([&](auto I) {
    constexpr int i = I;
    bf16x8 b[4];
#pragma unroll
    for (int n = 0; n < 4; ++n) b[n] = cat8(trread(smem + L_U + b3[n] + i * 16384), trread(smem + L_U + b3[n] + i * 16384 + 2048));
    pin(ao[i][0]); pin(ao[i][1]);
#pragma unroll
    for (int j = 4 + 2 * i; j < 6 + 2 * i; ++j)
      if (j < 11) dma16(ud, w3off, min(j * 8 + w, 85) * 8192, (j * 8 + w) * 1024);
#pragma unroll
    for (int n = 0; n < 4; ++n)
#pragma unroll
      for (int t = 0; t < 2; ++t)
        ac3[n][t] = mfma32(b[n], asb8(u32x4{ao[i][0][2 * t], ao[i][0][2 * t + 1], ao[i][1][2 * t], ao[i][1][2 * t + 1]}), ac3[n][t]);
  });
  asm volatile("buffer_load_dwordx4 %0, %1, %2, 0 offen" : "=v"(xres) : "v"(go), "s"(xd) : "memory");
  if (bid == 0 && w == 7) {
#pragma nounroll
    for (int spin = 0; spin < SPIN_CAP; ++spin) {
      u32x4 f[4];
#pragma unroll
      for (int j = 0; j < 4; ++j) ld128d(f[j], wsd, lane3 * 128, WS_RST + j * 8192);
      WAITV(0);
#pragma unroll
      for (int j = 0; j < 4; ++j) pin(f[j]);
      if (__all(f[0][1] == tag && f[1][1] == tag && f[2][1] == tag && f[3][1] == tag)) break;
    }
    if (lane3 == 0) __builtin_amdgcn_raw_buffer_store_b32(tag, wsr, WS_EP, 0, 16);
  }
  u32x4 au[6][2];
  auto ld_au = [&](int i) {
#pragma unroll
    for (int h = 0; h < 2; ++h) ld128d(au[i][h], wsd, l16, a3 + (32 + h + ((i < 5 || kq < 2) ? 8 * i : 32)) * 1024);
  };
#pragma unroll
  for (int i = 0; i < 6; ++i) ld_au(i);
  waitv<12>();
  LBAR();
  STAMP(14);
  sfor<6>([&](auto I) {
    constexpr int i = I;
    bf16x8 b[4];
    const int io = (i < 5 || kq < 2) ? i * 16384 : 0;
#pragma unroll
    for (int n = 0; n < 4; ++n) b[n] = cat8(trread(smem + b3[n] + io), trread(smem + b3[n] + io + 2048));
    waitv<10 - 2 * i>();
#pragma nounroll
    for (int spin = 0; spin < SPIN_CAP; ++spin) {
      pin(au[i][0]); pin(au[i][1]);
      const u32x4 mx = __builtin_elementwise_max(au[i][0], au[i][1]);
      if (__builtin_expect(__all(max(max(mx[0], mx[1]), max(mx[2], mx[3])) != EMPTY), 1)) break;
      __builtin_amdgcn_s_sleep(PSLEEP);
      ld_au(i);
      WAITV(0);
    }
    if (i < 5 || kq < 2) {
#pragma unroll
      for (int n = 0; n < 4; ++n)
#pragma unroll
        for (int t = 0; t < 2; ++t)
          ac3[n][t] = mfma32(b[n], asb8(u32x4{au[i][0][2 * t], au[i][0][2 * t + 1], au[i][1][2 * t], au[i][1][2 * t + 1]}), ac3[n][t]);
    }
  });
  STAMP(15);
#pragma unroll
  for (int n = 0; n < 4; ++n)
#pragma unroll
    for (int t = 0; t < 2; ++t)
      *(f32x4*)(smem + L_U + kq * 16384 + (th * 32 + t * 16 + r3) * 256 + (((n * 4 + g3) ^ r3) << 4)) = ac3[n][t];
  LBAR();
  {
    pin(xres);
    const int row = tid3 >> 3, c8 = tid3 & 7;
    f32x4 s0 = {bflo(xres[0]), bfhi(xres[0]), bflo(xres[1]), bfhi(xres[1])};
    f32x4 s1 = {bflo(xres[2]), bfhi(xres[2]), bflo(xres[3]), bfhi(xres[3])};
#pragma unroll
    for (int j = 0; j < 4; ++j) {
      s0 += *(const f32x4*)(smem + L_U + j * 16384 + row * 256 + (((2 * c8) ^ (row & 15)) << 4));
      s1 += *(const f32x4*)(smem + L_U + j * 16384 + row * 256 + (((2 * c8 + 1) ^ (row & 15)) << 4));
    }
    const u32x2 lo = pk4(s0), hi = pk4(s1);
    st128d(u32x4{lo[0], lo[1], hi[0], hi[1]}, mkdesc(out, 2048u * 1024u), go, 0);
  }
  STAMP(16);
}

extern "C" void kernel_launch(void* const* d_in, const int* in_sizes, int n_in, void* d_out, int out_size, void* d_ws,
                              size_t ws_size, hipStream_t stream) {
  (void)hipFuncSetAttribute((const void*)fused, hipFuncAttributeMaxDynamicSharedMemorySize, LDS_BYTES);
  fused<<<256, 512, LDS_BYTES, stream>>>((const char*)d_in[0], (const char*)d_in[1], (const char*)d_in[2],
                                         (const char*)d_in[3], (const char*)d_in[5], (const char*)d_in[6],
                                         (const char*)d_in[7], (const char*)d_in[8], (char*)d_out, (char*)d_ws);
}
```

```cpp
#include <hip/hip_runtime.h>
#include <hip/hip_bf16.h>
#include <stdint.h>

typedef __bf16 bf16x8 __attribute__((ext_vector_type(8)));
typedef __bf16 bf16x2 __attribute__((ext_vector_type(2)));
typedef short s16x4 __attribute__((ext_vector_type(4)));
typedef short s16x8 __attribute__((ext_vector_type(8)));
typedef float f32x4 __attribute__((ext_vector_type(4)));
typedef float f32x2 __attribute__((ext_vector_type(2)));
typedef unsigned u32x4 __attribute__((ext_vector_type(4)));
typedef unsigned u32x2 __attribute__((ext_vector_type(2)));
#define LAS __attribute__((address_space(3)))
#define DI __device__ __forceinline__

constexpr int WS_EP  = 0;
constexpr int WS_FLZ = 1024;
constexpr int WS_FLO = WS_FLZ + 8192;
constexpr int WS_FLU = WS_FLO + 8192;
constexpr int WS_CS  = WS_FLU + 16384;
constexpr int WS_RST = WS_CS + 16 * 4 * 1024;
constexpr int WS_ZST = WS_RST + 2048 * 16;
constexpr int KVP_SZ = 1088 * 16;
constexpr int WS_KVP = WS_ZST + 16 * 8 * 128 * 16;
constexpr int WS_KVR = WS_KVP + 8 * 16 * KVP_SZ;
constexpr int WS_Z2F = WS_KVR + 8 * 1088 * 8;
constexpr int WS_Z1F = WS_Z2F + 16 * 44 * 4096;
constexpr int WS_A3F = WS_Z1F + 64 * 44 * 1024;
constexpr int WS_END = WS_A3F + 64 * 76 * 1024;
static_assert(WS_Z2F % 128 == 0 && WS_KVP % 128 == 0 && WS_KVR % 128 == 0, "alignment");

template <int V> struct IC { static constexpr int value = V; constexpr operator int() const { return V; } };
template <int I, int N, class F> DI void sfor_(F&& f) { if constexpr (I < N) { f(IC<I>{}); sfor_<I + 1, N>(f); } }
template <int N, class F> DI void sfor(F&& f) { sfor_<0, N>(f); }
template <int N> DI void waitv() { asm volatile("s_waitcnt vmcnt(%0)" :: "n"(N) : "memory"); }

DI float asf(unsigned u) { return __builtin_bit_cast(float, u); }
DI unsigned asu(float f) { return __builtin_bit_cast(unsigned, f); }
DI u32x4 mkdesc(const void* p, unsigned bytes) {
  unsigned long long a = (unsigned long long)p;
  u32x4 d = {(unsigned)a, (unsigned)(a >> 32) & 0xffffu, bytes, 0x00020000u};
  return d;
}
DI void dma16(u32x4 desc, int voff, int soff, int lds) {
  asm volatile("s_mov_b32 m0, %3\n\ts_nop 0\n\tbuffer_load_dwordx4 %0, %1, %2 offen lds"
               :: "v"(voff), "s"(desc), "s"(soff), "s"(lds) : "memory");
}
DI void dma16d(u32x4 desc, int voff, int soff, int lds) {
  asm volatile("s_mov_b32 m0, %3\n\ts_nop 0\n\tbuffer_load_dwordx4 %0, %1, %2 offen sc1 lds"
               :: "v"(voff), "s"(desc), "s"(soff), "s"(lds) : "memory");
}
DI u32x2 poll64(u32x4 desc, int voff, int soff) {
  u32x2 r;
  asm volatile("buffer_load_dwordx2 %0, %1, %2, %3 offen sc1\n\ts_waitcnt vmcnt(0)" : "=&v"(r) : "v"(voff), "s"(desc), "s"(soff) : "memory");
  return r;
}
DI void ld128d(u32x4& r, u32x4 desc, int voff, int soff) {
  asm volatile("buffer_load_dwordx4 %0, %1, %2, %3 offen sc1" : "=v"(r) : "v"(voff), "s"(desc), "s"(soff) : "memory");
}
DI void ld64ds(u32x2& r, u32x4 desc, int voff, int soff) {
  asm volatile("buffer_load_dwordx2 %0, %1, %2, %3 offen sc1" : "=v"(r) : "v"(voff), "s"(desc), "s"(soff) : "memory");
}
DI void ld64p(u32x2& r, u32x4 desc, int voff, int soff) {
  asm volatile("buffer_load_dwordx2 %0, %1, %2, %3 offen" : "=v"(r) : "v"(voff), "s"(desc), "s"(soff) : "memory");
}
DI void ldu16(unsigned& r, u32x4 desc, int voff, int soff) {
  asm volatile("buffer_load_ushort %0, %1, %2, %3 offen" : "=v"(r) : "v"(voff), "s"(desc), "s"(soff) : "memory");
}
DI void st128d(u32x4 v, u32x4 desc, int voff, int soff) {
  asm volatile("buffer_store_dwordx4 %0, %1, %2, %3 offen sc1\n\ts_nop 1" :: "v"(v), "v"(voff), "s"(desc), "s"(soff) : "memory");
}
DI void gld32(unsigned& r, const char* p) { asm volatile("global_load_dword %0, %1, off" : "=&v"(r) : "v"(p) : "memory"); }
DI void hold(unsigned v) { asm volatile("" :: "v"(v)); }
DI void pin(u32x4& v) { asm volatile("" : "+v"(v)); }
DI void pin(u32x2& v) { asm volatile("" : "+v"(v)); }
DI void pin(unsigned& v) { asm volatile("" : "+v"(v)); }
DI s16x4 trread(const char* p) { return __builtin_amdgcn_ds_read_tr16_b64_v4i16((LAS s16x4*)p); }
DI bf16x8 cat8(s16x4 a, s16x4 b) {
  s16x8 r = __builtin_shufflevector(a, b, 0, 1, 2, 3, 4, 5, 6, 7);
  return __builtin_bit_cast(bf16x8, r);
}
DI bf16x8 asb8(u32x4 v) { return __builtin_bit_cast(bf16x8, v); }
DI f32x4 mfma32(bf16x8 a, bf16x8 b, f32x4 c) { return __builtin_amdgcn_mfma_f32_16x16x32_bf16(a, b, c, 0, 0, 0); }
DI f32x4 mfma16(u32x2 a, u32x2 b, f32x4 c) {
  return __builtin_amdgcn_mfma_f32_16x16x16bf16_1k(__builtin_bit_cast(s16x4, a), __builtin_bit_cast(s16x4, b), c, 0, 0, 0);
}
template <int CTRL> DI float dppadd(float v) { return v + asf(__builtin_amdgcn_update_dpp(0u, asu(v), CTRL, 0xf, 0xf, true)); }
DI unsigned pk2(float a, float b) {
  f32x2 f = {a, b};
  return __builtin_bit_cast(unsigned, __builtin_convertvector(f, bf16x2));
}
DI u32x2 pk4(f32x4 v) { return u32x2{pk2(v[0], v[1]), pk2(v[2], v[3])}; }
DI float bflo(unsigned u) { return asf(u << 16); }
DI float bfhi(unsigned u) { return asf(u & 0xffff0000u); }
DI float dot2(unsigned a, unsigned b, float c) {
  return __builtin_amdgcn_fdot2_f32_bf16(__builtin_bit_cast(bf16x2, a), __builtin_bit_cast(bf16x2, b), c, false);
}
DI float fmx(float a, float b) { return __builtin_elementwise_maximum(a, b); }
DI float fmn(float a, float b) { return __builtin_elementwise_minimum(a, b); }
template <bool MAX> DI float rop(unsigned a, unsigned b) { return MAX ? fmx(asf(a), asf(b)) : asf(a) + asf(b); }
template <bool MAX> DI void redg2(float& a, float& b) {
  auto r = __builtin_amdgcn_permlane16_swap(asu(a), asu(b), false, false);
  float m = rop<MAX>(r[0], r[1]);
  auto r2 = __builtin_amdgcn_permlane32_swap(asu(m), asu(m), false, false);
  m = rop<MAX>(r2[0], r2[1]);
  auto r3 = __builtin_amdgcn_permlane16_swap(asu(m), asu(m), false, false);
  a = asf(r3[0]); b = asf(r3[1]);
}
template <bool MAX> DI float redg4(const float (&v)[4]) {
  auto r = __builtin_amdgcn_permlane16_swap(asu(v[0]), asu(v[1]), false, false);
  auto q = __builtin_amdgcn_permlane16_swap(asu(v[2]), asu(v[3]), false, false);
  auto t = __builtin_amdgcn_permlane32_swap(asu(rop<MAX>(r[0], r[1])), asu(rop<MAX>(q[0], q[1])), false, false);
  return rop<MAX>(t[0], t[1]);
}
DI void bcast4(float m, float (&v)[4]) {
  auto t = __builtin_amdgcn_permlane32_swap(asu(m), asu(m), false, false);
  unsigned t0 = t[0], t1 = t[1];
  auto r = __builtin_amdgcn_permlane16_swap(t0, t0, false, false);
  auto q = __builtin_amdgcn_permlane16_swap(t1, t1, false, false);
  v[0] = asf(r[0]); v[1] = asf(r[1]); v[2] = asf(q[0]); v[3] = asf(q[1]);
}
constexpr float L2E = 1.4426950408889634f;
DI f32x4 exp4(f32x4 v) {
  return f32x4{__builtin_amdgcn_exp2f(v[0]), __builtin_amdgcn_exp2f(v[1]), __builtin_amdgcn_exp2f(v[2]), __builtin_amdgcn_exp2f(v[3])};
}
DI float sum4(f32x4 v) { f32x2 t = v.xy + v.zw; return t[0] + t[1]; }
template <class F> DI void hedgehog4(const f32x4 (&p)[4], u32x4 (&o)[4], F&& bg) {
  float mx[4], mn[4];
#pragma unroll
  for (int i = 0; i < 4; ++i) {
    mx[i] = fmx(fmx(p[i][0], p[i][1]), fmx(p[i][2], p[i][3]));
    mn[i] = -fmn(fmn(p[i][0], p[i][1]), fmn(p[i][2], p[i][3]));
  }
  bg(IC<0>{});
  bcast4(redg4<true>(mx), mx);
  bcast4(redg4<true>(mn), mn);
  asm volatile("" : "+v"(mx[0]), "+v"(mx[1]), "+v"(mx[2]), "+v"(mx[3]), "+v"(mn[0]), "+v"(mn[1]), "+v"(mn[2]), "+v"(mn[3]));
  bg(IC<1>{});
  f32x4 ep[4], en[4];
  float sp[4], sn[4];
  sfor<4>([&](auto I) {
    constexpr int i = I;
    ep[i] = exp4(p[i] - mx[i]);
    en[i] = exp4(-p[i] - mn[i]);
    sp[i] = sum4(ep[i]);
    sn[i] = sum4(en[i]);
    asm volatile("" : "+v"(sp[i]), "+v"(sn[i]));
    bg(IC<2 + i>{});
  });
  bcast4(__builtin_amdgcn_rcpf(redg4<false>(sp)), sp);
  bcast4(__builtin_amdgcn_rcpf(redg4<false>(sn)), sn);
  asm volatile("" : "+v"(sp[0]), "+v"(sp[1]), "+v"(sp[2]), "+v"(sp[3]), "+v"(sn[0]), "+v"(sn[1]), "+v"(sn[2]), "+v"(sn[3]));
  bg(IC<6>{});
#pragma unroll
  for (int i = 0; i < 4; ++i) {
    const u32x2 a = pk4(ep[i] * sp[i]), b = pk4(en[i] * sn[i]);
    o[i] = u32x4{a[0], a[1], b[0], b[1]};
    pin(o[i]);
  }
  bg(IC<7>{});
}
template <int N> DI void gelu16(f32x4 (&acc)[4][3], const float (&k1)[4], const float (&k2)[4], const float (&irs)[4]) {
  f32x4 t[4];
#pragma unroll
  for (int m = 0; m < 4; ++m) {
    const f32x4 a = acc[m][N], K1 = {k1[m], k1[m], k1[m], k1[m]}, K2 = {k2[m], k2[m], k2[m], k2[m]};
    t[m] = a * __builtin_elementwise_fma(a * a, K2, K1);
  }
#pragma unroll
  for (int m = 0; m < 4; ++m) t[m] = exp4(t[m]);
#pragma unroll
  for (int m = 0; m < 4; ++m) { const f32x4 I = {irs[m], irs[m], irs[m], irs[m]}; t[m] = __builtin_elementwise_fma(t[m], I, I); }
#pragma unroll
  for (int m = 0; m < 4; ++m)
    acc[m][N] *= f32x4{__builtin_amdgcn_rcpf(t[m][0]), __builtin_amdgcn_rcpf(t[m][1]), __builtin_amdgcn_rcpf(t[m][2]), __builtin_amdgcn_rcpf(t[m][3])};
  asm volatile("" : "+v"(acc[0][N]), "+v"(acc[1][N]), "+v"(acc[2][N]), "+v"(acc[3][N]));
}
constexpr int DROP = 0x7f000000;
#define WAITV(n) asm volatile("s_waitcnt vmcnt(" #n ")" ::: "memory")
#define WAITL0() asm volatile("s_waitcnt lgkmcnt(0)" ::: "memory")
#define SB() __builtin_amdgcn_sched_barrier(0)
#define BAR() do { SB(); __builtin_amdgcn_s_barrier(); SB(); } while (0)
#define LBAR() do { WAITL0(); BAR(); } while (0)
constexpr int PSLEEP = 4, AUD = 2;
constexpr int SPIN_CAP = 1 << 18;
constexpr unsigned TAGX = 0x5bd1e995u;
constexpr unsigned EMPTY = 0xffffffffu;

constexpr int L_SLOT = 40960;
constexpr int L_CB = 32768;
constexpr int L_U = 98304;
constexpr int L_ZS = L_U + 6144;
constexpr int L_RING = 24576;
constexpr int LDS_BYTES = 163840;
constexpr int CODE_LINES = 300;
constexpr int p2P(int i) { return i + 4 < 16 ? 3 : 0; }
constexpr int p2E(int i) { return i < 0 ? 0 : (i == 2 ? 2 : 0) + (i == 9 ? 1 : 0) + (i == 12 ? 3 : 0) + (i == 13 ? 7 : 0); }
constexpr int p2cnt(int st) { return p2E(st - 3) + p2P(st - 2) + p2E(st - 2) + p2P(st - 1) + p2E(st - 1); }

__global__ __launch_bounds__(512, 2) void fused(const char* __restrict__ x, const char* __restrict__ wqkv,
                                                const char* __restrict__ wphi, const char* __restrict__ wo,
                                                const char* __restrict__ win, const char* __restrict__ wsp,
                                                const char* __restrict__ bsp, const char* __restrict__ wout,
                                                char* out, char* ws) {
  extern __shared__ __attribute__((aligned(16))) char smem[];
  unsigned pfc;
  {
    const unsigned long long pc = __builtin_amdgcn_s_getpc() & ~127ull;
    const unsigned li = min((blockIdx.x >> 3) * 10u + min(threadIdx.x, 9u), (unsigned)CODE_LINES - 1u);
    asm volatile("global_load_dword %0, %1, %2" : "=&v"(pfc) : "v"(li * 128u), "s"(pc) : "memory");
    SB();
  }
  const int tid = threadIdx.x, lane = tid & 63;
  const int w = __builtin_amdgcn_readfirstlane(tid >> 6);
  const int r16 = lane & 15, g = lane >> 4;
  const int tq = r16 >> 2, tp = r16 & 3;
  const int bid = blockIdx.x, xcd = bid & 7, jj = bid >> 3;
  const int rt16 = (xcd >> 2) * 8 + (jj >> 2);
  const bool isqkv = (jj & 2) == 0;
  const int grp = (xcd & 3) * 2 + (jj & 1);
  const int cg = isqkv ? grp : 8 + grp;
  const int zft0 = grp < 7 ? grp * 6 : 40;
  const int znft = grp < 6 ? 6 : (grp == 6 ? 4 : 3);
  const int wm = w >> 2, wn = (w & 3) ^ wm;
  const int rot1 = (rt16 & 3) * 2;
  const int rt64 = xcd * 4 + (jj >> 3);
  const int cgp = jj & 7;
  const int ft0 = cgp < 7 ? cgp * 6 : 40;
  const int npair = cgp < 6 ? 3 : 2;
  const int th = w & 1, kq = w >> 1;
  const int rot2 = cgp;

  int xsrc[2], wsrc[3];
#pragma unroll
  for (int u = 0; u < 2; ++u) {
    int row = 16 * w + 8 * u + (lane >> 3);
    xsrc[u] = (rt16 * 128 + row) * 1024 + (((lane & 7) ^ ((row >> 1) & 7)) << 4);
  }
  const int wstride = isqkv ? 64 * 3072 : 64 * 2752;
#pragma unroll
  for (int u = 0; u < 3; ++u) {
    int ci = u * 64 + lane;
    int rl = (ci * 2731) >> 16, c = ci - rl * 24;
    int h2 = ((rl >> 1) & 1) | ((w & 1) << 1);
    int cs = c ^ (h2 << 1);
    int vq = (8 * w + rl) * 3072 + (cs >> 3) * 1024 + grp * 128 + (cs & 7) * 16;
    int cz = min(cs < 12 ? cs : cs - 12, znft * 2 - 1);
    int vz = (8 * w + rl) * 2752 + (cs < 12 ? 1376 : 0) + zft0 * 32 + cz * 16;
    wsrc[u] = isqkv ? vq : vz;
  }
  asm volatile("" : "+v"(xsrc[0]), "+v"(xsrc[1]), "+v"(wsrc[0]), "+v"(wsrc[1]), "+v"(wsrc[2]));
  asm volatile("" : "+s"(x), "+s"(wqkv), "+s"(wphi), "+s"(wo), "+s"(win), "+s"(wsp), "+s"(bsp), "+s"(wout), "+s"(out), "+s"(ws));
  const __amdgpu_buffer_rsrc_t wsr = __builtin_amdgcn_make_buffer_rsrc(ws, 0, WS_END, 0x00020000);
  const u32x4 wsd = mkdesc(ws, WS_END);
  const u32x4 xd = mkdesc(x, 2048u * 1024u);
  const u32x4 wd = isqkv ? mkdesc(wqkv, 512u * 3072u) : mkdesc(win, 512u * 2752u);
  const u32x4 ad = mkdesc(wsp, 2048u * 4096u);
  const f32x4 zero4 = {0.f, 0.f, 0.f, 0.f};
  const unsigned ONES = 0x3f803f80u;
  const bf16x8 ones8 = asb8(u32x4{ONES, ONES, ONES, ONES});
  auto dma1 = [&](int st, int u) {
    const int slot = (st & 3) * L_SLOT, sr = (st + rot1) & 7;
    if (u < 2) dma16(xd, xsrc[u], sr * 128, slot + (16 * w + 8 * u) * 128);
    else dma16(wd, wsrc[u - 2], sr * wstride, slot + 16384 + (3 * w + u - 2) * 1024);
  };
#pragma unroll
  for (int u = 0; u < 5; ++u) dma1(0, u);
  unsigned ep;
  u32x4 xrow;
  asm volatile("buffer_load_dword %0, off, %2, 0 sc1\n\tbuffer_load_dwordx4 %1, %3, %4, 0 offen"
               : "=&v"(ep), "=&v"(xrow) : "s"(wsd), "v"((bid * 8 + w) * 1024 + lane * 16), "s"(xd) : "memory");
#pragma unroll
  for (int u = 0; u < 5; ++u) dma1(1, u);
  u32x2 wrow;
  ld64p(wrow, mkdesc(wphi, 32u * 512u), r16 * 32 + g * 8, (grp * 4 + wn) * 512);

  const int asrc = (rt64 * 64 + th * 32 + (lane >> 3)) * 4096 + kq * 128 + (((lane & 7) ^ ((lane >> 4) & 3)) << 4);
  auto dmaA = [&](int c, int u) {
    dma16(ad, asrc ^ ((u & 1) << 6), ((c + rot2) & 7) * 512 + u * 8 * 4096, (c % 3) * L_CB + w * 4096 + u * 1024);
  };
  auto piece = [&](int p) {
    if (p < 30) dma1(2 + p / 5, p % 5);
    else if (p < 42) dmaA((p - 30) >> 2, (p - 30) & 3);
  };
  const bool zeven = (wn & 1) == 0;
  const int hb = 3 * (wn >> 1);
  int li[3];
  li[0] = zeven ? hb : hb + 2; li[1] = hb + 1; li[2] = zeven ? hb : hb + 2;
  int aoff[2], boff[3], sgi[3];
  sgi[0] = isqkv ? wn : li[0];
  sgi[1] = isqkv ? 4 + wn : (zeven ? li[1] : 6 + li[1]);
  sgi[2] = isqkv ? 8 + wn : 6 + li[2];
#pragma unroll
  for (int ks = 0; ks < 2; ++ks) aoff[ks] = (wm * 64 + r16) * 128 + (((4 * ks + g) ^ ((r16 >> 1) & 7)) << 4);
  {
    int h2 = ((tq >> 1) & 1) | ((g & 1) << 1);
#pragma unroll
    for (int i = 0; i < 3; ++i) boff[i] = 16384 + (8 * g + tq) * 384 + (((2 * sgi[i] + (tp >> 1)) ^ (h2 << 1)) << 4) + (tp & 1) * 8;
  }
  f32x4 acc[4][3], csum[3];
#pragma unroll
  for (int i = 0; i < 3; ++i) { acc[0][i] = zero4; acc[1][i] = zero4; acc[2][i] = zero4; acc[3][i] = zero4; csum[i] = zero4; }
  bf16x8 fa[2][4], fb[2][3];
  u32x4 rst, cst;
  unsigned tag = 0, pf;
  float st_s = 0.f, st_q = 0.f;
  float rs[4], ka[4], kb[4], mh[4], ml[4];
  const bool csprod = rt16 < 4 && wm == 0;
  auto ld_stats = [&]() {
    ld128d(rst, wsd, lane * 16, WS_RST + (rt16 * 128 + wm * 64) * 16);
    ld128d(cst, wsd, lane * 16, WS_CS + (cg * 4 + wn) * 1024);
  };
  auto rdA = [&](int st, int ks, int m) { fa[ks][m] = *(const bf16x8*)(smem + (st & 3) * L_SLOT + aoff[ks] + m * 2048); };
  auto rdB = [&](int st, int ks, int n) {
    const char* p = smem + (st & 3) * L_SLOT + boff[n] + ks * 12288;
    fb[ks][n] = cat8(trread(p), trread(p + 1536));
  };
  waitv<8>();
  hold(pfc);
  BAR();
  rdB(0, 0, 0); rdA(0, 0, 0); rdA(0, 0, 1); rdA(0, 0, 2); rdA(0, 0, 3); rdB(0, 0, 1); rdB(0, 0, 2);
  sfor<16>([&](auto B) {
    constexpr int b = B, i = b >> 1, ks = b & 1;
    constexpr int nst = (b + 1) >> 1, nks = ks ^ 1;
    constexpr int p0 = b < 10 ? 3 * b : 20 + b, p1 = b < 10 ? 3 * b + 3 : (b < 14 ? 21 + b : 0);
    sfor<6>([&](auto N) {
      constexpr int p = N, n = p >> 1, m = (p & 1) * 2;
      SB();
      acc[m][n] = mfma32(fb[ks][n], fa[ks][m], acc[m][n]);
      acc[m + 1][n] = mfma32(fb[ks][n], fa[ks][m + 1], acc[m + 1][n]);
      SB();
      if constexpr (nst < 8) {
        if constexpr (p < 3) rdB(nst, nks, p);
        if constexpr (p < 4) rdA(nst, nks, p);
      }
      if constexpr (p >= 1 && p0 + p - 1 < p1) piece(p0 + p - 1);
      if constexpr (b == 8 && p == 4) ld_stats();
      if constexpr (b == 10 && (p == 2 || p == 3))
        st128d(u32x4{EMPTY, EMPTY, EMPTY, EMPTY}, wsd, (p == 2 || kq + 4 < 2 * npair) ? lane * 16 : DROP, WS_A3F + ((rt64 * 2 + th) * 76 + 32 + ft0 + kq + 4 * (p - 2)) * 1024);
      if constexpr (b == 10 && p == 4) {
        const int li = bid * 40 + w * 5 + min(lane, 4);
        gld32(pf, lane > 4 ? bsp + rt64 * 128 : (li < 4096 ? wo + li * 128 : wout + min(li - 4096, 5503) * 128));
      }
      if constexpr (b == 13) {
        if constexpr (p == 0) {
          pin(rst); pin(cst); pin(wrow);
#pragma nounroll
          for (int spin = 0; spin < SPIN_CAP; ++spin) {
            if (__builtin_expect(__all(rst[1] == tag && rst[3] == tag && (cst[0] ^ cst[1] ^ cst[2] ^ cst[3]) == (tag ^ TAGX)), 1)) break;
            __builtin_amdgcn_s_sleep(PSLEEP);
            ld_stats();
            WAITV(0);
            pin(rst); pin(cst);
          }
        }
        if constexpr (p == 1) bcast4(asf(pk2(-asf(rst[2]), -asf(rst[2]))), mh);
        if constexpr (p == 2) bcast4(asf(pk2(-asf(rst[2]) - bflo(pk2(-asf(rst[2]), 0.f)), 0.f)), ml);
        if constexpr (p == 3) bcast4(isqkv ? asf(rst[0]) : __builtin_amdgcn_rcpf(asf(rst[0])), rs);
        if constexpr (p == 4) bcast4(asf(rst[0]) * (isqkv ? L2E : -2.0f * 0.7978845608028654f * L2E), ka);
        if constexpr (p == 5) bcast4(asf(rst[0]) * asf(rst[0]) * asf(rst[0]) * (-2.0f * 0.7978845608028654f * L2E * 0.044715f), kb);
      }
      if constexpr (b == 14) {
        constexpr int q = (p + 3) % 6, fn = q >> 1, fm = (q & 1) * 2;
        const bf16x8 co = asb8(u32x4{cst[fn], cst[fn], 0u, 0u});
        acc[fm][fn] = mfma32(co, asb8(u32x4{asu(mh[fm]), asu(ml[fm]), 0u, 0u}), acc[fm][fn]);
        acc[fm + 1][fn] = mfma32(co, asb8(u32x4{asu(mh[fm + 1]), asu(ml[fm + 1]), 0u, 0u}), acc[fm + 1][fn]);
      }
      if constexpr (b == 1) {
        if constexpr (p == 0) {
          pin(ep); pin(xrow);
          tag = __builtin_amdgcn_readfirstlane(ep) + 1u;
#pragma unroll
          for (int e = 0; e < 4; ++e) { st_s = dot2(xrow[e], ONES, st_s); st_q = dot2(xrow[e], xrow[e], st_q); }
        }
        if constexpr (p == 1) redg2<false>(st_s, st_q);
        if constexpr (p == 2) { st_s = dppadd<0x128>(st_s); st_q = dppadd<0x128>(st_q); st_s = dppadd<0x124>(st_s); st_q = dppadd<0x124>(st_q); }
        if constexpr (p == 3) { st_s = dppadd<0x122>(st_s); st_q = dppadd<0x122>(st_q); st_s = dppadd<0x121>(st_s); st_q = dppadd<0x121>(st_q); }
        if constexpr (p == 4) {
          float mean = st_s * (1.0f / 512.0f);
          float var = fmx(st_q * (1.0f / 512.0f) - mean * mean, 0.f);
          float rs = __builtin_amdgcn_rsqf(var + 1e-5f);
          if (lane == 0)
            st128d(u32x4{asu(rs), tag, asu(mean), tag}, wsd, WS_RST + (bid * 8 + w) * 16, 0);
        }
      }
    });
    if constexpr (b < 4) {
      if (csprod) {
#pragma unroll
        for (int n = 0; n < 3; ++n) csum[n] = mfma32(ones8, fb[ks][n], csum[n]);
        if constexpr (b == 3) {
          u32x4 o;
#pragma unroll
          for (int n = 0; n < 3; ++n) o[n] = pk2(csum[n][0], csum[n][0] - bflo(pk2(csum[n][0], 0.f)));
          o[3] = tag ^ TAGX ^ o[0] ^ o[1] ^ o[2];
          if (g == 0) st128d(o, wsd, r16 * 16, WS_CS + (cg * 4 + wn) * 1024 + rot1 * 128);
        }
      }
    }
    if constexpr (ks == 0 && i < 7) {
      WAITL0();
      waitv<(i == 0 ? 4 : (i < 4 ? i + 3 : (i == 4 ? 9 : (i == 5 ? 11 : 6))))>();
      BAR();
    }
  });
  bf16x8 af[8][2][2];
  const int aro = w * 4096 + r16 * 128;
  auto wsp_ld = [&](int c) {
#pragma unroll
    for (int e = 0; e < 2; ++e)
#pragma unroll
      for (int t = 0; t < 2; ++t)
        af[c][e][t] = *(const bf16x8*)(smem + (c % 3) * L_CB + aro + t * 2048 + (((4 * e + g) ^ ((r16 >> 1) & 7)) << 4));
    SB();
  };
  auto wsp_rd = [&](int c) {
    SB();
    if (c == 1) hold(pf);
    if (c < 6) { WAITV(8); } else if (c == 6) { WAITV(4); } else { WAITV(0); }
    wsp_ld(c);
  };
  auto wsp_is = [&](int c, int u0, int u1) {
    SB();
    if (u0 == 0) WAITL0();
    for (int u = u0; u < u1; ++u) dmaA(c, u);
    SB();
  };
  char* scr = smem + L_U + w * 8192;
  u32x4 pq[4];
  if (isqkv) {
    f32x4 lg[4];
    u32x4 pk[4];
    const unsigned one = (r16 & 1) ? 0x3f800000u : 0x3f80u;
    const u32x2 wph = pk4(mfma16(wrow, u32x2{(tq == g && tp < 2) ? one : 0u, (tq == g && tp >= 2) ? one : 0u}, zero4));
#pragma unroll
    for (int m = 0; m < 4; ++m) lg[m] = mfma16(wph, pk4(acc[m][1] * ka[m]), zero4);
    hedgehog4(lg, pk, [&](auto I) {
      constexpr int i = I;
      if constexpr (i >= 1 && i <= 4) wsp_is(1 + ((i - 1) >> 1), ((i - 1) & 1) * 2, ((i - 1) & 1) * 2 + 2);
    });
    f32x4 kv[2] = {zero4, zero4}, kz[2] = {zero4, zero4};
    char* sc3 = smem + L_U + w * 3072;
#pragma unroll
    for (int mp = 0; mp < 2; ++mp) {
#pragma unroll
      for (int m = 0; m < 2; ++m) {
        *(u32x4*)(sc3 + (16 * m + r16) * 64 + g * 16) = pk[2 * mp + m];
        *(u32x2*)(sc3 + 2048 + (16 * m + r16) * 32 + g * 8) = pk4(acc[2 * mp + m][2] * rs[2 * mp + m]);
      }
      const int row = 8 * g + tq;
      const char* pp = sc3 + row * 64 + tp * 16;
      const char* pv = sc3 + 2048 + row * 32 + tp * 8;
      bf16x8 ap = cat8(trread(pp), trread(pp + 256));
      bf16x8 an = cat8(trread(pp + 8), trread(pp + 264));
      bf16x8 bv = cat8(trread(pv), trread(pv + 128));
      kv[0] = mfma32(ap, bv, kv[0]);
      kv[1] = mfma32(an, bv, kv[1]);
      kz[0] = mfma32(ap, ones8, kz[0]);
      kz[1] = mfma32(an, ones8, kz[1]);
    }
#pragma unroll
    for (int m = 0; m < 4; ++m) lg[m] = mfma16(wph, pk4(acc[m][0] * ka[m]), zero4);
    *(f32x4*)(sc3 + lane * 16) = wm ? kv[0] : kv[1];
    if (r16 == 0) *(f32x4*)(sc3 + 1024 + g * 16) = wm ? kz[0] : kz[1];
    LBAR();
    {
      const char* p = smem + L_U + (w ^ 5) * 3072;
      const f32x4 a = (wm ? kv[1] : kv[0]) + *(const f32x4*)(p + lane * 16);
      const f32x4 b = (wm ? kz[1] : kz[0]) + *(const f32x4*)(p + 1024 + g * 16);
      const int base = WS_KVP + (grp * 16 + rt16) * KVP_SZ;
      const int hs = wn * 2 + wm;
      st128d(u32x4{asu(a[0]), tag, asu(a[1]), tag}, wsd, lane * 16, base + hs * 2048);
      st128d(u32x4{asu(a[2]), tag, asu(a[3]), tag}, wsd, lane * 16, base + hs * 2048 + 1024);
      const float v0 = r16 ? b[2] : b[0], v1 = r16 ? b[3] : b[1];
      st128d(u32x4{asu(v0), tag, asu(v1), tag}, wsd, r16 < 2 ? g * 32 + r16 * 16 : DROP, base + 16384 + hs * 128);
    }
    u32x4 a, b;
    const int l1off = (lane >> 3) * KVP_SZ + (lane & 7) * 16;
    auto ld_l1 = [&](int sub) {
      ld128d(a, wsd, l1off, WS_KVP + grp * 16 * KVP_SZ + sub * 128);
      ld128d(b, wsd, l1off, WS_KVP + (grp * 16 + 8) * KVP_SZ + sub * 128);
    };
    hedgehog4(lg, pq, [&](auto I) {
      constexpr int i = I;
      if constexpr (i == 0) { SB(); WAITV(3); hold(pf); wsp_ld(0); wsp_ld(1); wsp_ld(2); }
      if constexpr (i >= 1 && i <= 6) wsp_is(3 + ((i - 1) >> 1), ((i - 1) & 1) * 2, ((i - 1) & 1) * 2 + 2);
      if constexpr (i == 7) ld_l1(rt16 * 8 + w);
    });
    SB(); WAITV(10); wsp_ld(3);
    wsp_is(6, 0, 4);
    SB(); WAITV(10); wsp_ld(4);
    wsp_is(7, 0, 4);
    WAITV(8);
#pragma nounroll
    for (int rep = 0; rep < ((w == 0 && rt16 < 8) ? 2 : 1); ++rep) {
      const int sub = rep ? 128 + rt16 : rt16 * 8 + w;
#pragma nounroll
      for (int spin = rep ? -1 : 0; spin < SPIN_CAP; ++spin) {
        if (spin >= 0) {
          pin(a); pin(b);
          if (__builtin_expect(__all(a[1] == tag && a[3] == tag && b[1] == tag && b[3] == tag), 1)) break;
          __builtin_amdgcn_s_sleep(8);
        }
        ld_l1(sub);
        WAITV(0);
      }
      float v0 = asf(a[0]) + asf(b[0]), v1 = asf(a[2]) + asf(b[2]);
      v0 = dppadd<0x128>(v0); v1 = dppadd<0x128>(v1);
      redg2<false>(v0, v1);
      const int dsto = rep ? (sub * 8 + lane) * 8 : ((sub >> 4) * 64 + (sub & 7) * 8 + lane) * 16 + ((sub >> 3) & 1) * 8;
      __builtin_amdgcn_raw_buffer_store_b64(u32x2{pk2(v0, v1), tag}, wsr, lane < 8 ? dsto : DROP, WS_KVR + grp * 1088 * 8, 16);
    }
  } else {
    const bool v0 = li[0] < znft, v1 = li[1] < znft;
    gelu16<0>(acc, ka, kb, rs);

    {
      f32x4 s4[4], q4[4];
#pragma unroll
      for (int m = 0; m < 4; ++m) { s4[m] = v0 ? acc[m][0] : zero4; q4[m] = s4[m] * s4[m]; }
      if (zeven) {
        gelu16<1>(acc, ka, kb, rs);
#pragma unroll
        for (int m = 0; m < 4; ++m) {
          const f32x4 y = v1 ? acc[m][1] : zero4;
          s4[m] += y; q4[m] = __builtin_elementwise_fma(y, y, q4[m]);
        }
      }
      float s[4], q[4];
#pragma unroll
      for (int m = 0; m < 4; ++m) { s[m] = sum4(s4[m]); q[m] = sum4(q4[m]); }
      *(f32x2*)(smem + L_ZS + (wn >> 1) * 8192 + (wn & 1) * 1024 + (wm * 64 + lane) * 8) = f32x2{redg4<false>(s), redg4<false>(q)};
    }

    LBAR();
    if (tid < 128) {
      f32x2 a = *(const f32x2*)(smem + L_ZS + tid * 8) + *(const f32x2*)(smem + L_ZS + 1024 + tid * 8);
      f32x2 b = *(const f32x2*)(smem + L_ZS + 8192 + tid * 8) + *(const f32x2*)(smem + L_ZS + 9216 + tid * 8);
      st128d(u32x4{asu(a[0] + b[0]), tag, asu(a[1] + b[1]), tag}, wsd,
                                             WS_ZST + ((rt16 * 8 + grp) * 128 + tid) * 16, 0);
    }
    wsp_is(1, 0, 4);
    gelu16<2>(acc, ka, kb, rs);
    u32x4 zs[2];
    const int zso = WS_ZST + (rt16 * 8 + w) * 2048;
    auto ld_zs = [&]() { ld128d(zs[0], wsd, lane * 16, zso); ld128d(zs[1], wsd, lane * 16, zso + 1024); };
    ld_zs();
    wsp_is(2, 0, 4);
    WAITV(4);
    hold(pf);
    wsp_ld(0); wsp_ld(1);
    wsp_is(3, 0, 4);
#pragma nounroll
    for (int spin = 0; spin < SPIN_CAP; ++spin) {
      pin(zs[0]); pin(zs[1]);
      if (__all(zs[0][1] == tag && zs[0][3] == tag && zs[1][1] == tag && zs[1][3] == tag)) break;
      __builtin_amdgcn_s_sleep(2);
      ld_zs();
      WAITV(0);
    }
    *(f32x2*)(scr + 4096 + lane * 8) = f32x2{asf(zs[0][0]), asf(zs[0][2])};
    *(f32x2*)(scr + 4608 + lane * 8) = f32x2{asf(zs[1][0]), asf(zs[1][2])};
    LBAR();
    {
      f32x2 sq = {0.f, 0.f};
#pragma unroll
      for (int j = 0; j < 8; ++j) sq += *(const f32x2*)(smem + L_U + j * 8192 + 4096 + (wm * 64 + lane) * 8);
      const float mean = sq[0] * (1.0f / 688.0f);
      const float var = fmx(sq[1] * (1.0f / 688.0f) - mean * mean, 0.f);
      const float r = __builtin_amdgcn_rsqf(var + 1e-5f);
      float zr[4], zc[4];
      bcast4(r, zr);
      bcast4(-mean * r, zc);
      const int dst = WS_Z2F + (((rt16 >> 1) * 2 * 44 + zft0) * 4 + ((2 * rt16 + wm) & 3)) * 1024;
#pragma unroll
      for (int n = 0; n < 2; ++n) {
#pragma unroll
        for (int m = 0; m < 4; ++m) *(u32x2*)(scr + n * 2048 + (16 * m + r16) * 32 + g * 8) = pk4(acc[m][n] * zr[m] + zc[m]);
        const int dn = dst + li[n] * 4096, vo = (n == 0 ? v0 : (v1 && zeven)) ? lane * 16 : DROP;
#pragma unroll
        for (int kk = 0; kk < 2; ++kk) {
          const char* p = scr + n * 2048 + (32 * kk + 8 * g + tq) * 32 + tp * 8;
          bf16x8 f = cat8(trread(p), trread(p + 128));
          st128d(__builtin_bit_cast(u32x4, f), wsd, vo, dn + kk * 44 * 4096);
        }
      }
    }
    if (!zeven) gelu16<1>(acc, ka, kb, rs);
    WAITV(0);
    BAR();
    if (tid == 0) __builtin_amdgcn_raw_buffer_store_b64(u32x2{tag, tag ^ TAGX}, wsr, WS_FLZ + grp * 1024 + rt16 * 8, 0, 16);
    wsp_is(4, 0, 4);
    wsp_ld(2); wsp_ld(3);
    wsp_is(5, 0, 4);
    wsp_is(6, 0, 4);
    wsp_rd(4);
    wsp_is(7, 0, 4);
#pragma unroll
    for (int mp = 0; mp < 2; ++mp) {
      const u32x2 a = pk4(acc[2 * mp][2]), b = pk4(acc[2 * mp + 1][2]), c = pk4(acc[2 * mp][1]), d = pk4(acc[2 * mp + 1][1]);
      pq[mp] = u32x4{a[0], a[1], b[0], b[1]};
      pq[2 + mp] = u32x4{c[0], c[1], d[0], d[1]};
    }
  }
  unsigned biasv[2];
#pragma unroll
  for (int t = 0; t < 2; ++t) ldu16(biasv[t], mkdesc(bsp, 4096u), r16 * 2, (rt64 * 64 + th * 32 + t * 16) * 2);
  wsp_rd(5); wsp_rd(6); wsp_rd(7);
  pin(biasv[0]); pin(biasv[1]);
  if (!isqkv) {
    const int z1o = WS_Z1F + ((rt16 * 4 + wm * 2) * 44 + zft0) * 1024;
    const int vo2 = li[2] < znft ? lane * 16 : DROP, vo1 = (li[1] < znft && !zeven) ? lane * 16 : DROP;
#pragma unroll
    for (int mp = 0; mp < 2; ++mp) {
      st128d(pq[mp], wsd, vo2, z1o + (mp * 44 + li[2]) * 1024);
      st128d(pq[2 + mp], wsd, vo1, z1o + (mp * 44 + li[1]) * 1024);
    }
  }
  int lz = lane;
  asm volatile("" : "+v"(lz));
  const int l16 = lz * 16;
  auto poll_z = [&]() -> unsigned {
    u32x2 f = poll64(wsd, ((l16 >> 1) + 16 * rot2) & 127, WS_FLZ + cgp * 1024);
    return (unsigned)__ballot(f[0] == tag && f[1] == (tag ^ TAGX)) & 0xffffu;
  };
  const int w3off = (lz >> 3) * 1024 + cgp * 128 + (((lz & 7) ^ ((lz >> 4) << 1)) << 4);
  const u32x4 od = mkdesc(wo, 512u * 1024u);
  if (isqkv) {
    u32x4 kr[4];
    auto ld_kr = [&]() {
#pragma unroll
      for (int j = 0; j < 2; ++j) {
        ld128d(kr[j], wsd, l16, WS_KVR + (grp * 1088 + wn * 256) * 8 + j * 1024);
        ld128d(kr[2 + j], wsd, (l16 >> 4) & 48, WS_KVR + (grp * 1088 + 1024 + wn * 16) * 8 + j * 64);
      }
    };
    BAR();
#pragma unroll
    for (int i = 0; i < 8; ++i) dma16(od, w3off, (i * 8 + w) * 8192, L_U + (i * 8 + w) * 1024);
#pragma nounroll
    for (int spin = 0; spin < SPIN_CAP; ++spin) {
      ld_kr();
      WAITV(0);
      bool ok = true;
#pragma unroll
      for (int j = 0; j < 4; ++j) { pin(kr[j]); ok = ok && kr[j][1] == tag && kr[j][3] == tag; }
      if (__builtin_expect(__all(ok), 1)) break;
      __builtin_amdgcn_s_sleep(8);
    }
    const bf16x8 kvo = asb8(u32x4{kr[0][0], kr[0][2], kr[1][0], kr[1][2]});
    const bf16x8 zo = asb8(u32x4{kr[2][0], kr[2][2], kr[3][0], kr[3][2]});
    u32x2 ov[4];
#pragma unroll
    for (int m = 0; m < 4; ++m) {
      const f32x4 num = mfma32(kvo, asb8(pq[m]), zero4), den = mfma32(zo, asb8(pq[m]), zero4);
      ov[m] = pk4(num * __builtin_amdgcn_rcpf(den[0] + 1e-6f));
    }
#pragma unroll
    for (int mp = 0; mp < 2; ++mp)
      st128d(u32x4{ov[2 * mp][0], ov[2 * mp][1], ov[2 * mp + 1][0], ov[2 * mp + 1][1]}, wsd,
             l16, WS_A3F + ((rt16 * 4 + wm * 2 + mp) * 76 + grp * 4 + wn) * 1024);
  }
  else {
    BAR();
    if (w > 0) {
#pragma unroll
      for (int i = 0; i < 10; ++i)
        if (i < 9 || w == 1) dma16(od, w3off, (i * 7 + w - 1) * 8192, L_U + (i * 7 + w - 1) * 1024);
    }
  }
  unsigned rdy = 0;
  if (w == 0) {
#pragma nounroll
    for (int spin = 0; spin < SPIN_CAP; ++spin) {
      rdy = poll_z();
      if ((rdy & 15u) == 15u) break;
      __builtin_amdgcn_s_sleep(PSLEEP);
    }
    if (lane == 0) *(unsigned*)(smem + 2 * L_CB) = rdy;
  }
  LBAR();
  rdy = __builtin_amdgcn_readfirstlane(*(const unsigned*)(smem + 2 * L_CB));
  const int bsrc = WS_Z2F + ft0 * 4096 + w * 1024;
  const int l16n = npair == 3 ? l16 : DROP;
  auto dma2 = [&](int st, int u) {
    const int n = 2 * u + (w >> 2), q = w & 3;
    dma16d(wsd, u < 2 ? l16 : l16n, bsrc +
           ((st + 2 * rot2) & 15) * (44 * 4096) + u * 8192, (st & 3) * L_RING + (n ^ (u < 2 ? q : (q & 1))) * 4096 + q * 1024);
  };
#pragma unroll
  for (int st = 0; st < 2; ++st)
#pragma unroll
    for (int u = 0; u < 3; ++u) dma2(st, u);
  waitv<3>();
  f32x4 ac2[6][2];
#pragma unroll
  for (int n = 0; n < 6; ++n)
#pragma unroll
    for (int t = 0; t < 2; ++t) { const float b = (n == 0 || (n == 4 && kq < 2)) ? bflo(biasv[t]) : 0.f; ac2[n][t] = f32x4{b, b, b, b}; }
  const u32x4 ud = mkdesc(wout, 688u * 1024u);
  bf16x8 fz[2][6];
  auto rdZ = [&](int st, int n) { fz[st & 1][n] = *(const bf16x8*)(smem + (st & 3) * L_RING + n * 4096 + kq * 1024 + l16); };
  u32x4 ao[4][2];
  u32x4 z1v[2];
  u32x2 ofl;
  const int a3 = WS_A3F + ((rt64 * 2 + th) * 76 + 2 * kq) * 1024;
  auto flo = [&]() {
    return l16 < 512 ? WS_FLO + rt64 * 256 + (l16 >> 1) : (l16 < 640 ? WS_FLU + rt64 * 512 + ((l16 >> 1) & 63) : WS_FLZ + cgp * 1024 + 512 + (rt64 >> 1) * 8);
  };
  BAR();
#pragma unroll
  for (int n = 0; n < 6; ++n) rdZ(0, n);
#pragma unroll
  for (int st = 2; st < 4; ++st)
#pragma unroll
    for (int u = 0; u < 3; ++u) dma2(st, u);
  char* rb = smem + 36864 + th * 18432 + l16;
  auto red_wr = [&](int i) {
    if (i == 4 && kq < 2) return;
    const int blk = i < 4 ? (i ^ kq) * 3 + i - 1 : (i ^ (kq & 1)) * 3 + (i == 5 ? (kq & 2) : 1);
    const u32x2 a = pk4(ac2[i][0]), b = pk4(ac2[i][1]);
    *(u32x4*)(rb + (blk << 10)) = u32x4{a[0], a[1], b[0], b[1]};
  };
  sfor<16>([&](auto ST) {
    constexpr int st = ST;
    if constexpr (st < 15) {
      WAITL0();
      waitv<p2cnt(st)>();
      BAR();
    }
    sfor<6>([&](auto N) {
      constexpr int n = N;
      constexpr int ord[6] = {1, 2, 3, 5, 4, 0}, c = st == 15 ? ord[n] : n;
      SB();
      ac2[c][0] = mfma32(fz[st & 1][c], af[st >> 1][st & 1][0], ac2[c][0]);
      ac2[c][1] = mfma32(fz[st & 1][c], af[st >> 1][st & 1][1], ac2[c][1]);
      SB();
      if constexpr (st == 15 && n >= 1) red_wr(ord[n - 1]);
      if constexpr (st < 15 && n >= 1 && n <= 3) { rdZ(st + 1, 2 * n - 2); rdZ(st + 1, 2 * n - 1); }
      if constexpr (st + 4 < 16 && n == 0 && (st & 1) == 0) {
        if (__builtin_expect(((rdy >> (st + 4)) & 3u) != 3u, 0)) {
#pragma nounroll
          for (int spin = 0; spin < SPIN_CAP; ++spin) {
            rdy = poll_z();
            if (((rdy >> (st + 4)) & 3u) == 3u) break;
            __builtin_amdgcn_s_sleep(PSLEEP);
          }
        }
      }
      if constexpr (st + 4 < 16 && n < 3) dma2(st + 4, n);
      if constexpr (n == 3) {
        if constexpr (st == 12 || st == 13) {
#pragma unroll
          for (int j = 3 * (st - 12); j < (st == 12 ? 3 : 4); ++j) dma16(ud, w3off, (j * 8 + w) * 8192, (j * 8 + w) * 1024);
        }
        if constexpr (st == 2) {
          const int fo = isqkv ? WS_FLO + ((rt16 * 2 + wm) * 32 + grp * 4 + wn) * 8 : WS_FLZ + grp * 1024 + 512 + rt16 * 8;
          __builtin_amdgcn_raw_buffer_store_b64(u32x2{tag, tag ^ TAGX}, wsr, (l16 == 0 && (isqkv || w == 1)) ? 0 : DROP, fo, 16);
          __builtin_amdgcn_raw_buffer_store_b64(u32x2{tag, tag ^ TAGX}, wsr, (l16 == 0 && w == 2) ? 0 : DROP, WS_FLU + rt64 * 512 + cgp * 8, 16);
        }
        if constexpr (st == 9) ld64ds(ofl, wsd, flo(), 0);
        if constexpr (st == 13) {
          pin(ofl);
#pragma nounroll
          for (int spin = 0; spin < SPIN_CAP; ++spin) {
            if (__builtin_expect(__all(ofl[0] == tag && ofl[1] == (tag ^ TAGX)), 1)) break;
            __builtin_amdgcn_s_sleep(PSLEEP);
            ofl = poll64(wsd, flo(), 0);
          }
#pragma unroll
          for (int e = 0; e < 2; ++e)
            ld128d(z1v[e], wsd, l16, WS_Z1F + ((rt64 * 2 + th) * 44 + ft0 + min(kq + 4 * e, 2 * npair - 1)) * 1024);
        }
        if constexpr (st == 13 || st == 14) {
#pragma unroll
          for (int i = 0; i < 2; ++i)
#pragma unroll
            for (int h = 0; h < 2; ++h) ld128d(ao[2 * (st - 13) + i][h], wsd, l16, a3 + (8 * (2 * (st - 13) + i) + h) * 1024);
        }
      }
    });
  });
  {
    WAITV(8);
    pin(z1v[0]); pin(z1v[1]);
    LBAR();
#pragma unroll
    for (int e = 0; e < 2; ++e) {
      const int n = kq + 4 * e;
      if (e == 0 || n < 2 * npair) {
        u32x4 o, pr[3];
#pragma unroll
        for (int j = 0; j < 3; ++j) pr[j] = *(const u32x4*)(rb + ((n * 3 + j) << 10));
#pragma unroll
        for (int t = 0; t < 2; ++t) {
          f32x4 s = ac2[4 * e][t];
#pragma unroll
          for (int j = 0; j < 3; ++j) s += f32x4{bflo(pr[j][2 * t]), bfhi(pr[j][2 * t]), bflo(pr[j][2 * t + 1]), bfhi(pr[j][2 * t + 1])};
          o[2 * t] = pk2(bflo(z1v[e][2 * t]) * s[0], bfhi(z1v[e][2 * t]) * s[1]);
          o[2 * t + 1] = pk2(bflo(z1v[e][2 * t + 1]) * s[2], bfhi(z1v[e][2 * t + 1]) * s[3]);
        }
        if (ft0 + n >= 43) o = u32x4{0u, 0u, 0u, 0u};
        st128d(o, wsd, l16, WS_A3F + ((rt64 * 2 + th) * 76 + 32 + ft0 + n) * 1024);
      }
    }
  }
  f32x4 ac3[4][2];
#pragma unroll
  for (int n = 0; n < 4; ++n) { ac3[n][0] = zero4; ac3[n][1] = zero4; }
  int ly = l16;
  asm volatile("" : "+v"(ly));
  const int lane3 = ly >> 4, r3 = lane3 & 15, g3 = lane3 >> 4, tid3 = w * 64 + lane3;
  int b3[4];
  {
    const int k = 4 * g3 + (r3 >> 2), tp = r3 & 3;
    const int h2 = ((k >> 1) & 1) | (((k >> 2) & 1) << 1);
#pragma unroll
    for (int n = 0; n < 4; ++n) b3[n] = k * 128 + (((2 * n + (tp >> 1)) ^ (h2 << 1)) << 4) + (tp & 1) * 8 + kq * 4096;
  }
  const int go = (rt64 * 64 + (tid3 >> 3)) * 1024 + cgp * 128 + (tid3 & 7) * 16;
  u32x4 xres;
  if (kq + 4 < 2 * npair) { WAITV(2); } else { WAITV(1); }
  LBAR();
  sfor<4>([&](auto I) {
    constexpr int i = I;
    bf16x8 b[4];
#pragma unroll
    for (int n = 0; n < 4; ++n) b[n] = cat8(trread(smem + L_U + b3[n] + i * 16384), trread(smem + L_U + b3[n] + i * 16384 + 2048));
    pin(ao[i][0]); pin(ao[i][1]);
#pragma unroll
    for (int j = 4 + 2 * i; j < 6 + 2 * i; ++j)
      if (j < 11) dma16(ud, w3off, min(j * 8 + w, 85) * 8192, (j * 8 + w) * 1024);
#pragma unroll
    for (int n = 0; n < 4; ++n)
#pragma unroll
      for (int t = 0; t < 2; ++t)
        ac3[n][t] = mfma32(b[n], asb8(u32x4{ao[i][0][2 * t], ao[i][0][2 * t + 1], ao[i][1][2 * t], ao[i][1][2 * t + 1]}), ac3[n][t]);
  });
  asm volatile("buffer_load_dwordx4 %0, %1, %2, 0 offen" : "=v"(xres) : "v"(go), "s"(xd) : "memory");
  if (bid == 0 && w == 7) {
#pragma nounroll
    for (int spin = 0; spin < SPIN_CAP; ++spin) {
      u32x4 f[4];
#pragma unroll
      for (int j = 0; j < 4; ++j) ld128d(f[j], wsd, lane3 * 128, WS_RST + j * 8192);
      WAITV(0);
#pragma unroll
      for (int j = 0; j < 4; ++j) pin(f[j]);
      if (__all(f[0][1] == tag && f[1][1] == tag && f[2][1] == tag && f[3][1] == tag)) break;
    }
    if (lane3 == 0) __builtin_amdgcn_raw_buffer_store_b32(tag, wsr, WS_EP, 0, 16);
  }
  u32x4 au[6][2];
  auto ld_au = [&](int i) {
#pragma unroll
    for (int h = 0; h < 2; ++h) ld128d(au[i][h], wsd, l16, a3 + (32 + h + ((i < 5 || kq < 2) ? 8 * i : 32)) * 1024);
  };
#pragma unroll
  for (int i = 0; i < AUD; ++i) ld_au(i);
  waitv<2 * AUD>();
  LBAR();
  sfor<6>([&](auto I) {
    constexpr int i = I;
    bf16x8 b[4];
    const int io = (i < 5 || kq < 2) ? i * 16384 : 0;
#pragma unroll
    for (int n = 0; n < 4; ++n) b[n] = cat8(trread(smem + b3[n] + io), trread(smem + b3[n] + io + 2048));
    if constexpr (i + AUD < 6) ld_au(i + AUD);
    waitv<(i + AUD < 6 ? 2 * AUD : 10 - 2 * i)>();
#pragma nounroll
    for (int spin = 0; spin < SPIN_CAP; ++spin) {
      pin(au[i][0]); pin(au[i][1]);
      const u32x4 mx = __builtin_elementwise_max(au[i][0], au[i][1]);
      if (__builtin_expect(__all(max(max(mx[0], mx[1]), max(mx[2], mx[3])) != EMPTY), 1)) break;
      __builtin_amdgcn_s_sleep(PSLEEP);
      ld_au(i);
      WAITV(0);
    }
    if (i < 5 || kq < 2) {
#pragma unroll
      for (int n = 0; n < 4; ++n)
#pragma unroll
        for (int t = 0; t < 2; ++t)
          ac3[n][t] = mfma32(b[n], asb8(u32x4{au[i][0][2 * t], au[i][0][2 * t + 1], au[i][1][2 * t], au[i][1][2 * t + 1]}), ac3[n][t]);
    }
  });
#pragma unroll
  for (int n = 0; n < 4; ++n)
#pragma unroll
    for (int t = 0; t < 2; ++t)
      *(f32x4*)(smem + L_U + kq * 16384 + (th * 32 + t * 16 + r3) * 256 + (((n * 4 + g3) ^ r3) << 4)) = ac3[n][t];
  LBAR();
  {
    pin(xres);
    const int row = tid3 >> 3, c8 = tid3 & 7;
    f32x4 s0 = {bflo(xres[0]), bfhi(xres[0]), bflo(xres[1]), bfhi(xres[1])};
    f32x4 s1 = {bflo(xres[2]), bfhi(xres[2]), bflo(xres[3]), bfhi(xres[3])};
#pragma unroll
    for (int j = 0; j < 4; ++j) {
      s0 += *(const f32x4*)(smem + L_U + j * 16384 + row * 256 + (((2 * c8) ^ (row & 15)) << 4));
      s1 += *(const f32x4*)(smem + L_U + j * 16384 + row * 256 + (((2 * c8 + 1) ^ (row & 15)) << 4));
    }
    const u32x2 lo = pk4(s0), hi = pk4(s1);
    st128d(u32x4{lo[0], lo[1], hi[0], hi[1]}, mkdesc(out, 2048u * 1024u), go, 0);
  }
}

extern "C" void kernel_launch(void* const* d_in, const int* in_sizes, int n_in, void* d_out, int out_size, void* d_ws,
                              size_t ws_size, hipStream_t stream) {
  (void)hipFuncSetAttribute((const void*)fused, hipFuncAttributeMaxDynamicSharedMemorySize, LDS_BYTES);
  fused<<<256, 512, LDS_BYTES, stream>>>((const char*)d_in[0], (const char*)d_in[1], (const char*)d_in[2],
                                         (const char*)d_in[3], (const char*)d_in[5], (const char*)d_in[6],
                                         (const char*)d_in[7], (const char*)d_in[8], (char*)d_out, (char*)d_ws);
}
```
